```python
import torch

def _softmax(x, axis=-1):
    return torch.softmax(x, dim=axis)
def _gelu(x):
    return torch.nn.functional.gelu(x, approximate="tanh")
def _silu(x):
    return torch.nn.functional.silu(x)
def _sigmoid(x):
    return torch.sigmoid(x)
def _layernorm(x, axis=-1, eps=1e-5):
    m = x.mean(dim=axis, keepdim=True)
    v = x.var(dim=axis, keepdim=True, unbiased=False)
    return (x - m) / torch.sqrt(v + eps)
def _scatter_add(tgt, idx, src, axis=0):
    return tgt.clone().index_add_(axis, idx, src)
def _segment_sum(src, seg, n):
    out = torch.zeros((n,) + src.shape[1:], dtype=src.dtype)
    return out.index_add_(0, seg, src)
def _pad(x, pw):
    flat = tuple(v for pair in reversed(pw) for v in pair)
    return torch.nn.functional.pad(x, flat)

kernel_name = 'interleave_layers__softmax_full_mha__dense_activation_mish__prompt'

def setup_inputs():

    _w = (lambda a: (lambda t: t.to(torch.bfloat16) if t.is_floating_point() else t)(torch.as_tensor(a)))
    def _A_setup():
        B, S, D = 1, 2048, 448
        return {
            "x": _w((lambda *s: torch.randn(*s, dtype=torch.bfloat16) * 0.05)(B, S, D)),
            "wqkv": _w((lambda *s: torch.randn(*s, dtype=torch.bfloat16) * 0.05)(D, 3 * D)),
            "wo": _w((lambda *s: torch.randn(*s, dtype=torch.bfloat16) * 0.05)(D, D)),
        }
    _A_in = _A_setup()

    def _B_setup():
        B, S, D, F = 1, 2048, 448, 1792
        return {
            "x": _w((lambda *s: torch.randn(*s, dtype=torch.bfloat16) * 0.05)(B, S, D)),
            "w1": _w((lambda *s: torch.randn(*s, dtype=torch.bfloat16) * 1.6)(D, F) * (D ** -0.5)),
            "b1": _w((lambda *s: torch.randn(*s, dtype=torch.bfloat16) * 0.2)(F)),
            "w2": _w((lambda *s: torch.randn(*s, dtype=torch.bfloat16) * 0.2)(F, D) * (F ** -0.5)),
        }
    _B_in = _B_setup()

    return {
        "A_x": _A_in["x"],
        "A_wqkv": _A_in["wqkv"],
        "A_wo": _A_in["wo"],
        "B_x": _B_in["x"],
        "B_w1": _B_in["w1"],
        "B_b1": _B_in["b1"],
        "B_w2": _B_in["w2"],
    }

def reference(A_x, A_wqkv, A_wo, B_x, B_w1, B_b1, B_w2):
    def _A_ref(x, wqkv, wo):
        B, S, D = x.shape
        H = 16; Dh = D // H
        qkv = (lambda x, *d: x.permute(*d))((x @ wqkv).reshape(B, S, 3, H, Dh), 2, 0, 3, 1, 4)
        q, k, v = qkv[0], qkv[1], qkv[2]
        scale = Dh ** -0.5
        scores = torch.einsum("bhqd,bhkd->bhqk", q, k) * scale
        p = _softmax(scores, axis=-1)
        o = torch.einsum("bhqk,bhkd->bhqd", p, v)
        return (lambda x, *d: x.permute(*d))(o, 0, 2, 1, 3).reshape(B, S, D) @ wo

    def _B_ref(x, w1, b1, w2):
        h = torch.einsum("bsd,df->bsf", x, w1) + b1
        sp = torch.log1p(torch.exp(-torch.abs(h))) + torch.where(h > 0, h, torch.zeros_like(h))
        act = h * torch.tanh(sp)
        return torch.einsum("bsf,fd->bsd", act, w2)

    x = A_x
    x = x + _A_ref(_layernorm(x, axis=-1), A_wqkv, A_wo)
    x = x + _B_ref(_layernorm(x, axis=-1), B_w1, B_b1, B_w2)
    x = x + _A_ref(_layernorm(x, axis=-1), A_wqkv, A_wo)
    x = x + _B_ref(_layernorm(x, axis=-1), B_w1, B_b1, B_w2)
    return x
```

```cpp
#include <hip/hip_runtime.h>
#include <utility>

#define DI __device__ __forceinline__
#define LAS __attribute__((address_space(3)))
#define GAS __attribute__((address_space(1)))
typedef float f32x16 __attribute__((ext_vector_type(16)));
typedef float f32x4 __attribute__((ext_vector_type(4)));
typedef float f32x2 __attribute__((ext_vector_type(2)));
typedef unsigned u32x4 __attribute__((ext_vector_type(4)));
typedef unsigned u32x2 __attribute__((ext_vector_type(2)));
typedef short v4i16 __attribute__((ext_vector_type(4)));
typedef __bf16 bf16x2 __attribute__((ext_vector_type(2)));
typedef __amdgpu_buffer_rsrc_t rsrc_t;
typedef unsigned long long u64;
typedef unsigned short u16;

constexpr unsigned WS_FLAGS = 4096;
constexpr unsigned WS_STAMP = 704 * 1024;
constexpr unsigned WS_DATA = 1u << 20;
constexpr unsigned L_Q = 0, L_KV = 2u << 20, L_O = 6u << 20, L_X1 = 8u << 20, L_AF = 10u << 20, L_X2 = 17u << 20, L_STRIDE = 19u << 20;
constexpr unsigned WS_BYTES = WS_DATA + 2 * L_STRIDE;
constexpr unsigned IMG = 28672;
constexpr unsigned LDS_WO = 0;
constexpr unsigned LDS_R1 = 57600;
constexpr unsigned LDS_HALF = LDS_R1 + 3 * IMG;
constexpr unsigned LDS_AUG = 161792;
constexpr unsigned LDS_SCR48 = LDS_AUG - 49152;
constexpr unsigned LDS_SCR16 = LDS_AUG - 16384;
constexpr unsigned LDS_BYTES = 163840;
constexpr int PART1 = 53;

#define SB() __builtin_amdgcn_sched_barrier(0)
#define BAR() asm volatile("s_waitcnt lgkmcnt(0)\n\ts_barrier" ::: "memory")
#define BAR_VM() asm volatile("s_waitcnt vmcnt(0) lgkmcnt(0)\n\ts_barrier" ::: "memory")
#define WAIT_VM0() asm volatile("s_waitcnt vmcnt(0)" ::: "memory")
#define RLX_AGENT __ATOMIC_RELAXED, __HIP_MEMORY_SCOPE_AGENT

template <int N, class F> DI void sfor_impl(F&& f, std::integral_constant<int, N>) {}
template <class F, int... Is> DI void sfor_seq(F&& f, std::integer_sequence<int, Is...>) { (f(std::integral_constant<int, Is>{}), ...); }
template <int N, class F> DI void sfor(F&& f) { sfor_seq(f, std::make_integer_sequence<int, N>{}); }

DI void mfma(f32x16& c, const u32x4& a, const u32x4& b) { asm volatile("v_mfma_f32_32x32x16_bf16 %0, %1, %2, %0" : "+v"(c) : "v"(a), "v"(b)); }
DI void mfma0(f32x16& c, const u32x4& a, const u32x4& b) { asm volatile("v_mfma_f32_32x32x16_bf16 %0, %1, %2, 0" : "=&v"(c) : "v"(a), "v"(b)); }
template <int R> DI void mfma_qb(f32x16& c, const u32x4& a) { asm volatile("v_mfma_f32_32x32x16_bf16 %0, %1, a[%c2:%c3], %0" : "+v"(c) : "v"(a), "i"(R), "i"(R + 3)); }
template <int R> DI void mfma0_qb(f32x16& c, const u32x4& a) { asm volatile("v_mfma_f32_32x32x16_bf16 %0, %1, a[%c2:%c3], 0" : "=&v"(c) : "v"(a), "i"(R), "i"(R + 3)); }
template <int R> DI void mfma_wa(f32x16& c, const u32x4& b) { asm volatile("v_mfma_f32_32x32x16_bf16 %0, a[%c2:%c3], %1, %0" : "+v"(c) : "v"(b), "i"(R), "i"(R + 3)); }
template <int R> DI void mfma0_wa(f32x16& c, const u32x4& b) { asm volatile("v_mfma_f32_32x32x16_bf16 %0, a[%c2:%c3], %1, 0" : "=&v"(c) : "v"(b), "i"(R), "i"(R + 3)); }
template <int R, int OFF> DI void tr_to_acc(unsigned addr) {
    asm volatile("ds_read_b64_tr_b16 a[%c1:%c2], %0 offset:%c5\n\tds_read_b64_tr_b16 a[%c3:%c4], %0 offset:%c6"
                 :: "v"(addr), "i"(R), "i"(R + 1), "i"(R + 2), "i"(R + 3), "i"(OFF), "i"(OFF + 512) : "memory"); }
template <int R> DI void q_load(unsigned voff, __amdgpu_buffer_rsrc_t rs, unsigned soff) {
    asm volatile("buffer_load_dwordx4 a[%c3:%c4], %0, %1, %2 offen sc1\n\tbuffer_load_dwordx4 a[%c5:%c6], %0, %1, %2 offen offset:1024 sc1"
                 :: "v"(voff), "s"(rs), "s"(soff), "i"(R), "i"(R + 3), "i"(R + 4), "i"(R + 7) : "memory", "a255"); }
DI void mfma_n(f32x16& c, const u32x4& a, const u32x4& b) { asm volatile("s_nop 1\n\tv_mfma_f32_32x32x16_bf16 %0, %1, %2, %0" : "+v"(c) : "v"(a), "v"(b)); }
DI void mfma0_safe(f32x16& c, const u32x4& a, const u32x4& b) { asm volatile("s_nop 1\n\tv_mfma_f32_32x32x16_bf16 %0, %1, %2, 0\n\ts_nop 11" : "=&v"(c) : "v"(a), "v"(b)); }
DI void mfma_safe(f32x16& c, const u32x4& a, const u32x4& b) { asm volatile("s_nop 1\n\tv_mfma_f32_32x32x16_bf16 %0, %1, %2, %0\n\ts_nop 11" : "+v"(c) : "v"(a), "v"(b)); }
#define SETTLE1(a) asm volatile("s_nop 11" : "+v"(a))
#define SETTLE2(a, b) asm volatile("s_nop 11" : "+v"(a), "+v"(b))
#define SETTLE3(a, b, c) asm volatile("s_nop 11" : "+v"(a), "+v"(b), "+v"(c))
#define SETTLE4(a, b, c, d) asm volatile("s_nop 11" : "+v"(a), "+v"(b), "+v"(c), "+v"(d))

DI unsigned cvtpk(float lo, float hi) { f32x2 v = {lo, hi}; bf16x2 b = __builtin_convertvector(v, bf16x2); return __builtin_bit_cast(unsigned, b); }
DI float bflo(unsigned u) { return __builtin_bit_cast(float, u << 16); }
DI float bfhi(unsigned u) { return __builtin_bit_cast(float, u & 0xffff0000u); }
DI float dot2(unsigned a, unsigned b, float c) { return __builtin_amdgcn_fdot2_f32_bf16(__builtin_bit_cast(bf16x2, a), __builtin_bit_cast(bf16x2, b), c, false); }
DI float u2f(unsigned u) { return __builtin_bit_cast(float, u); }
DI unsigned f2u(float f) { return __builtin_bit_cast(unsigned, f); }
DI float half_sum(float v) { auto r = __builtin_amdgcn_permlane32_swap(f2u(v), f2u(v), false, false); return u2f(r[0]) + u2f(r[1]); }
DI float half_max(float v) { auto r = __builtin_amdgcn_permlane32_swap(f2u(v), f2u(v), false, false); return __builtin_fmaxf(u2f(r[0]), u2f(r[1])); }
DI float upper_half(float v) { auto r = __builtin_amdgcn_permlane32_swap(f2u(v), f2u(v), false, false); return u2f(r[1]); }
DI u32x4 ldg(rsrc_t rs, unsigned voff, unsigned soff) { return __builtin_amdgcn_raw_buffer_load_b128(rs, voff, soff, 16); }
DI void stg(u32x4 v, rsrc_t rs, unsigned voff, unsigned soff) { __builtin_amdgcn_raw_buffer_store_b128(v, rs, voff, soff, 16); asm volatile("s_nop 1" : "+v"(v) :: "memory"); }
DI void dma(unsigned lds, unsigned voff, rsrc_t rs, unsigned soff) {
    asm volatile("s_mov_b32 m0, %0\n\ts_nop 0\n\tbuffer_load_dwordx4 %1, %2, %3 offen lds" :: "s"(lds), "v"(voff), "s"(rs), "s"(soff) : "memory"); }

DI void pin_v(u32x4& x) { asm volatile("" : "+v"(x)); }
DI void pin_v(unsigned& x) { asm volatile("" : "+v"(x)); }
DI void pin_v(float& x) { asm volatile("" : "+v"(x)); }
DI void kv_load(u32x4 (&d)[4], unsigned voff, rsrc_t rs, unsigned soff) {
    asm volatile("buffer_load_dwordx4 %0, %4, %5, %6 offen sc1\n\tbuffer_load_dwordx4 %1, %4, %5, %6 offen offset:1024 sc1\n\t"
                 "buffer_load_dwordx4 %2, %4, %5, %6 offen offset:2048 sc1\n\tbuffer_load_dwordx4 %3, %4, %5, %6 offen offset:3072 sc1"
                 : "=&v"(d[0]), "=&v"(d[1]), "=&v"(d[2]), "=&v"(d[3]) : "v"(voff), "s"(rs), "s"(soff) : "memory"); }
template <int N0, int N1> DI void kv_wait(u32x4 (&d)[4], int layer) {
    asm volatile("s_waitcnt vmcnt(%c5)\n\ts_cmp_eq_u32 %4, 0\n\ts_cbranch_scc1 1f\n\ts_waitcnt vmcnt(%c6)\n1:\n\t; KVW %0 %1 %2 %3"
                 : "+v"(d[0]), "+v"(d[1]), "+v"(d[2]), "+v"(d[3]) : "s"(layer), "i"(N0), "i"(N1) : "memory", "scc"); }
template <int N> DI void wait_vm() { asm volatile("s_waitcnt vmcnt(%c0) lgkmcnt(0)" :: "i"(N) : "memory"); }
DI u32x4 trfrag(unsigned a1, unsigned a2) {
    v4i16 lo = __builtin_amdgcn_ds_read_tr16_b64_v4i16((LAS v4i16*)(size_t)a1), hi = __builtin_amdgcn_ds_read_tr16_b64_v4i16((LAS v4i16*)(size_t)a2);
    u32x2 l = __builtin_bit_cast(u32x2, lo), h = __builtin_bit_cast(u32x2, hi);
    return (u32x4){l[0], l[1], h[0], h[1]}; }
DI void lds_st16(unsigned a, f32x4 v) { *(LAS f32x4*)(size_t)a = v; }
DI f32x4 lds_ld16(unsigned a) { return *(LAS f32x4*)(size_t)a; }
constexpr int xrow(int f) { return (f % 7 == 6) ? 112 * (f / 7) + 48 : 112 * (f / 7) + (f % 7 >= 3 ? 56 : 0) + 16 * ((f % 7) % 3); }
DI u32x4 pack8(const f32x16& v, int s, float k) {
    return (u32x4){cvtpk(v[8 * s] * k, v[8 * s + 1] * k), cvtpk(v[8 * s + 2] * k, v[8 * s + 3] * k), cvtpk(v[8 * s + 4] * k, v[8 * s + 5] * k), cvtpk(v[8 * s + 6] * k, v[8 * s + 7] * k)}; }
DI unsigned hi_lo(float v, float& hi) { unsigned h = cvtpk(v, 0.f); hi = bflo(h); return h & 0xffffu; }

#define STAMP(i) do {} while (0)
#define PRESLEEP 8

__global__ __launch_bounds__(256, 1) void mega(const u16* __restrict__ x, const u16* __restrict__ wqkv, const u16* __restrict__ wo, const u16* __restrict__ w1,
                                               const u16* __restrict__ b1, const u16* __restrict__ w2, u16* __restrict__ out, char* __restrict__ ws) {
    int lane = threadIdx.x & 63, w = __builtin_amdgcn_readfirstlane(threadIdx.x >> 6);
    const int b = blockIdx.x;
    int g = (b & 7) * 2 + (b >> 7), j = (b >> 3) & 15;
    int r, hh, m, n, t, u; unsigned trl, l16;
#define FRESH() do { asm volatile("" : "+s"(g), "+s"(j), "+s"(w), "+v"(lane)); r = lane & 31; hh = lane >> 5; m = j >> 3; n = j & 7; t = w >> 1; u = w & 1; \
        trl = ((lane & 15) >> 2) * 64 + ((lane >> 4) & 1) * 32 + (lane & 3) * 8; l16 = lane * 16; } while (0)
    const rsrc_t rsX = __builtin_amdgcn_make_buffer_rsrc((void*)x, 0, 2048 * 896, 0x00020000);
    const rsrc_t rsQKV = __builtin_amdgcn_make_buffer_rsrc((void*)wqkv, 0, 448 * 2688, 0x00020000);
    const rsrc_t rsWO = __builtin_amdgcn_make_buffer_rsrc((void*)wo, 0, 448 * 896, 0x00020000);
    const rsrc_t rsW1 = __builtin_amdgcn_make_buffer_rsrc((void*)w1, 0, 448 * 3584, 0x00020000);
    const rsrc_t rsW2 = __builtin_amdgcn_make_buffer_rsrc((void*)w2, 0, 1792 * 896, 0x00020000);
    const rsrc_t rsW = __builtin_amdgcn_make_buffer_rsrc((void*)ws, 0, WS_BYTES, 0x00020000);
    constexpr unsigned lds0 = 0;
    u64 tag;
    bool dead = false;

    f32x16 resid;

    auto publish = [&](int S, bool cross) {
        BAR_VM();
        if (w == 0 && lane == 0) __hip_atomic_store((GAS u64*)(ws + WS_FLAGS + ((S * 16 + (cross ? j : g)) * 16 + (cross ? g : j)) * 256), tag, RLX_AGENT);
    };
    auto poll = [&](int S) {
        GAS u64* f = (GAS u64*)(ws + WS_FLAGS + ((S * 16 + g) * 16 + (lane & 15)) * 256);
        __builtin_amdgcn_s_sleep(PRESLEEP);
        if (!dead) {
            int spin = 0;
            for (;; ++spin) {
                u64 v = __hip_atomic_load(f, RLX_AGENT);
                if (__all(v == tag)) break;
                if (spin > 400000) { dead = true; break; }
                __builtin_amdgcn_s_sleep(1);
            }
        }
    };
    auto dma_wqkv = [&](int p, unsigned voff) { const int img = p / 28, kk = p % 28; dma(lds0 + LDS_R1 + p * 1024, voff, rsQKV, img * 896 + j * 56 + kk * (16 * 2688)); };
    auto dma_w1 = [&](int p, unsigned voff) { const int img = p / 28, kk = p % 28; dma(lds0 + LDS_R1 + p * 1024, voff, rsW1, img * 64 + j * 224 + kk * (16 * 3584)); };
    auto publish_x = [&](unsigned base) {
        const unsigned tb = base + (4 * g + 2 * m + t) * 32768, f0 = 7 * (n >> 1) + 3 * (n & 1);
        u32x4 p0 = pack8(resid, 0, 1.f), p1 = pack8(resid, 1, 1.f);
        float s1 = 0.f, s2 = 0.f;
        sfor<4>([&](auto i) { s1 = dot2(p0[i.value], 0x3F803F80u, s1); s2 = dot2(p0[i.value], p0[i.value], s2); });
        sfor<2>([&](auto i) { s1 = dot2(p1[i.value], 0x3F803F80u, s1); s2 = dot2(p1[i.value], p1[i.value], s2); });
        if (u == 0) {
            sfor<2>([&](auto i) { s1 = dot2(p1[2 + i.value], 0x3F803F80u, s1); s2 = dot2(p1[2 + i.value], p1[2 + i.value], s2); });
            stg(p0, rsW, l16, tb + f0 * 1024); stg(p1, rsW, l16, tb + f0 * 1024 + 1024);
        } else {
            stg(p0, rsW, l16, tb + f0 * 1024 + 2048);
            auto a = __builtin_amdgcn_permlane32_swap(p1[0], p1[0], false, false), c = __builtin_amdgcn_permlane32_swap(p1[1], p1[1], false, false);
            if (hh == (n & 1)) stg((u32x4){a[0], c[0], a[1], c[1]}, rsW, l16, tb + (7 * (n >> 1) + 6) * 1024);
        }
        s1 = half_sum(s1); s2 = half_sum(s2);
        if (hh == 0) __builtin_amdgcn_raw_buffer_store_b64((u32x2){f2u(s1), f2u(s2)}, rsW, r * 8, tb + 28672 + (2 * n + u) * 256, 16);
    };
    auto reduce4 = [&](f32x16& o, const f32x16& v1, const f32x16& v2, const f32x16& v3) {
        const unsigned sw = lds0 + LDS_SCR48 + w * 12288 + l16;
        sfor<4>([&](auto k) { lds_st16(sw + k.value * 1024, (f32x4){v1[4 * k.value], v1[4 * k.value + 1], v1[4 * k.value + 2], v1[4 * k.value + 3]}); });
        sfor<4>([&](auto k) { lds_st16(sw + 4096 + k.value * 1024, (f32x4){v2[4 * k.value], v2[4 * k.value + 1], v2[4 * k.value + 2], v2[4 * k.value + 3]}); });
        sfor<4>([&](auto k) { lds_st16(sw + 8192 + k.value * 1024, (f32x4){v3[4 * k.value], v3[4 * k.value + 1], v3[4 * k.value + 2], v3[4 * k.value + 3]}); });
        BAR();
        sfor<3>([&](auto d) { const unsigned a = lds0 + LDS_SCR48 + (w ^ (d.value + 1)) * 12288 + d.value * 4096 + l16;
            sfor<4>([&](auto k) { f32x4 v = lds_ld16(a + k.value * 1024); sfor<4>([&](auto e) { o[4 * k.value + e.value] += v[e.value]; }); }); });
    };
    auto make_xaug = [&](float s1, float s2, float& rstd) {
        const float mean = s1 * (1.f / 448.f), var = __builtin_fmaxf(s2 * (1.f / 448.f) - mean * mean, 0.f) + 1e-5f;
        rstd = __builtin_amdgcn_rsqf(var);
        float mh, sh; const unsigned mhb = hi_lo(-mean, mh), shb = hi_lo(var * rstd, sh);
        const unsigned d0 = cvtpk(mh, -mean - mh), d1 = mhb | (shb << 16), d2 = cvtpk(var * rstd - sh, 0.f);
        return hh == 0 ? (u32x4){d0, d1, d2, 0u} : (u32x4){0u, 0u, 0u, 0u};
    };
    auto make_waug = [&](float cs, unsigned bias16) {
        float ch; const unsigned chb = hi_lo(cs, ch);
        return (u32x4){chb | (chb << 16), (cvtpk(cs - ch, 0.f) & 0xffffu) | (bias16 << 16), bias16, 0u};
    };

#pragma unroll 1
    for (int layer = 0; layer < 2; ++layer) {
        const unsigned LB = WS_DATA + layer * L_STRIDE;
        FRESH();
        STAMP(0);
        {
            u32x4 xf[28]; float s1 = 0.f, s2 = 0.f;
            unsigned tA1, tA2;
            if (layer == 0) {
                const unsigned vA = ((4 * g + w) * 32 + r) * 896 + 16 * hh, vD = vA + 96 * hh;
                sfor<28>([&](auto f) { xf[f.value] = __builtin_amdgcn_raw_buffer_load_b128(rsX, (f.value % 7 == 6 ? vD : vA) + 2 * xrow(f.value), 0, 0); });
                SB();
                const unsigned vo = (lane >> 2) * 2688 + (lane & 3) * 16;
                for (int i = 0; i < 21; ++i) dma_wqkv(w * 21 + i, vo);
                if (w == 0) *(LAS unsigned*)(size_t)(lds0 + LDS_WO + 2 * IMG + lane * 4) = 0u;
                tag = __hip_atomic_load((GAS u64*)ws, RLX_AGENT);
                SB();
                float a1 = 0.f, a2 = 0.f;
                sfor<28>([&](auto f) { sfor<2>([&](auto i) {
                    s1 = dot2(xf[f.value][2 * i.value], 0x3F803F80u, s1); s2 = dot2(xf[f.value][2 * i.value], xf[f.value][2 * i.value], s2);
                    a1 = dot2(xf[f.value][2 * i.value + 1], 0x3F803F80u, a1); a2 = dot2(xf[f.value][2 * i.value + 1], xf[f.value][2 * i.value + 1], a2); }); });
                s1 += a1; s2 += a2;
                tA1 = trl + 512 * hh; tA2 = tA1 + 256;
                BAR_VM();
                tag += 1;
            } else {
                const unsigned tb = LB - L_STRIDE + L_X2 + (4 * g + w) * 32768; u32x2 st[8];
                sfor<8>([&](auto i) { st[i.value] = __builtin_amdgcn_raw_buffer_load_b64(rsW, (8 * hh + i.value) * 256 + r * 8, tb + 28672, 16); });
                sfor<28>([&](auto f) { xf[f.value] = ldg(rsW, l16 + (f.value & 3) * 1024, tb + (f.value >> 2) * 4096); });
                SB();
                sfor<8>([&](auto i) { s1 += u2f(st[i.value][0]); s2 += u2f(st[i.value][1]); });
                tA1 = trl + 256 * hh; tA2 = tA1 + 512;
            }
            SB();
            STAMP(1);
            tA1 += lds0 + LDS_R1; tA2 += lds0 + LDS_R1;
            unsigned tD = lds0 + LDS_R1 + trl + 56 * 64 * hh, tA1v = tA1 + 2 * IMG, tA2v = tA2 + 2 * IMG, tDv = tD + 2 * IMG;
            pin_v(s1); pin_v(s2); pin_v(tA1); pin_v(tA2); pin_v(tD); pin_v(tA1v); pin_v(tA2v); pin_v(tDv);
            auto wfrag = [&](int img, int f) { const unsigned o = (img & 1) * IMG + xrow(f) * 64;
                if (img == 2) return (f % 7 == 6) ? trfrag(tDv + o, tDv + o + 256) : trfrag(tA1v + o, tA2v + o);
                return (f % 7 == 6) ? trfrag(tD + o, tD + o + 256) : trfrag(tA1 + o, tA2 + o); };
            f32x16 aq, ak, av; float cq = 0.f, ck = 0.f, cv = 0.f;
            u32x4 wq = wfrag(0, 0), wk = wfrag(1, 0), wv = wfrag(2, 0);
            sfor<28>([&](auto f) {
                u32x4 nq, nk, nv;
                if constexpr (f.value < 27) { nq = wfrag(0, f.value + 1); nk = wfrag(1, f.value + 1); nv = wfrag(2, f.value + 1); }
                SB();
                if constexpr (f.value == 0) mfma0(aq, wq, xf[0]); else mfma(aq, wq, xf[f.value]);
                sfor<4>([&](auto i) { cq = dot2(wq[i.value], 0x3F803F80u, cq); }); pin_v(cq);
                SB();
                if constexpr (f.value == 0) mfma0(ak, wk, xf[0]); else mfma(ak, wk, xf[f.value]);
                sfor<4>([&](auto i) { ck = dot2(wk[i.value], 0x3F803F80u, ck); }); pin_v(ck);
                SB();
                if constexpr (f.value == 0) mfma0(av, xf[0], wv); else mfma(av, xf[f.value], wv);
                sfor<4>([&](auto i) { cv = dot2(wv[i.value], 0x3F803F80u, cv); }); pin_v(cv);
                SB();
                if constexpr (f.value < 27) { wq = nq; wk = nk; wv = nv; }
            });
            s1 = half_sum(s1); s2 = half_sum(s2);
            float rstd; const u32x4 xa = make_xaug(s1, s2, rstd);
            const unsigned rb = lds0 + LDS_HALF + w * 128;
            if (hh == 0) *(LAS float*)(size_t)(rb + r * 4) = rstd;
            const u32x4 gq = make_waug(half_sum(cq), 0u), gk = make_waug(half_sum(ck), 0u), gv = make_waug(half_sum(cv), 0u);
            mfma_n(aq, gq, xa); mfma_n(ak, gk, xa); mfma_n(av, xa, gv);
            f32x4 rs4[4];
            sfor<4>([&](auto i) { rs4[i.value] = lds_ld16(rb + (8 * i.value + 4 * hh) * 4); });
            SETTLE3(aq, ak, av);
            const unsigned T = 4 * g + w;
            const float qs = rstd * (0.18898223650461361f * 1.4426950408889634f);
            u32x4 q0 = pack8(aq, 0, qs), q1 = pack8(aq, 1, qs);
            if (hh) { q1[2] = 0u; q1[3] = 0u; }
            stg(q0, rsW, l16, LB + L_Q + (j * 64 + T) * 2048); stg(q1, rsW, l16 + 1024, LB + L_Q + (j * 64 + T) * 2048);
            const unsigned kvb = LB + L_KV + (j * 64 + T) * 4096;
            stg(pack8(ak, 0, rstd), rsW, l16, kvb); stg(pack8(ak, 1, rstd), rsW, l16 + 1024, kvb);
            sfor<16>([&](auto i) { av[i.value] *= rs4[i.value >> 2][i.value & 3]; });
            u32x4 v0 = pack8(av, 0, 1.f), v1 = pack8(av, 1, 1.f);
            if (r == 28) { v0 = (u32x4){0x3F803F80u, 0x3F803F80u, 0x3F803F80u, 0x3F803F80u}; v1 = v0; }
            stg(v0, rsW, l16 + 2048, kvb); stg(v1, rsW, l16 + 3072, kvb);
        }
        STAMP(2);
        publish(layer * 5 + 0, true);
        STAMP(3);
        FRESH();
        if (w == 0) poll(layer * 5 + 0);
        if (layer == 0) {
            const unsigned vo = ((4 * g + 2 * m + t) * 32 + r) * 896 + (56 * n + 32 * u + 4 * hh) * 2;
            sfor<4>([&](auto i) { u32x2 v = __builtin_amdgcn_raw_buffer_load_b64(rsX, vo + 16 * i.value, 0, 0);
                resid[4 * i.value] = bflo(v[0]); resid[4 * i.value + 1] = bfhi(v[0]); resid[4 * i.value + 2] = bflo(v[1]); resid[4 * i.value + 3] = bfhi(v[1]); });
        }
        BAR_VM();
        STAMP(4);
        FRESH();
        {
            u32x4 kv[4][4];
            f32x16 O[4], S[2]; u32x4 p0, p1;
            const unsigned kvs = LB + L_KV + (g * 64 + 16 * w) * 4096;
            sfor<4>([&](auto c) { q_load<224 + 8 * c.value>(l16, rsW, LB + L_Q + (g * 64 + 4 * j + (w ^ c.value)) * 2048); });
            auto kvload = [&](auto tau) { kv_load(kv[tau.value & 3], l16, rsW, kvs + tau.value * 4096); };
            kvload(std::integral_constant<int, 0>{}); kvload(std::integral_constant<int, 1>{}); kvload(std::integral_constant<int, 2>{});
            const unsigned vo896 = (lane >> 2) * 896 + (lane & 3) * 16, vo3584 = (lane >> 2) * 3584 + (lane & 3) * 16;
            const unsigned ring = lds0 + LDS_SCR48 + w * 12288, ringr = ring + trl + 256 * hh;
            asm volatile("s_waitcnt vmcnt(8)\n\t; KVW %0 %1 %2 %3" : "+v"(kv[0][0]), "+v"(kv[0][1]), "+v"(kv[0][2]), "+v"(kv[0][3]) :: "memory");
            mfma0_qb<224>(S[0], kv[0][0]); mfma_qb<228>(S[0], kv[0][1]);
            SETTLE1(S[0]);
            SB();
            sfor<64>([&](auto ii) {
                constexpr int i = ii.value, tau = i >> 2, c = i & 3, nx = i + 1, ntau = nx >> 2, nc = nx & 3;
                f32x16& Sc = S[i & 1]; f32x16& Sn = S[nx & 1];
                if constexpr (c == 3 && ntau < 16) {
                    constexpr int later = (ntau + 2 < 16 ? 2 : (15 - ntau));
                    constexpr int first = ntau >= 3 ? ntau - 2 : 0;
                    constexpr int hooks = (tau < 13 ? tau : 13) - first + 1 > 0 ? (tau < 13 ? tau : 13) - first + 1 : 0;
                    constexpr int n0 = 4 * later + 6 * hooks, n1 = 4 * later + hooks;
                    kv_wait<n0, n1>(kv[ntau & 3], layer);
                }
                auto ex = [&](int k, u32x4& p) {
                    const float e0 = __builtin_amdgcn_exp2f(Sc[4 * k]), e1 = __builtin_amdgcn_exp2f(Sc[4 * k + 1]), e2 = __builtin_amdgcn_exp2f(Sc[4 * k + 2]), e3 = __builtin_amdgcn_exp2f(Sc[4 * k + 3]);
                    p[2 * (k & 1)] = cvtpk(e0, e1); p[2 * (k & 1) + 1] = cvtpk(e2, e3);
                    pin_v(p);
                };
                if constexpr (nx < 64) mfma0_qb<224 + 8 * nc>(Sn, kv[ntau & 3][0]);
                ex(0, p0); SB();
                if constexpr (nx < 64) mfma_qb<228 + 8 * nc>(Sn, kv[ntau & 3][1]);
                ex(1, p0); SB();
                if constexpr (i > 0) mfma(O[(i - 1) & 3], kv[((i - 1) >> 2) & 3][3], p1);
                ex(2, p1); SB();
                if constexpr (c == 0) {
                    if constexpr (tau < 14) {
                        if (4 * tau + w < PART1) dma_w1(4 * tau + w, vo3584); else dma_w1(w, vo3584);
                        if (layer == 0) {
                            const int p = 4 * tau + w; dma(lds0 + LDS_WO + p * 1024, vo896, rsWO, (p / 28) * 64 + n * 112 + (p % 28) * (16 * 896));
                        }
                    }
                    if (layer == 0) {
                        if constexpr (tau >= 2) {
                            constexpr int nb = (tau + 1 < 16 ? 4 : 0) + (tau - 1 < 14 ? 6 : 0) + (tau + 2 < 16 ? 4 : 0) + (tau < 14 ? 2 : 0);
                            wait_vm<nb>();
                            sfor<4>([&](auto k) { constexpr int idx = 4 * (tau - 2) + k.value; tr_to_acc<4 * idx, (idx % 12) * 1024>(ringr); });
                        }
                        if constexpr (tau < 14) sfor<4>([&](auto k) { constexpr int idx = 4 * tau + k.value;
                            dma(ring + (idx % 12) * 1024, vo896, rsW2, (448 * w + 16 * (idx >> 1)) * 896 + n * 112 + ((idx & 1) ^ u) * 64); });
                    }
                    if constexpr (tau + 3 < 16) kvload(std::integral_constant<int, tau + 3>{});
                    SB();
                }
                if constexpr (i < 4) mfma0(O[c], kv[0][2], p0); else mfma(O[c], kv[tau & 3][2], p0);
                ex(3, p1); SB();
            });
            mfma_n(O[3], kv[3][3], p1);
            SETTLE4(O[0], O[1], O[2], O[3]);
            STAMP(5);
            FRESH();
            f32x16 o = O[0];
            reduce4(o, O[1], O[2], O[3]);
            float l = upper_half(o[12]);
            float amax = 0.f;
            sfor<16>([&](auto i) { if (i.value < 12 || true) amax = __builtin_fmaxf(amax, (i.value >= 12 && hh) ? 0.f : __builtin_fabsf(o[i.value])); });
            const bool ok = l >= 5.4e-20f && l <= 1.8e19f && amax <= 3.0e38f;
            if (!__all(ok)) {
                float mrun = -INFINITY;
                float zero = 0.f; pin_v(zero);
                sfor<16>([&](auto i) { o[i.value] = zero; });
                const u32x4 qa = ldg(rsW, l16, LB + L_Q + (g * 64 + 4 * j + w) * 2048), qb = ldg(rsW, l16 + 1024, LB + L_Q + (g * 64 + 4 * j + w) * 2048);
#pragma unroll 1
                for (int T = 0; T < 64; ++T) {
                    const unsigned so = LB + L_KV + (g * 64 + T) * 4096;
                    const u32x4 k0 = ldg(rsW, l16, so), k1 = ldg(rsW, l16 + 1024, so), v0 = ldg(rsW, l16 + 2048, so), v1 = ldg(rsW, l16 + 3072, so);
                    f32x16 s;
                    mfma0_safe(s, k0, qa); mfma_safe(s, k1, qb);
                    float mt = s[0]; sfor<15>([&](auto i) { mt = __builtin_fmaxf(mt, s[i.value + 1]); });
                    const float mn = __builtin_fmaxf(mrun, half_max(mt)), al = __builtin_amdgcn_exp2f(mrun - mn);
                    mrun = mn;
                    sfor<16>([&](auto i) { o[i.value] *= al; s[i.value] = __builtin_amdgcn_exp2f(s[i.value] - mn); });
                    mfma_safe(o, v0, pack8(s, 0, 1.f)); mfma_safe(o, v1, pack8(s, 1, 1.f));
                }
                l = upper_half(o[12]);
            }
            const float rl = __builtin_amdgcn_rcpf(l);
            u32x4 o0 = pack8(o, 0, rl), o1 = pack8(o, 1, rl);
            if (hh) { o1[2] = 0u; o1[3] = 0u; }
            const unsigned ob = LB + L_O + (4 * j + w) * 32768 + g * 2048;
            stg(o0, rsW, l16, ob); stg(o1, rsW, l16 + 1024, ob);
        }
        STAMP(6);
        publish(layer * 5 + 1, true);
        STAMP(7);
        FRESH();
        if (w == 0) poll(layer * 5 + 1);
        else { const unsigned vo = (lane >> 2) * 3584 + (lane & 3) * 16; for (int p = PART1 + w - 1; p < 84; p += 3) dma_w1(p, vo); }
        BAR();
        STAMP(8);
        {
            u32x4 of[16];
            const unsigned ob = LB + L_O + (4 * g + 2 * m + t) * 32768 + u * 16384;
            sfor<16>([&](auto s) { of[s.value] = ldg(rsW, l16 + (s.value & 3) * 1024, ob + (s.value >> 2) * 4096); });
            SB();
            const unsigned tA = lds0 + LDS_WO + trl + 256 * hh + u * (8 * 28 * 64); unsigned tO = tA + u * IMG, tS = tA + (u ^ 1) * IMG;
            pin_v(tO); pin_v(tS);
            auto wfrag = [&](int img, int s) { const unsigned o = (img ? tS : tO) + (28 * (s >> 1) + 16 * (s & 1)) * 64; return trfrag(o, o + 512); };
            f32x16 a0, a1; u32x4 wa = wfrag(0, 0), wb = wfrag(1, 0);
            sfor<16>([&](auto s) {
                u32x4 na, nb;
                if constexpr (s.value < 15) { na = wfrag(0, s.value + 1); nb = wfrag(1, s.value + 1); }
                SB();
                if constexpr (s.value == 0) { mfma0(a0, wa, of[0]); mfma0(a1, wb, of[0]); } else { mfma(a0, wa, of[s.value]); mfma(a1, wb, of[s.value]); }
                SB();
                if constexpr (s.value < 15) { wa = na; wb = nb; }
            });
            SETTLE2(a0, a1);
            const f32x16 snd = a1, own = a0;
            sfor<4>([&](auto k) { lds_st16(lds0 + LDS_SCR16 + w * 4096 + k.value * 1024 + l16, (f32x4){snd[4 * k.value], snd[4 * k.value + 1], snd[4 * k.value + 2], snd[4 * k.value + 3]}); });
            BAR();
            sfor<4>([&](auto k) { f32x4 v = lds_ld16(lds0 + LDS_SCR16 + (w ^ 1) * 4096 + k.value * 1024 + l16); sfor<4>([&](auto e) { resid[4 * k.value + e.value] += own[4 * k.value + e.value] + v[e.value]; }); });
            publish_x(LB + L_X1);
        }
        STAMP(9);
        publish(layer * 5 + 2, false);
        STAMP(10);
        FRESH();
        {
            const unsigned hl = ((lane & 15) >> 2) * 32 + (lane & 3) * 8;
            if (w == 0) poll(layer * 5 + 2);
            else {
                if (w == 3) { const unsigned vo = (lane >> 1) * 3584 + (lane & 1) * 16; for (int p = 0; p < 14; ++p) dma(lds0 + LDS_HALF + p * 1024, vo, rsW1, j * 224 + 192 + p * (32 * 3584)); }
                if (layer == 0) {
                    float cs = 0.f; const unsigned a = lds0 + LDS_R1 + (w - 1) * IMG + trl + 256 * hh;
#pragma unroll 4
                    for (int f = 0; f < 28; ++f) { const u32x4 v = trfrag(a + f * 1024, a + f * 1024 + 512); sfor<4>([&](auto i) { cs = dot2(v[i.value], 0x3F803F80u, cs); }); }
                    u32x4 ga = make_waug(half_sum(cs), b1[112 * j + 32 * (w - 1) + r]);
                    if (hh == 0) *(LAS u32x4*)(size_t)(lds0 + LDS_AUG + (w - 1) * 512 + r * 16) = ga;
                    if (w == 3) {
                        WAIT_VM0();
                        cs = 0.f; const unsigned ah = lds0 + LDS_HALF + hl + 128 * hh;
#pragma unroll 4
                        for (int f = 0; f < 28; ++f) { const u32x4 v = trfrag(ah + f * 512, ah + f * 512 + 256); sfor<4>([&](auto i) { cs = dot2(v[i.value], 0x3F803F80u, cs); }); }
                        ga = make_waug(half_sum(cs), b1[112 * j + 96 + (r & 15)]);
                        if (hh == 0) *(LAS u32x4*)(size_t)(lds0 + LDS_AUG + 3 * 512 + r * 16) = ga;
                    }
                }
            }
            BAR_VM();
            STAMP(11);
            u32x4 xf[28]; u32x2 st[8];
            const unsigned tb = LB + L_X1 + (4 * g + w) * 32768;
            sfor<8>([&](auto i) { st[i.value] = __builtin_amdgcn_raw_buffer_load_b64(rsW, (8 * hh + i.value) * 256 + r * 8, tb + 28672, 16); });
            sfor<28>([&](auto f) { xf[f.value] = ldg(rsW, l16 + (f.value & 3) * 1024, tb + (f.value >> 2) * 4096); });
            SB();
            float s1 = 0.f, s2 = 0.f;
            sfor<8>([&](auto i) { s1 += u2f(st[i.value][0]); s2 += u2f(st[i.value][1]); });
            s1 = half_sum(s1); s2 = half_sum(s2);
            float rstd; u32x4 xa = make_xaug(s1, s2, rstd);
            float c1 = rstd * 1.4426950408889634f; pin_v(xa); pin_v(c1);
            SB();
            unsigned tA = lds0 + LDS_R1 + trl + 256 * hh, tD = lds0 + LDS_R1 + trl + 56 * 64 * hh, tA2 = tA + 2 * IMG, tD2 = tD + 2 * IMG;
            unsigned hA = lds0 + LDS_HALF + hl + 128 * hh, hD = lds0 + LDS_HALF + hl + 56 * 32 * hh, tG = lds0 + LDS_AUG + r * 16;
            pin_v(tA); pin_v(tD); pin_v(tA2); pin_v(tD2); pin_v(hA); pin_v(hD); pin_v(tG);
            auto wfrag = [&](int img, int f) {
                if (f == 28) return *(LAS u32x4*)(size_t)(tG + img * 512);
                if (img == 3) { const unsigned o = xrow(f) * 32; return (f % 7 == 6) ? trfrag(hD + o, hD + o + 128) : trfrag(hA + o, hA + o + 256); }
                const unsigned o = (img & 1) * IMG + xrow(f) * 64;
                if (img == 2) return (f % 7 == 6) ? trfrag(tD2 + o, tD2 + o + 256) : trfrag(tA2 + o, tA2 + o + 512);
                return (f % 7 == 6) ? trfrag(tD + o, tD + o + 256) : trfrag(tA + o, tA + o + 512); };
            f32x16 h0, h1, h2, h3; u32x4 wa = wfrag(0, 0), wb = wfrag(1, 0);
            sfor<28>([&](auto f) {
                const u32x4 na = wfrag(0, f.value + 1), nb = wfrag(1, f.value + 1);
                SB();
                if constexpr (f.value == 0) { mfma0(h0, wa, xf[0]); mfma0(h1, wb, xf[0]); } else { mfma(h0, wa, xf[f.value]); mfma(h1, wb, xf[f.value]); }
                SB();
                wa = na; wb = nb;
            });
            u32x4 wc = wfrag(2, 0);
            mfma_n(h0, wa, xa); mfma_n(h1, wb, xa);
            const unsigned ab = LB + L_AF + (4 * g + w) * 114688 + j * 7168;
            auto mish2 = [&](float a, float b2) {
                const float ta = a * c1, tb2 = b2 * c1;
                const float ea = __builtin_amdgcn_exp2f(ta), eb = __builtin_amdgcn_exp2f(tb2);
                float da = __builtin_fmaf(ea, -0.5f, -1.f), db = __builtin_fmaf(eb, -0.5f, -1.f);
                pin_v(da); pin_v(db);
                da = __builtin_fmaf(ea, da, -1.f); db = __builtin_fmaf(eb, db, -1.f);
                const float ra = __builtin_amdgcn_rcpf(da), rb2 = __builtin_amdgcn_rcpf(db);
                float ya = __builtin_fmaf(ta, ra, ta), yb = __builtin_fmaf(tb2, rb2, tb2);
                pin_v(ya); pin_v(yb);
                return cvtpk(ya, yb);
            };
            u32x4 pk;
            auto tile_pass = [&](f32x16& acc, auto img, u32x4& wcur, f32x16& prev, int fbase) {
                sfor<29>([&](auto f) {
                    u32x4 nw;
                    if constexpr (f.value < 28) nw = wfrag(img.value, f.value + 1); else if constexpr (img.value == 2) nw = wfrag(3, 0);
                    SB();
                    if constexpr (f.value == 0) mfma0(acc, wcur, xf[0]); else if constexpr (f.value < 28) mfma(acc, wcur, xf[f.value]); else mfma(acc, wcur, xa);
                    if constexpr (f.value >= 4 && f.value < 28 && (f.value - 4) % 3 == 0) {
                        constexpr int k = (f.value - 4) / 3;
                        pk[k & 3] = mish2(prev[2 * k], prev[2 * k + 1]); pin_v(pk);
                        if constexpr ((k & 3) == 3) stg(pk, rsW, l16, ab + (fbase + (k >> 2)) * 1024);
                    }
                    SB();
                    if constexpr (f.value < 28 || img.value == 2) wcur = nw;
                });
            };
            tile_pass(h2, std::integral_constant<int, 2>{}, wc, h0, 0);
            tile_pass(h3, std::integral_constant<int, 3>{}, wc, h1, 2);
            sfor<8>([&](auto k) { pk[k.value & 3] = mish2(h2[2 * k.value], h2[2 * k.value + 1]); if constexpr ((k.value & 3) == 3) stg(pk, rsW, l16, ab + (4 + (k.value >> 2)) * 1024); });
            SETTLE1(h3);
            sfor<4>([&](auto k) { pk[k.value] = mish2(h3[2 * k.value], h3[2 * k.value + 1]); });
            stg(pk, rsW, l16, ab + 6 * 1024);
        }
        STAMP(12);
        publish(layer * 5 + 3, false);
        STAMP(13);
        FRESH();
        if (w == 0) poll(layer * 5 + 3);
        else if (layer == 0) { const unsigned vo = (lane >> 2) * 2688 + (lane & 3) * 16; for (int p = w - 1; p < PART1; p += 3) dma_wqkv(p, vo); }
        BAR();
        STAMP(14);
        {
            constexpr int WIN = 18;
            u32x4 af[28][2];
            const unsigned ab = LB + L_AF + (4 * g + 2 * m + t) * 114688 + w * 28672, ab2 = LB + L_AF + (4 * g + 2 * m + (t ^ 1)) * 114688 + w * 28672;
            auto aload = [&](auto k) { af[k.value][0] = ldg(rsW, l16 + (k.value & 3) * 1024, ab + (k.value >> 2) * 4096); af[k.value][1] = ldg(rsW, l16 + (k.value & 3) * 1024, ab2 + (k.value >> 2) * 4096); };
            sfor<WIN>([&](auto k) { aload(k); });
            SB();
            f32x16 y[4];
            sfor<28>([&](auto k) {
                if constexpr (k.value == 0) { mfma0_wa<0>(y[0], af[0][0]); mfma0_wa<4>(y[1], af[0][0]); mfma0_wa<0>(y[2], af[0][1]); mfma0_wa<4>(y[3], af[0][1]); }
                else { mfma_wa<8 * k.value>(y[0], af[k.value][0]); mfma_wa<8 * k.value + 4>(y[1], af[k.value][0]);
                       mfma_wa<8 * k.value>(y[2], af[k.value][1]); mfma_wa<8 * k.value + 4>(y[3], af[k.value][1]); }
                SB();
                if constexpr (k.value + WIN < 28) aload(std::integral_constant<int, k.value + WIN>{});
                SB();
            });
            SETTLE4(y[0], y[1], y[2], y[3]);
            STAMP(15);
            FRESH();
            f32x16 o = y[0];
            reduce4(o, y[1], y[2], y[3]);
            sfor<16>([&](auto i) { resid[i.value] = __builtin_fmaf(o[i.value], 0.6931471805599453f, resid[i.value]); });
        }
        if (layer == 0) {
            publish_x(LB + L_X2);
            STAMP(16);
            publish(4, false);
            STAMP(17);
            if (w == 0) poll(4);
            else { const unsigned vo = (lane >> 2) * 2688 + (lane & 3) * 16; for (int p = PART1 + w - 1; p < 84; p += 3) dma_wqkv(p, vo); }
            BAR_VM();
        } else {
            const u32x4 p0 = pack8(resid, 0, 1.f), p1 = pack8(resid, 1, 1.f);
            GAS char* op = (GAS char*)out + ((4 * g + 2 * m + t) * 32 + r) * 896 + 112 * n + 64 * u + 16 * hh;
            auto a = __builtin_amdgcn_permlane32_swap(p0[0], p0[2], false, false), c = __builtin_amdgcn_permlane32_swap(p0[1], p0[3], false, false);
            *(GAS u32x4*)op = (u32x4){a[0], c[0], a[1], c[1]};
            a = __builtin_amdgcn_permlane32_swap(p1[0], p1[2], false, false); c = __builtin_amdgcn_permlane32_swap(p1[1], p1[3], false, false);
            if (u == 0 || hh == 0) *(GAS u32x4*)(op + 32) = (u32x4){a[0], c[0], a[1], c[1]};
            if (b == 0 && threadIdx.x == 0) __hip_atomic_store((GAS u64*)ws, tag, RLX_AGENT);
            STAMP(16);
        }
    }
}

extern "C" void kernel_launch(void* const* d_in, const int* in_sizes, int n_in, void* d_out, int out_size, void* d_ws, size_t ws_size, hipStream_t stream) {
    (void)hipFuncSetAttribute((const void*)mega, hipFuncAttributeMaxDynamicSharedMemorySize, LDS_BYTES);
    mega<<<256, 256, LDS_BYTES, stream>>>((const u16*)d_in[0], (const u16*)d_in[1], (const u16*)d_in[2], (const u16*)d_in[4], (const u16*)d_in[5], (const u16*)d_in[6],
                                          (u16*)d_out, (char*)d_ws);
}
```

```cpp
#include <hip/hip_runtime.h>
#include <utility>

#define DI __device__ __forceinline__
#define LAS __attribute__((address_space(3)))
#define GAS __attribute__((address_space(1)))
typedef float f32x16 __attribute__((ext_vector_type(16)));
typedef float f32x4 __attribute__((ext_vector_type(4)));
typedef float f32x2 __attribute__((ext_vector_type(2)));
typedef unsigned u32x4 __attribute__((ext_vector_type(4)));
typedef unsigned u32x2 __attribute__((ext_vector_type(2)));
typedef short v4i16 __attribute__((ext_vector_type(4)));
typedef __bf16 bf16x2 __attribute__((ext_vector_type(2)));
typedef __amdgpu_buffer_rsrc_t rsrc_t;
typedef unsigned long long u64;
typedef unsigned short u16;

constexpr unsigned WS_FLAGS = 4096;
constexpr unsigned WS_STAMP = 200u << 20;
constexpr unsigned WS_X0 = 1u << 20;
constexpr unsigned WS_WT = 3u << 20;
constexpr unsigned T_Q = 0, T_1 = 86016, T_O = T_1 + 100352, T_2 = T_O + 57344, T_CQ = T_2 + 229376, T_C1 = T_CQ + 6144, T_STRIDE = 491520;
constexpr unsigned WS_DATA = 11u << 20;
constexpr unsigned L_Q = 0, L_KV = 2u << 20, L_O = 6u << 20, L_X1 = 8u << 20, L_AF = 10u << 20, L_X2 = 17u << 20, L_STRIDE = 19u << 20;
constexpr unsigned WS_BYTES = WS_DATA + 2 * L_STRIDE;
constexpr unsigned IMG = 28672;
constexpr unsigned LDS_WO = 0;
constexpr unsigned LDS_R1 = 57600;
constexpr unsigned LDS_HALF = LDS_R1 + 3 * IMG;
constexpr unsigned LDS_AUG = 160256;
constexpr unsigned LDS_SCR48 = LDS_AUG - 49152;
constexpr unsigned LDS_SCR16 = LDS_AUG - 16384;
constexpr unsigned LDS_BYTES = 163840;
#define CODE_BYTES 23000

#define SB() __builtin_amdgcn_sched_barrier(0)
#define BAR() asm volatile("s_waitcnt lgkmcnt(0)\n\ts_barrier" ::: "memory")
#define BAR_VM() asm volatile("s_waitcnt vmcnt(0) lgkmcnt(0)\n\ts_barrier" ::: "memory")
#define WAIT_VM0() asm volatile("s_waitcnt vmcnt(0)" ::: "memory")
#define RLX_AGENT __ATOMIC_RELAXED, __HIP_MEMORY_SCOPE_AGENT

template <int N, class F> DI void sfor_impl(F&& f, std::integral_constant<int, N>) {}
template <class F, int... Is> DI void sfor_seq(F&& f, std::integer_sequence<int, Is...>) { (f(std::integral_constant<int, Is>{}), ...); }
template <int N, class F> DI void sfor(F&& f) { sfor_seq(f, std::make_integer_sequence<int, N>{}); }

DI void mfma(f32x16& c, const u32x4& a, const u32x4& b) { asm volatile("v_mfma_f32_32x32x16_bf16 %0, %1, %2, %0" : "+v"(c) : "v"(a), "v"(b)); }
DI void mfma0(f32x16& c, const u32x4& a, const u32x4& b) { asm volatile("v_mfma_f32_32x32x16_bf16 %0, %1, %2, 0" : "=&v"(c) : "v"(a), "v"(b)); }
template <int R> DI void mfma_qb(f32x16& c, const u32x4& a) { asm volatile("v_mfma_f32_32x32x16_bf16 %0, %1, a[%c2:%c3], %0" : "+v"(c) : "v"(a), "i"(R), "i"(R + 3)); }
template <int R> DI void mfma0_qb(f32x16& c, const u32x4& a) { asm volatile("v_mfma_f32_32x32x16_bf16 %0, %1, a[%c2:%c3], 0" : "=&v"(c) : "v"(a), "i"(R), "i"(R + 3)); }
template <int R> DI void mfma_wa(f32x16& c, const u32x4& b) { asm volatile("v_mfma_f32_32x32x16_bf16 %0, a[%c2:%c3], %1, %0" : "+v"(c) : "v"(b), "i"(R), "i"(R + 3)); }
template <int R> DI void mfma0_wa(f32x16& c, const u32x4& b) { asm volatile("v_mfma_f32_32x32x16_bf16 %0, a[%c2:%c3], %1, 0" : "=&v"(c) : "v"(b), "i"(R), "i"(R + 3)); }
template <int R> DI void mfmaz_qb(f32x16& c, const u32x4& a) { asm volatile("v_mfma_f32_32x32x16_bf16 %0, %1, a[%c2:%c3], 0" : "+v"(c) : "v"(a), "i"(R), "i"(R + 3)); }
template <int R, int OFF> DI void acc_load2(unsigned voff, __amdgpu_buffer_rsrc_t rs, unsigned soff) {
    asm volatile("buffer_load_dwordx4 a[%c3:%c4], %0, %1, %2 offen offset:%c7 sc1\n\tbuffer_load_dwordx4 a[%c5:%c6], %0, %1, %2 offen offset:%c8 sc1"
                 :: "v"(voff), "s"(rs), "s"(soff), "i"(R), "i"(R + 3), "i"(R + 4), "i"(R + 7), "i"(OFF), "i"(OFF + 1024) : "memory", "a255"); }
template <int R, int OFF> DI void acc_load1(unsigned voff, __amdgpu_buffer_rsrc_t rs, unsigned soff) {
    asm volatile("buffer_load_dwordx4 a[%c3:%c4], %0, %1, %2 offen offset:%c5 sc1" :: "v"(voff), "s"(rs), "s"(soff), "i"(R), "i"(R + 3), "i"(OFF) : "memory"); }
DI void mfma_n(f32x16& c, const u32x4& a, const u32x4& b) { asm volatile("s_nop 1\n\tv_mfma_f32_32x32x16_bf16 %0, %1, %2, %0" : "+v"(c) : "v"(a), "v"(b)); }
DI void mfma0_safe(f32x16& c, const u32x4& a, const u32x4& b) { asm volatile("s_nop 1\n\tv_mfma_f32_32x32x16_bf16 %0, %1, %2, 0\n\ts_nop 11" : "=&v"(c) : "v"(a), "v"(b)); }
DI void mfma_safe(f32x16& c, const u32x4& a, const u32x4& b) { asm volatile("s_nop 1\n\tv_mfma_f32_32x32x16_bf16 %0, %1, %2, %0\n\ts_nop 11" : "+v"(c) : "v"(a), "v"(b)); }
#define SETTLE1(a) asm volatile("s_nop 11" : "+v"(a))
#define SETTLE2(a, b) asm volatile("s_nop 11" : "+v"(a), "+v"(b))
#define SETTLE3(a, b, c) asm volatile("s_nop 11" : "+v"(a), "+v"(b), "+v"(c))
#define SETTLE4(a, b, c, d) asm volatile("s_nop 11" : "+v"(a), "+v"(b), "+v"(c), "+v"(d))

DI unsigned cvtpk(float lo, float hi) { f32x2 v = {lo, hi}; bf16x2 b = __builtin_convertvector(v, bf16x2); return __builtin_bit_cast(unsigned, b); }
DI float bflo(unsigned u) { return __builtin_bit_cast(float, u << 16); }
DI float bfhi(unsigned u) { return __builtin_bit_cast(float, u & 0xffff0000u); }
DI float dot2(unsigned a, unsigned b, float c) { return __builtin_amdgcn_fdot2_f32_bf16(__builtin_bit_cast(bf16x2, a), __builtin_bit_cast(bf16x2, b), c, false); }
DI float u2f(unsigned u) { return __builtin_bit_cast(float, u); }
DI unsigned f2u(float f) { return __builtin_bit_cast(unsigned, f); }
DI float half_sum(float v) { auto r = __builtin_amdgcn_permlane32_swap(f2u(v), f2u(v), false, false); return u2f(r[0]) + u2f(r[1]); }
DI float half_max(float v) { auto r = __builtin_amdgcn_permlane32_swap(f2u(v), f2u(v), false, false); return __builtin_fmaxf(u2f(r[0]), u2f(r[1])); }
DI float upper_half(float v) { auto r = __builtin_amdgcn_permlane32_swap(f2u(v), f2u(v), false, false); return u2f(r[1]); }
#define LDAUX 16
DI u32x4 ldg(rsrc_t rs, unsigned voff, unsigned soff) { return __builtin_amdgcn_raw_buffer_load_b128(rs, voff, soff, LDAUX); }
DI void stg(u32x4 v, rsrc_t rs, unsigned voff, unsigned soff) { __builtin_amdgcn_raw_buffer_store_b128(v, rs, voff, soff, 16); asm volatile("s_nop 1" : "+v"(v) :: "memory"); }
DI void dma(unsigned lds, unsigned voff, rsrc_t rs, unsigned soff) {
    asm volatile("s_nop 4\n\ts_mov_b32 m0, %0\n\ts_nop 0\n\tbuffer_load_dwordx4 %1, %2, %3 offen lds"
                 :: "s"(lds), "v"(voff), "s"(rs), "s"(__builtin_amdgcn_readfirstlane(soff)) : "memory"); }
DI void dma4(unsigned lds, unsigned voff, rsrc_t rs, unsigned soff) {
    asm volatile("s_mov_b32 m0, %0\n\ts_nop 0\n\tbuffer_load_dwordx4 %1, %2, %3 offen sc1 lds\n\tbuffer_load_dwordx4 %1, %2, %3 offen offset:1024 sc1 lds\n\t"
                 "buffer_load_dwordx4 %1, %2, %3 offen offset:2048 sc1 lds\n\tbuffer_load_dwordx4 %1, %2, %3 offen offset:3072 sc1 lds" :: "s"(lds), "v"(voff), "s"(rs), "s"(soff) : "memory"); }
DI void pin_v(u32x4& x) { asm volatile("" : "+v"(x)); }
DI void pin_v(unsigned& x) { asm volatile("" : "+v"(x)); }
DI void pin_v(float& x) { asm volatile("" : "+v"(x)); }
template <class T> DI void def_v(T& x) { asm volatile("" : "=v"(x)); }
DI void pin_v(f32x16& x) { asm volatile("" : "+v"(x)); }
template <int O0> DI void kv_load2(u32x4& d0, u32x4& d1, unsigned voff, rsrc_t rs, unsigned soff) {
    asm volatile("buffer_load_dwordx4 %0, %2, %3, %4 offen offset:%c5 sc1\n\tbuffer_load_dwordx4 %1, %2, %3, %4 offen offset:%c6 sc1"
                 : "+v"(d0), "+v"(d1) : "v"(voff), "s"(rs), "s"(soff), "i"(O0), "i"(O0 + 1024) : "memory"); }
DI void kv_load(u32x4 (&d)[4], unsigned voff, rsrc_t rs, unsigned soff) {
    asm volatile("buffer_load_dwordx4 %0, %4, %5, %6 offen sc1\n\tbuffer_load_dwordx4 %1, %4, %5, %6 offen offset:1024 sc1\n\t"
                 "buffer_load_dwordx4 %2, %4, %5, %6 offen offset:2048 sc1\n\tbuffer_load_dwordx4 %3, %4, %5, %6 offen offset:3072 sc1"
                 : "+v"(d[0]), "+v"(d[1]), "+v"(d[2]), "+v"(d[3]) : "v"(voff), "s"(rs), "s"(soff) : "memory"); }
template <int N> DI void kv_wait(u32x4 (&d)[4]) {
    asm volatile("s_waitcnt vmcnt(%c4)\n\t; KVW %0 %1 %2 %3" : "+v"(d[0]), "+v"(d[1]), "+v"(d[2]), "+v"(d[3]) : "i"(N) : "memory"); }
template <int N> DI void kv_wait2(u32x4& a, u32x4& b) { asm volatile("s_waitcnt vmcnt(%c2)\n\t; KVW %0 %1" : "+v"(a), "+v"(b) : "i"(N) : "memory"); }
template <int N> DI void bar_vm() { asm volatile("s_waitcnt vmcnt(%c0) lgkmcnt(0)\n\ts_barrier" :: "i"(N) : "memory"); }
DI u32x4 lds_frag(unsigned a) { return *(LAS u32x4*)(size_t)a; }
DI u32x4 trfrag(unsigned a1, unsigned a2) {
    v4i16 lo = __builtin_amdgcn_ds_read_tr16_b64_v4i16((LAS v4i16*)(size_t)a1), hi = __builtin_amdgcn_ds_read_tr16_b64_v4i16((LAS v4i16*)(size_t)a2);
    u32x2 l = __builtin_bit_cast(u32x2, lo), h = __builtin_bit_cast(u32x2, hi);
    return (u32x4){l[0], l[1], h[0], h[1]}; }
DI void lds_st16(unsigned a, f32x4 v) { *(LAS f32x4*)(size_t)a = v; }
DI f32x4 lds_ld16(unsigned a) { return *(LAS f32x4*)(size_t)a; }
constexpr int xrow(int f) { return (f % 7 == 6) ? 112 * (f / 7) + 48 : 112 * (f / 7) + (f % 7 >= 3 ? 56 : 0) + 16 * ((f % 7) % 3); }
DI u32x4 pack8(const f32x16& v, int s, float k) {
    return (u32x4){cvtpk(v[8 * s] * k, v[8 * s + 1] * k), cvtpk(v[8 * s + 2] * k, v[8 * s + 3] * k), cvtpk(v[8 * s + 4] * k, v[8 * s + 5] * k), cvtpk(v[8 * s + 6] * k, v[8 * s + 7] * k)}; }
DI unsigned hi_lo(float v, float& hi) { unsigned h = cvtpk(v, 0.f); hi = bflo(h); return h & 0xffffu; }

#define TS(i) do {} while (0)
#define TSFLUSH(n, base) do {} while (0)
#define STAMP(i) do {} while (0)
#define PRESLEEP 8

__global__ __launch_bounds__(256, 1) void mega(const u16* __restrict__ x, const u16* __restrict__ wqkv, const u16* __restrict__ wo, const u16* __restrict__ w1,
                                               const u16* __restrict__ b1, const u16* __restrict__ w2, u16* __restrict__ out, char* __restrict__ ws) {
    int lane = threadIdx.x & 63, w = __builtin_amdgcn_readfirstlane(threadIdx.x >> 6);
    const int b = blockIdx.x;
    int g = (b & 7) * 2 + (b >> 7), j = (b >> 3) & 15;
    int r, hh, m, n, t, u; unsigned trl, l16;
#define FRESH() do { asm volatile("" : "+s"(g), "+s"(j), "+s"(w), "+v"(lane)); r = lane & 31; hh = lane >> 5; m = j >> 3; n = j & 7; t = w >> 1; u = w & 1; \
        trl = ((lane & 15) >> 2) * 64 + ((lane >> 4) & 1) * 32 + (lane & 3) * 8; l16 = lane * 16; } while (0)
    const rsrc_t rsW = __builtin_amdgcn_make_buffer_rsrc((void*)ws, 0, WS_BYTES, 0x00020000);
    u64 tag;
    bool dead = false;
    f32x16 resid;
    int layer = 0;

    auto flag_up = [&](int S, bool cross) { __hip_atomic_store((GAS u64*)(ws + WS_FLAGS + ((S * 16 + (cross ? j : g)) * 16 + (cross ? g : j)) * 256), tag, RLX_AGENT); };
    auto publish = [&](int S, bool cross) {
        BAR_VM();
        if (w == 0 && lane == 0) flag_up(S, cross);
    };
    auto poll_at = [&](unsigned off) {
        GAS u64* f = (GAS u64*)(ws + WS_FLAGS + off);
        __builtin_amdgcn_s_sleep(PRESLEEP);
        if (!dead) {
            for (int spin = 0;; ++spin) {
                u64 v = __hip_atomic_load(f, RLX_AGENT);
                if (__all(v == tag)) break;
                if (spin > 400000) { dead = true; break; }
                __builtin_amdgcn_s_sleep(1);
            }
        }
    };
    auto poll = [&](int S) { poll_at(((S * 16 + g) * 16 + (lane & 15)) * 256); };
    auto stage = [&](unsigned lds, unsigned src, int g0, int g1, int w0, int nw) {
        for (int q = g0 + w - w0; q < g1; q += nw) dma4(lds + q * 4096, l16, rsW, WS_WT + j * T_STRIDE + src + q * 4096);
    };
    auto publish_x = [&](unsigned base) {
        const unsigned tb = base + (4 * g + 2 * m + t) * 32768, f0 = 7 * (n >> 1) + 3 * (n & 1);
        u32x4 p0 = pack8(resid, 0, 1.f), p1 = pack8(resid, 1, 1.f);
        if (u == 0) { stg(p0, rsW, l16, tb + f0 * 1024); stg(p1, rsW, l16, tb + f0 * 1024 + 1024); }
        else {
            stg(p0, rsW, l16, tb + f0 * 1024 + 2048);
            auto a = __builtin_amdgcn_permlane32_swap(p1[0], p1[0], false, false), c = __builtin_amdgcn_permlane32_swap(p1[1], p1[1], false, false);
            if (hh == (n & 1)) stg((u32x4){a[0], c[0], a[1], c[1]}, rsW, l16, tb + (7 * (n >> 1) + 6) * 1024);
            p1[2] = 0u; p1[3] = 0u;
        }
        float s1 = 0.f, s2 = 0.f, s3 = 0.f, s4 = 0.f;
        sfor<4>([&](auto i) { s1 = dot2(p0[i.value], 0x3F803F80u, s1); s2 = dot2(p0[i.value], p0[i.value], s2); s3 = dot2(p1[i.value], 0x3F803F80u, s3); s4 = dot2(p1[i.value], p1[i.value], s4); });
        s1 = half_sum(s1 + s3); s2 = half_sum(s2 + s4);
        if (hh == 0) __builtin_amdgcn_raw_buffer_store_b64((u32x2){f2u(s1), f2u(s2)}, rsW, r * 8, tb + 28672 + (2 * n + u) * 256, 16);
    };
    auto reduce4 = [&](f32x16& o, const f32x16& v1, const f32x16& v2, const f32x16& v3) {
        unsigned sw = LDS_SCR48 + w * 12288 + l16; pin_v(sw);
        sfor<4>([&](auto k) { lds_st16(sw + k.value * 1024, (f32x4){v1[4 * k.value], v1[4 * k.value + 1], v1[4 * k.value + 2], v1[4 * k.value + 3]}); });
        sfor<4>([&](auto k) { lds_st16(sw + 4096 + k.value * 1024, (f32x4){v2[4 * k.value], v2[4 * k.value + 1], v2[4 * k.value + 2], v2[4 * k.value + 3]}); });
        sfor<4>([&](auto k) { lds_st16(sw + 8192 + k.value * 1024, (f32x4){v3[4 * k.value], v3[4 * k.value + 1], v3[4 * k.value + 2], v3[4 * k.value + 3]}); });
        BAR();
        sfor<3>([&](auto d) { unsigned a = LDS_SCR48 + (w ^ (d.value + 1)) * 12288 + d.value * 4096 + l16; pin_v(a);
            sfor<4>([&](auto k) { f32x4 v = lds_ld16(a + k.value * 1024); sfor<4>([&](auto e) { o[4 * k.value + e.value] += v[e.value]; }); }); });
    };
    auto make_xaug = [&](const u32x2 (&st)[8], float& rstd) {
        float s1 = (u2f(st[0][0]) + u2f(st[1][0])) + (u2f(st[2][0]) + u2f(st[3][0])) + ((u2f(st[4][0]) + u2f(st[5][0])) + (u2f(st[6][0]) + u2f(st[7][0])));
        float s2 = (u2f(st[0][1]) + u2f(st[1][1])) + (u2f(st[2][1]) + u2f(st[3][1])) + ((u2f(st[4][1]) + u2f(st[5][1])) + (u2f(st[6][1]) + u2f(st[7][1])));
        s1 = half_sum(s1); s2 = half_sum(s2);
        const float mean = s1 * (1.f / 448.f), var = __builtin_fmaxf(s2 * (1.f / 448.f) - mean * mean, 0.f) + 1e-5f;
        rstd = __builtin_amdgcn_rsqf(var);
        float mh, sh; const unsigned mhb = hi_lo(-mean, mh), shb = hi_lo(var * rstd, sh);
        const unsigned d0 = cvtpk(mh, -mean - mh), d1 = mhb | (shb << 16), d2 = cvtpk(var * rstd - sh, 0.f);
        return hh == 0 ? (u32x4){d0, d1, d2, 0u} : (u32x4){0u, 0u, 0u, 0u};
    };
    auto make_waug = [&](float cs, unsigned bias16) {
        float ch; const unsigned chb = hi_lo(cs, ch);
        return (u32x4){chb | (chb << 16), (cvtpk(cs - ch, 0.f) & 0xffffu) | (bias16 << 16), bias16, 0u};
    };

    FRESH();
    STAMP(18);
    {
        const rsrc_t rsX = __builtin_amdgcn_make_buffer_rsrc((void*)x, 0, 2048 * 896, 0x00020000);
        const rsrc_t rsQKV = __builtin_amdgcn_make_buffer_rsrc((void*)wqkv, 0, 448 * 2688, 0x00020000);
        const rsrc_t rsWO = __builtin_amdgcn_make_buffer_rsrc((void*)wo, 0, 448 * 896, 0x00020000);
        const rsrc_t rsW1 = __builtin_amdgcn_make_buffer_rsrc((void*)w1, 0, 448 * 3584, 0x00020000);
        const rsrc_t rsW2 = __builtin_amdgcn_make_buffer_rsrc((void*)w2, 0, 1792 * 896, 0x00020000);
        const unsigned stg0 = LDS_R1 + w * 9216, wt = WS_WT + j * T_STRIDE;
        u32x2 xv[4];
        const unsigned vx = ((4 * g + 2 * m + t) * 32 + r) * 896 + (56 * n + 32 * u + 4 * hh) * 2;
        sfor<4>([&](auto i) { xv[i.value] = __builtin_amdgcn_raw_buffer_load_b64(rsX, vx + 16 * i.value, 0, 0); });
        const int nf = g < 12 ? 2 : 1, row = lane >> 2; const unsigned c16 = (lane & 3) * 16;
        for (int i = 0; i < nf; ++i) {
            const int f = g + 16 * i, fm = f % 7, r0 = 112 * (f / 7) + (fm == 6 ? 48 : (fm >= 3 ? 8 : 0) + 16 * fm);
            const unsigned rr = row + ((fm == 6 && row >= 8) ? 48 : 0);
            if (w < 3) dma(stg0 + i * 1024, rr * 2688 + c16, rsQKV, r0 * 2688 + w * 896 + j * 56);
            dma(stg0 + (2 + i) * 1024, rr * 3584 + c16, rsW1, r0 * 3584 + j * 224 + w * 64);
        }
        const int n2 = w == 3 ? 5 : 3; const unsigned v896 = row * 896 + c16;
        for (int i = 0; i < n2; ++i) { const int idx = 14 * g + 3 * w + i; dma(stg0 + (4 + i) * 1024, v896, rsW2, (idx >> 1) * (16 * 896) + n * 112 + (idx & 1) * 64); }
        const int po = g + 16 * w; u32x4 ov;
        if (po < 56) ov = __builtin_amdgcn_raw_buffer_load_b128(rsWO, v896, (po / 28) * 64 + n * 112 + (po % 28) * (16 * 896), 0);
        tag = __hip_atomic_load((GAS u64*)ws, RLX_AGENT);
        const unsigned cw = *(GAS unsigned*)((GAS char*)__builtin_amdgcn_s_getpc() + min((unsigned)(((b >> 3) * 4 + w) * 8 + (lane & 7)) * 64u, (unsigned)CODE_BYTES));
        if (w == 0) *(LAS unsigned*)(size_t)(LDS_WO + 2 * IMG + lane * 4) = 0u;
        SB();
        sfor<4>([&](auto i) { resid[4 * i.value] = bflo(xv[i.value][0]); resid[4 * i.value + 1] = bfhi(xv[i.value][0]); resid[4 * i.value + 2] = bflo(xv[i.value][1]); resid[4 * i.value + 3] = bfhi(xv[i.value][1]); });
        { unsigned keep = cw; pin_v(keep); }
        WAIT_VM0();
        STAMP(19);
        tag += 1;
        publish_x(WS_X0);
        if (po < 56) stg(ov, rsW, l16, wt + T_O + po * 1024);
        float cq = 0.f, c1 = 0.f;
        for (int i = 0; i < nf; ++i) {
            const int f = g + 16 * i; const bool D = f % 7 == 6;
            const unsigned a = stg0 + i * 1024 + trl + (D ? 512 : 256) * hh, d2 = D ? 256 : 512;
            if (w < 3) { const u32x4 v = trfrag(a, a + d2); stg(v, rsW, l16, wt + T_Q + (3 * f + w) * 1024); sfor<4>([&](auto k) { cq = dot2(v[k.value], 0x3F803F80u, cq); }); }
            const u32x4 v = trfrag(a + 2048, a + 2048 + d2);
            if (w < 3) stg(v, rsW, l16, wt + T_1 + w * IMG + f * 1024); else if (r < 16) stg(v, rsW, (hh * 16 + r) * 16, wt + T_1 + 3 * IMG + f * 512);
            sfor<4>([&](auto k) { c1 = dot2(v[k.value], 0x3F803F80u, c1); });
        }
        for (int i = 0; i < n2; ++i) { const unsigned a = stg0 + (4 + i) * 1024 + trl + 256 * hh; stg(trfrag(a, a + 512), rsW, l16, wt + T_2 + (14 * g + 3 * w + i) * 1024); }
        cq = half_sum(cq); c1 = half_sum(c1);
        if (hh == 0) {
            if (w < 3) __builtin_amdgcn_raw_buffer_store_b32(f2u(cq), rsW, r * 4, wt + T_CQ + (g * 3 + w) * 128, 16);
            __builtin_amdgcn_raw_buffer_store_b32(f2u(c1), rsW, r * 4, wt + T_C1 + (g * 4 + w) * 128, 16);
        }
        STAMP(20);
        BAR_VM();
        if (w == 0) {
            if (lane == 0) { flag_up(10, false); flag_up(11, true); }
            STAMP(21);
            poll_at((lane & 16) ? ((11 * 16 + j) * 16 + (lane & 15)) * 256 : ((10 * 16 + g) * 16 + (lane & 15)) * 256);
        }
        BAR();
    }

#pragma unroll 1
    for (; layer < 2; ++layer) {
        const unsigned LB = WS_DATA + layer * L_STRIDE;
        FRESH();
        STAMP(0);
        {
            u32x4 xf[28]; u32x2 st[8];
            const unsigned tb = (layer == 0 ? WS_X0 : WS_DATA + L_X2) + (4 * g + w) * 32768;
            auto xload = [&](int f) { xf[f] = ldg(rsW, l16 + (f & 3) * 1024, tb + (f >> 2) * 4096); };
            if (layer == 0) stage(LDS_R1, T_Q, 0, 13, 0, 4);
            sfor<8>([&](auto f) { xload(f.value); });
            if (layer == 0) bar_vm<8>();
            SB();
            STAMP(1);
            unsigned wa = LDS_R1 + l16, wb = wa + 16 * 3072; pin_v(wa); pin_v(wb);
            auto wfrag = [&](int f, int i) { return f < 16 ? lds_frag(wa + (3 * f + i) * 1024) : lds_frag(wb + (3 * (f - 16) + i) * 1024); };
            f32x16 aq, ak, av; u32x4 wf[3][3];
            sfor<2>([&](auto f) { sfor<3>([&](auto i) { wf[f.value][i.value] = wfrag(f.value, i.value); }); });
            sfor<28>([&](auto ff) {
                constexpr int f = ff.value;
                if constexpr (f == 15) BAR();
                if constexpr (f + 2 < 28) sfor<3>([&](auto i) { wf[(f + 2) % 3][i.value] = wfrag(f + 2, i.value); });
                if constexpr (f == 0) { if (layer == 0) stage(LDS_R1, T_Q, 13, 21, 0, 4); }
                if constexpr (f % 2 == 0 && f < 10) sfor<4>([&](auto i) { xload(8 + 2 * f + i.value); });
                if constexpr (f == 10) sfor<8>([&](auto i) { st[i.value] = __builtin_amdgcn_raw_buffer_load_b64(rsW, (8 * hh + i.value) * 256 + r * 8, tb + 28672, 16); });
                SB();
                if constexpr (f == 0) { mfma0(aq, wf[0][0], xf[0]); mfma0(ak, wf[0][1], xf[0]); mfma0(av, xf[0], wf[0][2]); }
                else { mfma(aq, wf[f % 3][0], xf[f]); mfma(ak, wf[f % 3][1], xf[f]); mfma(av, xf[f], wf[f % 3][2]); }
                SB();
            });
            if (layer == 0) {
                const unsigned wt = WS_WT + j * T_STRIDE; float cs[8], cq[8];
                sfor<8>([&](auto i) { cs[i.value] = u2f(__builtin_amdgcn_raw_buffer_load_b32(rsW, (8 * hh + i.value) * 512 + r * 4, wt + T_C1 + w * 128, 16));
                                      cq[i.value] = u2f(__builtin_amdgcn_raw_buffer_load_b32(rsW, (8 * hh + i.value) * 384 + r * 4, wt + T_CQ + (w == 3 ? 0 : w) * 128, 16)); });
                const unsigned bias = b1[112 * j + (w == 3 ? 96 + (r & 15) : 32 * w + r)];
                const u32x4 g1 = make_waug(half_sum(((cs[0] + cs[1]) + (cs[2] + cs[3])) + ((cs[4] + cs[5]) + (cs[6] + cs[7]))), bias);
                const u32x4 gq = make_waug(half_sum(((cq[0] + cq[1]) + (cq[2] + cq[3])) + ((cq[4] + cq[5]) + (cq[6] + cq[7]))), 0u);
                if (hh == 0) { *(LAS u32x4*)(size_t)(LDS_AUG + w * 512 + r * 16) = g1; if (w < 3) *(LAS u32x4*)(size_t)(LDS_AUG + 2048 + w * 512 + r * 16) = gq; }
                BAR();
            }
            const unsigned ga = LDS_AUG + 2048 + r * 16;
            const u32x4 gq = lds_frag(ga), gk = lds_frag(ga + 512), gv = lds_frag(ga + 1024);
            float rstd; const u32x4 xa = make_xaug(st, rstd);
            const unsigned rb = LDS_HALF + w * 128;
            if (hh == 0) *(LAS float*)(size_t)(rb + r * 4) = rstd;
            mfma_n(aq, gq, xa); mfma(ak, gk, xa); mfma(av, xa, gv);
            f32x4 rs4[4];
            sfor<4>([&](auto i) { rs4[i.value] = lds_ld16(rb + (8 * i.value + 4 * hh) * 4); });
            SETTLE3(aq, ak, av);
            const unsigned T = 4 * g + w, kvb = LB + L_KV + (j * 64 + T) * 4096;
            stg(pack8(ak, 0, rstd), rsW, l16, kvb); stg(pack8(ak, 1, rstd), rsW, l16 + 1024, kvb);
            sfor<16>([&](auto i) { av[i.value] *= rs4[i.value >> 2][i.value & 3]; });
            u32x4 v0 = pack8(av, 0, 1.f), v1 = pack8(av, 1, 1.f);
            if (r == 28) { v0 = (u32x4){0x3F803F80u, 0x3F803F80u, 0x3F803F80u, 0x3F803F80u}; v1 = v0; }
            stg(v0, rsW, l16 + 2048, kvb); stg(v1, rsW, l16 + 3072, kvb);
            const float qs = rstd * (0.18898223650461361f * 1.4426950408889634f);
            u32x4 q0 = pack8(aq, 0, qs), q1 = pack8(aq, 1, qs);
            if (hh) { q1[2] = 0u; q1[3] = 0u; }
            stg(q0, rsW, l16, LB + L_Q + (j * 64 + T) * 2048); stg(q1, rsW, l16 + 1024, LB + L_Q + (j * 64 + T) * 2048);
        }
        STAMP(2);
        publish(layer * 5 + 0, true);
        STAMP(3);
        FRESH();
        if (w == 0) poll(layer * 5 + 0);
        else if (layer == 0) stage(LDS_WO, T_O, 0, 14, 1, 3);
        BAR();
        STAMP(4);
        FRESH();
        {
            unsigned tsv[8]; TS(0);
            u32x4 kv[4][4]; f32x16 O[4], S[2]; u32x4 p0, p1;
            sfor<16>([&](auto i) { def_v(kv[i.value >> 2][i.value & 3]); }); def_v(S[0]); def_v(S[1]); def_v(p0);
            unsigned kvs = LB + L_KV + (g * 64 + 16 * w) * 4096;
            const unsigned qb = LB + L_Q + (g * 64 + 4 * j) * 2048;
            acc_load2<224, 0>(l16, rsW, qb + w * 2048); kv_load2<0>(kv[0][0], kv[0][1], l16, rsW, kvs);
            acc_load2<232, 0>(l16, rsW, qb + (w ^ 1) * 2048); kv_load2<2048>(kv[0][2], kv[0][3], l16, rsW, kvs);
            acc_load2<240, 0>(l16, rsW, qb + (w ^ 2) * 2048); acc_load2<248, 0>(l16, rsW, qb + (w ^ 3) * 2048);
            kv_load(kv[1], l16, rsW, kvs + 4096);
            { float zero = 0.f; pin_v(zero);
              sfor<4>([&](auto c) { sfor<16>([&](auto i) { O[c.value][i.value] = zero; }); pin_v(O[c.value]); });
              p1 = (u32x4){f2u(zero), f2u(zero), f2u(zero), f2u(zero)}; kv[3][3] = p1; pin_v(p1); pin_v(kv[3][3]); }
            unsigned w2a = l16 + u * 1024, w2b = l16 + (u ^ 1) * 1024; const unsigned w2s = WS_WT + j * T_STRIDE + T_2 + w * 57344;
            kv_wait2<12>(kv[0][0], kv[0][1]);
            TS(1);
            mfmaz_qb<224>(S[0], kv[0][0]); mfma_qb<228>(S[0], kv[0][1]);
            kv_wait2<4>(kv[0][2], kv[0][3]);
            SETTLE1(S[0]);
#pragma unroll 1
            for (int it = 0; it < 4; ++it) {
                if (layer == 0) {
                    auto w2l = [&](auto q) { sfor<7>([&](auto k) { constexpr int kk = 7 * q.value + k.value;
                        acc_load1<8 * kk, (kk & 1) * 2048>(w2a, rsW, w2s + (kk >> 1) * 4096); acc_load1<8 * kk + 4, (kk & 1) * 2048>(w2b, rsW, w2s + (kk >> 1) * 4096); }); };
                    if (it == 0) w2l(std::integral_constant<int, 0>{}); else if (it == 1) w2l(std::integral_constant<int, 1>{});
                    else if (it == 2) w2l(std::integral_constant<int, 2>{}); else w2l(std::integral_constant<int, 3>{});
                }
                SB();
                sfor<16>([&](auto ii) {
                    constexpr int i = ii.value, tau = i >> 2, c = i & 3, nx = (i + 1) & 15, ntau = nx >> 2, nc = nx & 3, pv = (i + 15) & 15;
                    f32x16& Sc = S[i & 1]; f32x16& Sn = S[nx & 1];
                    if constexpr (c == 3) kv_wait<(tau & 1) ? 4 : 8>(kv[ntau]);
                    auto ex = [&](int k, u32x4& p) {
                        const float e0 = __builtin_amdgcn_exp2f(Sc[4 * k]), e1 = __builtin_amdgcn_exp2f(Sc[4 * k + 1]), e2 = __builtin_amdgcn_exp2f(Sc[4 * k + 2]), e3 = __builtin_amdgcn_exp2f(Sc[4 * k + 3]);
                        p[2 * (k & 1)] = cvtpk(e0, e1); p[2 * (k & 1) + 1] = cvtpk(e2, e3);
                        pin_v(p);
                    };
                    mfmaz_qb<224 + 8 * nc>(Sn, kv[ntau][0]);
                    ex(0, p0); SB();
                    mfma_qb<228 + 8 * nc>(Sn, kv[ntau][1]);
                    ex(1, p0); SB();
                    mfma(O[pv & 3], kv[pv >> 2][3], p1);
                    if constexpr (i == 0 || i == 8) {
                        const unsigned so = (i == 8 && it == 3) ? WS_BYTES : kvs + (tau + 2) * 4096;
                        SB(); kv_load(kv[(tau + 2) & 3], l16, rsW, so); kv_load(kv[(tau + 3) & 3], l16, rsW, so + 4096); SB();
                    }
                    ex(2, p1); SB();
                    mfma(O[c], kv[tau][2], p0);
                    ex(3, p1); SB();
                });
                kvs += 16384;
            }
            mfma_n(O[3], kv[3][3], p1);
            kv_wait<0>(kv[0]); kv_wait<0>(kv[1]);
            SETTLE4(O[0], O[1], O[2], O[3]);
            TS(5); TSFLUSH(8, 0);
            STAMP(5);
            FRESH();
            f32x16 o = O[0];
            reduce4(o, O[1], O[2], O[3]);
            float l = upper_half(o[12]);
            float amax = 0.f;
            sfor<16>([&](auto i) { amax = __builtin_fmaxf(amax, (i.value >= 12 && hh) ? 0.f : __builtin_fabsf(o[i.value])); });
            const bool ok = l >= 5.4e-20f && l <= 1.8e19f && amax <= 3.0e38f;
            if (!__all(ok)) {
                float mrun = -INFINITY;
                float zero = 0.f; pin_v(zero);
                sfor<16>([&](auto i) { o[i.value] = zero; });
                const u32x4 qa = ldg(rsW, l16, LB + L_Q + (g * 64 + 4 * j + w) * 2048), qb2 = ldg(rsW, l16 + 1024, LB + L_Q + (g * 64 + 4 * j + w) * 2048);
#pragma unroll 1
                for (int T = 0; T < 64; ++T) {
                    const unsigned so = LB + L_KV + (g * 64 + T) * 4096;
                    const u32x4 k0 = ldg(rsW, l16, so), k1 = ldg(rsW, l16 + 1024, so), v0 = ldg(rsW, l16 + 2048, so), v1 = ldg(rsW, l16 + 3072, so);
                    f32x16 s;
                    mfma0_safe(s, k0, qa); mfma_safe(s, k1, qb2);
                    float mt = s[0]; sfor<15>([&](auto i) { mt = __builtin_fmaxf(mt, s[i.value + 1]); });
                    const float mn = __builtin_fmaxf(mrun, half_max(mt)), al = __builtin_amdgcn_exp2f(mrun - mn);
                    mrun = mn;
                    sfor<16>([&](auto i) { o[i.value] *= al; s[i.value] = __builtin_amdgcn_exp2f(s[i.value] - mn); });
                    mfma_safe(o, v0, pack8(s, 0, 1.f)); mfma_safe(o, v1, pack8(s, 1, 1.f));
                }
                l = upper_half(o[12]);
            }
            const float rl = __builtin_amdgcn_rcpf(l);
            u32x4 o0 = pack8(o, 0, rl), o1 = pack8(o, 1, rl);
            if (hh) { o1[2] = 0u; o1[3] = 0u; }
            const unsigned ob = LB + L_O + (4 * j + w) * 32768 + g * 2048;
            stg(o0, rsW, l16, ob); stg(o1, rsW, l16 + 1024, ob);
        }
        STAMP(6);
        publish(layer * 5 + 1, true);
        STAMP(7);
        FRESH();
        if (w == 0) poll(layer * 5 + 1);
        else stage(LDS_R1, T_1, 0, 14, 1, 3);
        BAR();
        STAMP(8);
        {
            u32x4 of[16];
            const unsigned ob = LB + L_O + (4 * g + 2 * m + t) * 32768 + u * 16384;
            auto oload = [&](int s) { of[s] = ldg(rsW, l16 + (s & 3) * 1024, ob + (s >> 2) * 4096); };
            sfor<12>([&](auto s) { oload(s.value); });
            SB();
            const unsigned tA = LDS_WO + trl + 256 * hh + u * (8 * 28 * 64); unsigned tO = tA + u * IMG, tS = tA + (u ^ 1) * IMG;
            pin_v(tO); pin_v(tS);
            auto wfrag = [&](int img, int s) { const unsigned o = (img ? tS : tO) + (28 * (s >> 1) + 16 * (s & 1)) * 64; return trfrag(o, o + 512); };
            f32x16 a0, a1; u32x4 wf[3][2];
            sfor<2>([&](auto s) { wf[s.value][0] = wfrag(0, s.value); wf[s.value][1] = wfrag(1, s.value); });
            sfor<16>([&](auto ss) {
                constexpr int s = ss.value;
                if constexpr (s + 2 < 16) { wf[(s + 2) % 3][0] = wfrag(0, s + 2); wf[(s + 2) % 3][1] = wfrag(1, s + 2); }
                if constexpr (s == 1) sfor<4>([&](auto i) { oload(12 + i.value); });
                SB();
                if constexpr (s == 0) { mfma0(a1, wf[0][1], of[0]); mfma0(a0, wf[0][0], of[0]); } else { mfma(a1, wf[s % 3][1], of[s]); mfma(a0, wf[s % 3][0], of[s]); }
                SB();
            });
            SETTLE2(a0, a1);
            sfor<4>([&](auto k) { lds_st16(LDS_SCR16 + w * 4096 + k.value * 1024 + l16, (f32x4){a1[4 * k.value], a1[4 * k.value + 1], a1[4 * k.value + 2], a1[4 * k.value + 3]}); });
            sfor<16>([&](auto i) { resid[i.value] += a0[i.value]; });
            BAR();
            sfor<4>([&](auto k) { f32x4 v = lds_ld16(LDS_SCR16 + (w ^ 1) * 4096 + k.value * 1024 + l16); sfor<4>([&](auto e) { resid[4 * k.value + e.value] += v[e.value]; }); });
            publish_x(LB + L_X1);
        }
        STAMP(9);
        publish(layer * 5 + 2, false);
        STAMP(10);
        FRESH();
        if (w == 0) poll(layer * 5 + 2);
        else stage(LDS_R1, T_1, 14, 25, 1, 3);
        BAR();
        STAMP(11);
        {
            u32x4 xf[28]; u32x2 st[8];
            const unsigned tb = LB + L_X1 + (4 * g + w) * 32768;
            auto xload = [&](int f) { xf[f] = ldg(rsW, l16 + (f & 3) * 1024, tb + (f >> 2) * 4096); };
            sfor<12>([&](auto f) { xload(f.value); });
            SB();
            unsigned wA = LDS_R1 + l16, wC = wA + 2 * IMG, wH = LDS_HALF + (hh * 16 + (r & 15)) * 16, wG = LDS_AUG + r * 16;
            pin_v(wA); pin_v(wC); pin_v(wH); pin_v(wG);
            auto wfrag = [&](int img, int f) { return f == 28 ? lds_frag(wG + img * 512) : img == 3 ? lds_frag(wH + f * 512) : img == 2 ? lds_frag(wC + f * 1024) : lds_frag(wA + img * IMG + f * 1024); };
            f32x16 h0, h1, h2, h3; u32x4 wf[3][2], wp[5];
            sfor<2>([&](auto f) { wf[f.value][0] = wfrag(0, f.value); wf[f.value][1] = wfrag(1, f.value); });
            sfor<28>([&](auto ff) {
                constexpr int f = ff.value;
                if constexpr (f + 2 <= 28) { wf[(f + 2) % 3][0] = wfrag(0, f + 2); wf[(f + 2) % 3][1] = wfrag(1, f + 2); }
                if constexpr (f % 4 == 1 && f < 16) sfor<4>([&](auto i) { xload(11 + f + i.value); });
                if constexpr (f == 17) sfor<8>([&](auto i) { st[i.value] = __builtin_amdgcn_raw_buffer_load_b64(rsW, (8 * hh + i.value) * 256 + r * 8, tb + 28672, 16); });
                SB();
                if constexpr (f == 0) { mfma0(h0, wf[0][0], xf[0]); mfma0(h1, wf[0][1], xf[0]); } else { mfma(h0, wf[f % 3][0], xf[f]); mfma(h1, wf[f % 3][1], xf[f]); }
                SB();
            });
            BAR();
            sfor<4>([&](auto f) { wp[f.value] = wfrag(2, f.value); });
            float rstd; u32x4 xa = make_xaug(st, rstd);
            float c1 = rstd * 1.4426950408889634f; pin_v(xa); pin_v(c1);
            mfma_n(h0, wf[1][0], xa); mfma(h1, wf[1][1], xa);
            const unsigned ab = LB + L_AF + (4 * g + w) * 114688 + j * 7168;
            u32x4 pk[4]; float ta, tb2, ea, eb, da, db;
            auto chunk = [&](const f32x16& src, int pr, int ph, u32x4& dst) {
                if (ph == 0) { ta = src[2 * pr] * c1; tb2 = src[2 * pr + 1] * c1; pin_v(ta); pin_v(tb2); ea = __builtin_amdgcn_exp2f(ta); pin_v(ea); }
                if (ph == 1) { eb = __builtin_amdgcn_exp2f(tb2); da = __builtin_fmaf(ea, -0.5f, -1.f); pin_v(da); db = __builtin_fmaf(eb, -0.5f, -1.f); pin_v(db); }
                if (ph == 2) { da = __builtin_fmaf(ea, da, -1.f); pin_v(da); db = __builtin_fmaf(eb, db, -1.f); pin_v(db); da = __builtin_amdgcn_rcpf(da); pin_v(da); }
                if (ph == 3) { if ((pr & 3) == 0) def_v(dst); db = __builtin_amdgcn_rcpf(db); ta = __builtin_fmaf(ta, da, ta); pin_v(ta); tb2 = __builtin_fmaf(tb2, db, tb2); pin_v(tb2); dst[pr & 3] = cvtpk(ta, tb2); pin_v(dst); }
            };
            auto mish_pk = [&](float a, float b2) {
                const f32x2 tt = (f32x2){a, b2} * c1, e = {__builtin_amdgcn_exp2f(tt[0]), __builtin_amdgcn_exp2f(tt[1])};
                const f32x2 d = __builtin_elementwise_fma(e, __builtin_elementwise_fma(e, (f32x2){-0.5f, -0.5f}, (f32x2){-1.f, -1.f}), (f32x2){-1.f, -1.f});
                const f32x2 y = __builtin_elementwise_fma(tt, (f32x2){__builtin_amdgcn_rcpf(d[0]), __builtin_amdgcn_rcpf(d[1])}, tt);
                return cvtpk(y[0], y[1]);
            };
            sfor<58>([&](auto qq) {
                constexpr int q = qq.value, img = 2 + q / 29, f = q % 29, nq = q + 4;
                if constexpr (nq < 58) wp[nq % 5] = wfrag(2 + nq / 29, nq % 29);
                SB();
                f32x16& acc = img == 2 ? h2 : h3;
                if constexpr (f == 0) mfma0(acc, wp[q % 5], xf[0]); else if constexpr (f < 28) mfma(acc, wp[q % 5], xf[f]); else mfma(acc, wp[q % 5], xa);
                if constexpr (q >= 2) {
                    constexpr int pr = (q - 2) / 4, ph = (q - 2) % 4;
                    chunk(pr < 8 ? h0 : h1, pr & 7, ph, pk[pr >> 2]);
                    if constexpr (ph == 3 && (pr & 3) == 3) stg(pk[pr >> 2], rsW, l16, ab + (pr >> 2) * 1024);
                }
                SB();
            });
            pk[3][2] = mish_pk(h1[12], h1[13]); pk[3][3] = mish_pk(h1[14], h1[15]);
            stg(pk[3], rsW, l16, ab + 3 * 1024);
            sfor<2>([&](auto s) { sfor<4>([&](auto k) { pk[s.value][k.value] = mish_pk(h2[8 * s.value + 2 * k.value], h2[8 * s.value + 2 * k.value + 1]); }); stg(pk[s.value], rsW, l16, ab + (4 + s.value) * 1024); });
            SETTLE1(h3);
            sfor<4>([&](auto k) { pk[2][k.value] = mish_pk(h3[2 * k.value], h3[2 * k.value + 1]); });
            stg(pk[2], rsW, l16, ab + 6 * 1024);
        }
        STAMP(12);
        publish(layer * 5 + 3, false);
        STAMP(13);
        FRESH();
        if (w == 0) poll(layer * 5 + 3);
        else if (layer == 0) stage(LDS_R1, T_Q, 0, 13, 1, 3);
        BAR();
        STAMP(14);
        {
            u32x4 af[28][2];
            const unsigned ab = LB + L_AF + (4 * g + 2 * m + t) * 114688 + w * 28672, ab2 = LB + L_AF + (4 * g + 2 * m + (t ^ 1)) * 114688 + w * 28672;
            auto aload = [&](int k) { af[k][0] = ldg(rsW, l16 + (k & 3) * 1024, ab + (k >> 2) * 4096); af[k][1] = ldg(rsW, l16 + (k & 3) * 1024, ab2 + (k >> 2) * 4096); };
            unsigned tsv[8]; TS(0);
            sfor<6>([&](auto k) { aload(k.value); });
            SB(); TS(1);
            f32x16 y[4];
            sfor<28>([&](auto kk) {
                constexpr int k = kk.value;
                if constexpr (k % 2 == 0 && k + 6 < 28) { aload(k + 6); aload(k + 7); }
                SB();
                if constexpr (k == 0) { mfma0_wa<0>(y[0], af[0][0]); mfma0_wa<4>(y[1], af[0][0]); mfma0_wa<0>(y[2], af[0][1]); mfma0_wa<4>(y[3], af[0][1]); }
                else { mfma_wa<8 * k>(y[0], af[k][0]); mfma_wa<8 * k + 4>(y[1], af[k][0]); mfma_wa<8 * k>(y[2], af[k][1]); mfma_wa<8 * k + 4>(y[3], af[k][1]); }
                SB();
                if constexpr (k == 0) TS(2); if constexpr (k == 6) TS(3); if constexpr (k == 13) TS(4); if constexpr (k == 20) TS(5); if constexpr (k == 27) TS(6);
            });
            SETTLE4(y[0], y[1], y[2], y[3]);
            TS(7); TSFLUSH(8, 8);
            STAMP(15);
            FRESH();
            f32x16 o = y[0];
            reduce4(o, y[1], y[2], y[3]);
            sfor<16>([&](auto i) { resid[i.value] = __builtin_fmaf(o[i.value], 0.6931471805599453f, resid[i.value]); });
        }
        if (layer == 0) {
            publish_x(LB + L_X2);
            STAMP(16);
            publish(4, false);
            STAMP(17);
            if (w == 0) poll(4);
            else stage(LDS_R1, T_Q, 13, 21, 1, 3);
            BAR();
        } else {
            const u32x4 p0 = pack8(resid, 0, 1.f), p1 = pack8(resid, 1, 1.f);
            GAS char* op = (GAS char*)out + ((4 * g + 2 * m + t) * 32 + r) * 896 + 112 * n + 64 * u + 16 * hh;
            auto a = __builtin_amdgcn_permlane32_swap(p0[0], p0[2], false, false), c = __builtin_amdgcn_permlane32_swap(p0[1], p0[3], false, false);
            *(GAS u32x4*)op = (u32x4){a[0], c[0], a[1], c[1]};
            a = __builtin_amdgcn_permlane32_swap(p1[0], p1[2], false, false); c = __builtin_amdgcn_permlane32_swap(p1[1], p1[3], false, false);
            if (u == 0 || hh == 0) *(GAS u32x4*)(op + 32) = (u32x4){a[0], c[0], a[1], c[1]};
            if (b == 0 && threadIdx.x == 0) __hip_atomic_store((GAS u64*)ws, tag, RLX_AGENT);
            STAMP(16);
        }
    }
}

extern "C" void kernel_launch(void* const* d_in, const int* in_sizes, int n_in, void* d_out, int out_size, void* d_ws, size_t ws_size, hipStream_t stream) {
    (void)hipFuncSetAttribute((const void*)mega, hipFuncAttributeMaxDynamicSharedMemorySize, LDS_BYTES);
    mega<<<256, 256, LDS_BYTES, stream>>>((const u16*)d_in[0], (const u16*)d_in[1], (const u16*)d_in[2], (const u16*)d_in[4], (const u16*)d_in[5], (const u16*)d_in[6],
                                          (u16*)d_out, (char*)d_ws);
}
```

```cpp
#include <hip/hip_runtime.h>
#include <utility>

#define DI __device__ __forceinline__
#define LAS __attribute__((address_space(3)))
#define GAS __attribute__((address_space(1)))
typedef float f32x16 __attribute__((ext_vector_type(16)));
typedef float f32x4 __attribute__((ext_vector_type(4)));
typedef float f32x2 __attribute__((ext_vector_type(2)));
typedef unsigned u32x4 __attribute__((ext_vector_type(4)));
typedef unsigned u32x2 __attribute__((ext_vector_type(2)));
typedef short v4i16 __attribute__((ext_vector_type(4)));
typedef __bf16 bf16x2 __attribute__((ext_vector_type(2)));
typedef __amdgpu_buffer_rsrc_t rsrc_t;
typedef unsigned long long u64;
typedef unsigned short u16;

constexpr unsigned WS_FLAGS = 4096;
constexpr unsigned WS_STAMP = 200u << 20;
constexpr unsigned WS_X0 = 1u << 20;
constexpr unsigned WS_WT = 4u << 20;
constexpr unsigned T_Q = 0, T_1 = 86016, T_O = T_1 + 100352, T_2 = T_O + 57344, T_CQ = T_2 + 229376, T_C1 = T_CQ + 6144, T_STRIDE = 491520;
constexpr unsigned WS_DATA = 12u << 20;
#define PADX 0
#define PADKV 0
#define PADQ 0
#define PADO 0
#define PADA 0
constexpr unsigned XT = 32768 + PADX;
constexpr unsigned KVQ = 65536 + PADKV;
constexpr unsigned QG = 8192 + PADQ;
constexpr unsigned OH = 16384 + PADO;
constexpr unsigned AQ = 28672 + PADA;
constexpr unsigned L_Q = 0, L_KV = 3u << 20, L_O = 8u << 20, L_X1 = 11u << 20, L_AF = 14u << 20, L_X2 = 22u << 20, L_STRIDE = 25u << 20;
constexpr unsigned WS_BYTES = WS_DATA + 2 * L_STRIDE;
constexpr unsigned IMG = 28672;
constexpr unsigned LDS_WO = 0;
constexpr unsigned LDS_R1 = 57600;
constexpr unsigned LDS_HALF = LDS_R1 + 3 * IMG;
constexpr unsigned LDS_AUG = 160256;
constexpr unsigned LDS_SCR48 = LDS_AUG - 49152;
constexpr unsigned LDS_SCR16 = LDS_AUG - 16384;
constexpr unsigned LDS_BYTES = 163840;
#define CODE_BYTES 23000

#define SB() __builtin_amdgcn_sched_barrier(0)
#define BAR() asm volatile("s_waitcnt lgkmcnt(0)\n\ts_barrier" ::: "memory")
#define BAR_VM() do { __builtin_amdgcn_s_waitcnt(0x0070); asm volatile("s_waitcnt vmcnt(0) lgkmcnt(0)\n\ts_barrier" ::: "memory"); } while (0)
#define WAIT_VM0() asm volatile("s_waitcnt vmcnt(0)" ::: "memory")
#define RLX_AGENT __ATOMIC_RELAXED, __HIP_MEMORY_SCOPE_AGENT

template <int N, class F> DI void sfor_impl(F&& f, std::integral_constant<int, N>) {}
template <class F, int... Is> DI void sfor_seq(F&& f, std::integer_sequence<int, Is...>) { (f(std::integral_constant<int, Is>{}), ...); }
template <int N, class F> DI void sfor(F&& f) { sfor_seq(f, std::make_integer_sequence<int, N>{}); }

DI void mfma(f32x16& c, const u32x4& a, const u32x4& b) { asm volatile("v_mfma_f32_32x32x16_bf16 %0, %1, %2, %0" : "+v"(c) : "v"(a), "v"(b)); }
DI void mfma0(f32x16& c, const u32x4& a, const u32x4& b) { asm volatile("v_mfma_f32_32x32x16_bf16 %0, %1, %2, 0" : "=&v"(c) : "v"(a), "v"(b)); }
template <int R> DI void mfma_qb(f32x16& c, const u32x4& a) { asm volatile("v_mfma_f32_32x32x16_bf16 %0, %1, a[%c2:%c3], %0" : "+v"(c) : "v"(a), "i"(R), "i"(R + 3)); }
template <int R> DI void mfma0_qb(f32x16& c, const u32x4& a) { asm volatile("v_mfma_f32_32x32x16_bf16 %0, %1, a[%c2:%c3], 0" : "=&v"(c) : "v"(a), "i"(R), "i"(R + 3)); }
template <int R> DI void mfma_wa(f32x16& c, const u32x4& b) { asm volatile("v_mfma_f32_32x32x16_bf16 %0, a[%c2:%c3], %1, %0" : "+v"(c) : "v"(b), "i"(R), "i"(R + 3)); }
template <int R> DI void mfma0_wa(f32x16& c, const u32x4& b) { asm volatile("v_mfma_f32_32x32x16_bf16 %0, a[%c2:%c3], %1, 0" : "=&v"(c) : "v"(b), "i"(R), "i"(R + 3)); }
template <int R> DI void mfmaz_qb(f32x16& c, const u32x4& a) { asm volatile("v_mfma_f32_32x32x16_bf16 %0, %1, a[%c2:%c3], 0" : "+v"(c) : "v"(a), "i"(R), "i"(R + 3)); }
template <int R, int OFF> DI void acc_load2(unsigned voff, __amdgpu_buffer_rsrc_t rs, unsigned soff) {
    asm volatile("buffer_load_dwordx4 a[%c3:%c4], %0, %1, %2 offen offset:%c7 sc1\n\tbuffer_load_dwordx4 a[%c5:%c6], %0, %1, %2 offen offset:%c8 sc1"
                 :: "v"(voff), "s"(rs), "s"(soff), "i"(R), "i"(R + 3), "i"(R + 4), "i"(R + 7), "i"(OFF), "i"(OFF + 1024) : "memory", "a255"); }
template <int R, int OFF> DI void acc_load1(unsigned voff, __amdgpu_buffer_rsrc_t rs, unsigned soff) {
    asm volatile("buffer_load_dwordx4 a[%c3:%c4], %0, %1, %2 offen offset:%c5 sc1" :: "v"(voff), "s"(rs), "s"(soff), "i"(R), "i"(R + 3), "i"(OFF) : "memory"); }
DI void mfma_n(f32x16& c, const u32x4& a, const u32x4& b) { asm volatile("s_nop 1\n\tv_mfma_f32_32x32x16_bf16 %0, %1, %2, %0" : "+v"(c) : "v"(a), "v"(b)); }
DI void mfma0_safe(f32x16& c, const u32x4& a, const u32x4& b) { asm volatile("s_nop 1\n\tv_mfma_f32_32x32x16_bf16 %0, %1, %2, 0\n\ts_nop 11" : "=&v"(c) : "v"(a), "v"(b)); }
DI void mfma_safe(f32x16& c, const u32x4& a, const u32x4& b) { asm volatile("s_nop 1\n\tv_mfma_f32_32x32x16_bf16 %0, %1, %2, %0\n\ts_nop 11" : "+v"(c) : "v"(a), "v"(b)); }
DI void SETTLE1(f32x16& a) { asm volatile("s_nop 11" : "+v"(a)); }
#define SETTLE2(a, b) asm volatile("s_nop 11" : "+v"(a), "+v"(b))
#define SETTLE3(a, b, c) asm volatile("s_nop 11" : "+v"(a), "+v"(b), "+v"(c))
#define SETTLE4(a, b, c, d) asm volatile("s_nop 11" : "+v"(a), "+v"(b), "+v"(c), "+v"(d))

DI unsigned cvtpk(float lo, float hi) { f32x2 v = {lo, hi}; bf16x2 b = __builtin_convertvector(v, bf16x2); return __builtin_bit_cast(unsigned, b); }
DI float bflo(unsigned u) { return __builtin_bit_cast(float, u << 16); }
DI float bfhi(unsigned u) { return __builtin_bit_cast(float, u & 0xffff0000u); }
DI float dot2(unsigned a, unsigned b, float c) { return __builtin_amdgcn_fdot2_f32_bf16(__builtin_bit_cast(bf16x2, a), __builtin_bit_cast(bf16x2, b), c, false); }
DI float u2f(unsigned u) { return __builtin_bit_cast(float, u); }
DI unsigned f2u(float f) { return __builtin_bit_cast(unsigned, f); }
DI float half_sum(float v) { auto r = __builtin_amdgcn_permlane32_swap(f2u(v), f2u(v), false, false); return u2f(r[0]) + u2f(r[1]); }
DI float half_max(float v) { auto r = __builtin_amdgcn_permlane32_swap(f2u(v), f2u(v), false, false); return __builtin_fmaxf(u2f(r[0]), u2f(r[1])); }
DI float upper_half(float v) { auto r = __builtin_amdgcn_permlane32_swap(f2u(v), f2u(v), false, false); return u2f(r[1]); }
#define LDAUX 16
DI u32x4 ldg(rsrc_t rs, unsigned voff, unsigned soff) { return __builtin_amdgcn_raw_buffer_load_b128(rs, voff, soff, LDAUX); }
DI void stg(u32x4 v, rsrc_t rs, unsigned voff, unsigned soff) { __builtin_amdgcn_raw_buffer_store_b128(v, rs, voff, soff, 16); asm volatile("s_nop 1" : "+v"(v) :: "memory"); }
DI void dma(unsigned lds, unsigned voff, rsrc_t rs, unsigned soff) {
    asm volatile("s_nop 4\n\ts_mov_b32 m0, %0\n\ts_nop 0\n\tbuffer_load_dwordx4 %1, %2, %3 offen lds"
                 :: "s"(lds), "v"(voff), "s"(rs), "s"(__builtin_amdgcn_readfirstlane(soff)) : "memory"); }
DI void dma4(unsigned lds, unsigned voff, rsrc_t rs, unsigned soff) {
    asm volatile("s_mov_b32 m0, %0\n\ts_nop 0\n\tbuffer_load_dwordx4 %1, %2, %3 offen sc1 lds\n\tbuffer_load_dwordx4 %1, %2, %3 offen offset:1024 sc1 lds\n\t"
                 "buffer_load_dwordx4 %1, %2, %3 offen offset:2048 sc1 lds\n\tbuffer_load_dwordx4 %1, %2, %3 offen offset:3072 sc1 lds" :: "s"(lds), "v"(voff), "s"(rs), "s"(soff) : "memory"); }
template <unsigned C> DI unsigned sma(unsigned a, unsigned b) { unsigned r; asm("s_mul_i32 %0, %1, %2\n\ts_add_i32 %0, %0, %3" : "=&s"(r) : "s"(a), "i"(C), "s"(b) : "scc"); return r; }
DI void pin_v(u32x4& x) { asm volatile("" : "+v"(x)); }
DI void pin_v(unsigned& x) { asm volatile("" : "+v"(x)); }
DI void pin_v(float& x) { asm volatile("" : "+v"(x)); }
template <class T> DI void def_v(T& x) { asm volatile("" : "=v"(x)); }
DI void pin_v(f32x16& x) { asm volatile("" : "+v"(x)); }
template <int O0> DI void kv_load2(u32x4& d0, u32x4& d1, unsigned voff, rsrc_t rs, unsigned soff) {
    asm volatile("buffer_load_dwordx4 %0, %2, %3, %4 offen offset:%c5 sc1\n\tbuffer_load_dwordx4 %1, %2, %3, %4 offen offset:%c6 sc1"
                 : "+v"(d0), "+v"(d1) : "v"(voff), "s"(rs), "s"(soff), "i"(O0), "i"(O0 + 1024) : "memory"); }
DI void kv_load(u32x4 (&d)[4], unsigned voff, rsrc_t rs, unsigned soff) {
    asm volatile("buffer_load_dwordx4 %0, %4, %5, %6 offen sc1\n\tbuffer_load_dwordx4 %1, %4, %5, %6 offen offset:1024 sc1\n\t"
                 "buffer_load_dwordx4 %2, %4, %5, %6 offen offset:2048 sc1\n\tbuffer_load_dwordx4 %3, %4, %5, %6 offen offset:3072 sc1"
                 : "+v"(d[0]), "+v"(d[1]), "+v"(d[2]), "+v"(d[3]) : "v"(voff), "s"(rs), "s"(soff) : "memory"); }
template <int N> DI void kv_wait(u32x4 (&d)[4]) {
    asm volatile("s_waitcnt vmcnt(%c4)\n\t; KVW %0 %1 %2 %3" : "+v"(d[0]), "+v"(d[1]), "+v"(d[2]), "+v"(d[3]) : "i"(N) : "memory"); }
template <int N> DI void kv_wait2(u32x4& a, u32x4& b) { asm volatile("s_waitcnt vmcnt(%c2)\n\t; KVW %0 %1" : "+v"(a), "+v"(b) : "i"(N) : "memory"); }
template <int NS, int NB> DI void kv_waitc(u32x4 (&d)[4], int cond) {
    asm volatile("s_cmp_lg_u32 %4, 0\n\ts_cbranch_scc1 1f\n\ts_waitcnt vmcnt(%c5)\n1:\n\ts_waitcnt vmcnt(%c6)\n\t; KVW %0 %1 %2 %3"
                 : "+v"(d[0]), "+v"(d[1]), "+v"(d[2]), "+v"(d[3]) : "s"(cond), "i"(NS), "i"(NB) : "memory", "scc"); }
template <int OFF> DI void ald(u32x4& d, unsigned voff, rsrc_t rs, unsigned soff) {
    asm volatile("buffer_load_dwordx4 %0, %1, %2, %3 offen offset:%c4 sc1" : "=v"(d) : "v"(voff), "s"(rs), "s"(soff), "i"(OFF) : "memory"); }
template <int OFF> DI void ald(u32x2& d, unsigned voff, rsrc_t rs, unsigned soff) {
    asm volatile("buffer_load_dwordx2 %0, %1, %2, %3 offen offset:%c4 sc1" : "=v"(d) : "v"(voff), "s"(rs), "s"(soff), "i"(OFF) : "memory"); }
template <int N, class T> DI void await(T& d) { asm volatile("s_waitcnt vmcnt(%c1)\n\t; KVW %0" : "+v"(d) : "i"(N) : "memory"); }
template <class T> DI void landed(T& d) { asm volatile("; KVW %0" : "+v"(d) :: "memory"); }
template <int N> DI void bar_vm() { asm volatile("s_waitcnt vmcnt(%c0) lgkmcnt(0)\n\ts_barrier" :: "i"(N) : "memory"); }
DI void mfma_x(f32x16& c, const u32x4& a, const u32x4& b, const u32x4& d1, const u32x4& d2) { asm volatile("v_mfma_f32_32x32x16_bf16 %0, %1, %2, %0" : "+v"(c) : "v"(a), "v"(b), "v"(d1), "v"(d2)); }
template <bool CVT> DI void mfma4_mish(f32x16& c2, f32x16& c3, const u32x4& w0, const u32x4& w1, const u32x4& w2, const u32x4& w3, const u32x4& b0, const u32x4& b1,
                                       float& t0, float& t1, float& t2, float& t3, float& e0, float& e1, float& d0, float& d1, float xa, float xb, float c1, unsigned& r) {
#define MISH4_HEAD "v_mfma_f32_32x32x16_bf16 %0, %11, %15, %0\n\tv_exp_f32 %6, %2\n\tv_fma_f32 %9, %7, %9, -1.0\n\tv_fmac_f32 %4, %4, %8\n\t" \
                   "v_mfma_f32_32x32x16_bf16 %1, %12, %15, %1\n\tv_rcp_f32 %9, %9\n\tv_fma_f32 %8, %6, -0.5, -1.0\n\tv_mul_f32 %3, %19, %17\n\t" \
                   "v_mfma_f32_32x32x16_bf16 %0, %13, %16, %0\n\tv_exp_f32 %7, %3\n\tv_fma_f32 %8, %6, %8, -1.0\n\tv_fmac_f32 %5, %5, %9\n\t" \
                   "v_mfma_f32_32x32x16_bf16 %1, %14, %16, %1\n\tv_rcp_f32 %8, %8\n\tv_fma_f32 %9, %7, -0.5, -1.0\n\t"
    if constexpr (CVT) asm volatile(MISH4_HEAD "v_cvt_pk_bf16_f32 %10, %4, %5\n\tv_mul_f32 %4, %19, %18"
        : "+v"(c2), "+v"(c3), "+v"(t0), "+v"(t1), "+v"(t2), "+v"(t3), "+v"(e0), "+v"(e1), "+v"(d0), "+v"(d1), "=&v"(r)
        : "v"(w0), "v"(w1), "v"(w2), "v"(w3), "v"(b0), "v"(b1), "v"(xa), "v"(xb), "v"(c1));
    else asm volatile(MISH4_HEAD "v_mul_f32 %4, %19, %18"
        : "+v"(c2), "+v"(c3), "+v"(t0), "+v"(t1), "+v"(t2), "+v"(t3), "+v"(e0), "+v"(e1), "+v"(d0), "+v"(d1), "=&v"(r)
        : "v"(w0), "v"(w1), "v"(w2), "v"(w3), "v"(b0), "v"(b1), "v"(xa), "v"(xb), "v"(c1));
}
template <int OFF> DI void ald(float& d, unsigned voff, rsrc_t rs, unsigned soff) {
    asm volatile("buffer_load_dword %0, %1, %2, %3 offen offset:%c4 sc1 ; CORR" : "+v"(d) : "v"(voff), "s"(rs), "s"(soff), "i"(OFF) : "memory"); }
DI void ald_u16(unsigned& d, const void* p) { asm volatile("global_load_ushort %0, %1, off" : "+v"(d) : "v"(p) : "memory"); }
DI float addp(float a, float b) { float r = a + b; asm volatile("" : "+v"(r)); return r; }
struct XAug { float s1, s2, mean, var, rstd, mh, sd; unsigned mhb, d0, d1, d2; };
template <int STEP> DI void xaug_step(XAug& a, const u32x2 (&st)[8], u32x4& xa, int hh) {
    if constexpr (STEP == 0) { a.s1 = addp(addp(u2f(st[0][0]), u2f(st[1][0])), addp(u2f(st[2][0]), u2f(st[3][0]))); a.mean = addp(u2f(st[4][0]), u2f(st[5][0])); a.s1 = addp(a.s1, a.mean); }
    if constexpr (STEP == 1) { a.s1 = addp(a.s1, addp(u2f(st[6][0]), u2f(st[7][0]))); a.s2 = addp(addp(u2f(st[0][1]), u2f(st[1][1])), addp(u2f(st[2][1]), u2f(st[3][1]))); }
    if constexpr (STEP == 2) { a.s2 = addp(addp(a.s2, addp(u2f(st[4][1]), u2f(st[5][1]))), addp(u2f(st[6][1]), u2f(st[7][1]))); }
    if constexpr (STEP == 3) { a.s1 = half_sum(a.s1); pin_v(a.s1); }
    if constexpr (STEP == 4) { a.s2 = half_sum(a.s2); pin_v(a.s2); }
    if constexpr (STEP == 5) { a.mean = a.s1 * (-1.f / 448.f); pin_v(a.mean); a.var = a.s2 * (1.f / 448.f); pin_v(a.var); a.var = __builtin_fmaxf(__builtin_fmaf(-a.mean, a.mean, a.var), 0.f) + 1e-5f; pin_v(a.var); }
    if constexpr (STEP == 6) { a.rstd = __builtin_amdgcn_rsqf(a.var); a.mhb = cvtpk(a.mean, 0.f); a.mh = bflo(a.mhb); a.sd = a.var * a.rstd; pin_v(a.rstd); pin_v(a.mhb); pin_v(a.mh); pin_v(a.sd); }
    if constexpr (STEP == 7) { a.d0 = cvtpk(a.mh, a.mean - a.mh); a.d1 = cvtpk(a.mh, a.sd); a.d2 = cvtpk(a.sd - bfhi(a.d1), 0.f); pin_v(a.d0); pin_v(a.d1); pin_v(a.d2); }
    if constexpr (STEP == 8) { xa = hh == 0 ? (u32x4){a.d0, a.d1, a.d2, 0u} : (u32x4){0u, 0u, 0u, 0u}; pin_v(xa); }
}
DI u32x4 lds_frag(unsigned a) { return *(LAS u32x4*)(size_t)a; }
DI u32x4 trfrag(unsigned a1, unsigned a2) {
    v4i16 lo = __builtin_amdgcn_ds_read_tr16_b64_v4i16((LAS v4i16*)(size_t)a1), hi = __builtin_amdgcn_ds_read_tr16_b64_v4i16((LAS v4i16*)(size_t)a2);
    u32x2 l = __builtin_bit_cast(u32x2, lo), h = __builtin_bit_cast(u32x2, hi);
    return (u32x4){l[0], l[1], h[0], h[1]}; }
DI void lds_st16(unsigned a, f32x4 v) { *(LAS f32x4*)(size_t)a = v; }
DI f32x4 lds_ld16(unsigned a) { return *(LAS f32x4*)(size_t)a; }
constexpr int xrow(int f) { return (f % 7 == 6) ? 112 * (f / 7) + 48 : 112 * (f / 7) + (f % 7 >= 3 ? 56 : 0) + 16 * ((f % 7) % 3); }
DI u32x4 pack8(const f32x16& v, int s, float k) {
    return (u32x4){cvtpk(v[8 * s] * k, v[8 * s + 1] * k), cvtpk(v[8 * s + 2] * k, v[8 * s + 3] * k), cvtpk(v[8 * s + 4] * k, v[8 * s + 5] * k), cvtpk(v[8 * s + 6] * k, v[8 * s + 7] * k)}; }
DI unsigned hi_lo(float v, float& hi) { unsigned h = cvtpk(v, 0.f); hi = bflo(h); return h & 0xffffu; }

#define TS(i) do {} while (0)
#define RT(i) do {} while (0)
#define TSFLUSH() do {} while (0)
#define SHARED_FLAGS
#define FFN1_ST 16
#define FFN1_XA 27
#define PRESLEEP 8

__global__ __launch_bounds__(256, 1) void mega(const u16* __restrict__ x, const u16* __restrict__ wqkv, const u16* __restrict__ wo, const u16* __restrict__ w1,
                                               const u16* __restrict__ b1, const u16* __restrict__ w2, u16* __restrict__ out, char* __restrict__ ws) {
    int lane = threadIdx.x & 63, w = __builtin_amdgcn_readfirstlane(threadIdx.x >> 6);
    const int b = blockIdx.x;
    int g = (b & 7) * 2 + (b >> 7), j = (b >> 3) & 15;
    int r, hh, m, n, t, u; unsigned trl, l16;
    int layer = 0;
#define FRESH() do { asm volatile("" : "+s"(g), "+s"(j), "+s"(w), "+s"(layer), "+v"(lane)); r = lane & 31; hh = lane >> 5; \
        asm volatile("s_lshr_b32 %0, %4, 3\n\ts_and_b32 %1, %4, 7\n\ts_lshr_b32 %2, %5, 1\n\ts_and_b32 %3, %5, 1" : "=&s"(m), "=&s"(n), "=&s"(t), "=&s"(u) : "s"(j), "s"(w) : "scc"); \
        trl = ((lane & 15) >> 2) * 64 + ((lane >> 4) & 1) * 32 + (lane & 3) * 8; l16 = lane * 16; } while (0)
    const rsrc_t rsW = __builtin_amdgcn_make_buffer_rsrc((void*)ws, 0, WS_BYTES, 0x00020000);
    u64 tag;
    bool dead = false;
    f32x16 resid;
    unsigned tsv = 0;

    auto publish = [&](int S, bool cross) {
        BAR_VM();
        if (w == 0 && lane == 0) __hip_atomic_store((GAS u64*)(ws + WS_FLAGS + ((S * 16 + (cross ? j : g)) * 16 + (cross ? g : j)) * 256), tag, RLX_AGENT);
    };
    auto poll_at = [&](unsigned off) {
        GAS u64* f = (GAS u64*)(ws + WS_FLAGS + off);
        if (PRESLEEP) __builtin_amdgcn_s_sleep(PRESLEEP);
        if (!dead) {
            for (int spin = 0;; ++spin) {
                u64 v = __hip_atomic_load(f, RLX_AGENT);
                if (__all(v == tag)) break;
                if (spin > 400000) { dead = true; break; }
                __builtin_amdgcn_s_sleep(1);
            }
        }
    };
    auto poll = [&](int S) { poll_at(((S * 16 + g) * 16 + (lane & 15)) * 256); };
    auto stage = [&](unsigned lds, unsigned src, int g0, int g1, int w0, int nw) {
        const unsigned sb = sma<T_STRIDE>(j, WS_WT + src);
        for (int q = g0 + w - w0; q < g1; q += nw) dma4(lds + q * 4096, l16, rsW, sb + q * 4096);
    };
    auto publish_x = [&](unsigned base) {
        const unsigned tb = base + (4 * g + 2 * m + t) * XT, f0 = 7 * (n >> 1) + 3 * (n & 1);
        u32x4 p0 = pack8(resid, 0, 1.f), p1 = pack8(resid, 1, 1.f);
        if (u == 0) { stg(p0, rsW, l16, tb + f0 * 1024); stg(p1, rsW, l16, tb + f0 * 1024 + 1024); }
        else {
            stg(p0, rsW, l16, tb + f0 * 1024 + 2048);
            auto a = __builtin_amdgcn_permlane32_swap(p1[0], p1[0], false, false), c = __builtin_amdgcn_permlane32_swap(p1[1], p1[1], false, false);
            if (hh == (n & 1)) stg((u32x4){a[0], c[0], a[1], c[1]}, rsW, l16, tb + (7 * (n >> 1) + 6) * 1024);
            p1[2] = 0u; p1[3] = 0u;
        }
        float s1 = 0.f, s2 = 0.f, s3 = 0.f, s4 = 0.f;
        sfor<4>([&](auto i) { s1 = dot2(p0[i.value], 0x3F803F80u, s1); s2 = dot2(p0[i.value], p0[i.value], s2); s3 = dot2(p1[i.value], 0x3F803F80u, s3); s4 = dot2(p1[i.value], p1[i.value], s4); });
        s1 = half_sum(s1 + s3); s2 = half_sum(s2 + s4);
        if (hh == 0) __builtin_amdgcn_raw_buffer_store_b64((u32x2){f2u(s1), f2u(s2)}, rsW, r * 8, tb + 28672 + (2 * n + u) * 256, 16);
    };
    auto reduce4 = [&](f32x16& o, const f32x16& v1, const f32x16& v2, const f32x16& v3) {
        unsigned sw = LDS_SCR48 + w * 12288 + l16; pin_v(sw);
        sfor<4>([&](auto k) { lds_st16(sw + k.value * 1024, (f32x4){v1[4 * k.value], v1[4 * k.value + 1], v1[4 * k.value + 2], v1[4 * k.value + 3]}); });
        sfor<4>([&](auto k) { lds_st16(sw + 4096 + k.value * 1024, (f32x4){v2[4 * k.value], v2[4 * k.value + 1], v2[4 * k.value + 2], v2[4 * k.value + 3]}); });
        sfor<4>([&](auto k) { lds_st16(sw + 8192 + k.value * 1024, (f32x4){v3[4 * k.value], v3[4 * k.value + 1], v3[4 * k.value + 2], v3[4 * k.value + 3]}); });
        BAR();
        sfor<3>([&](auto d) { unsigned a = LDS_SCR48 + (w ^ (d.value + 1)) * 12288 + d.value * 4096 + l16; pin_v(a);
            sfor<4>([&](auto k) { f32x4 v = lds_ld16(a + k.value * 1024); sfor<4>([&](auto e) { o[4 * k.value + e.value] += v[e.value]; }); }); });
    };
    auto make_waug = [&](float cs, unsigned bias16) {
        float ch; const unsigned chb = hi_lo(cs, ch);
        return (u32x4){chb | (chb << 16), (cvtpk(cs - ch, 0.f) & 0xffffu) | (bias16 << 16), bias16, 0u};
    };

    {
        unsigned cw = *(GAS unsigned*)((GAS char*)__builtin_amdgcn_s_getpc() + min((unsigned)((b >> 3) * 256 + threadIdx.x) * 4u, (unsigned)CODE_BYTES));
        SB();
        FRESH();
        TS(18); RT(62);
        const rsrc_t rsX = __builtin_amdgcn_make_buffer_rsrc((void*)x, 0, 2048 * 896, 0x00020000);
        const rsrc_t rsQKV = __builtin_amdgcn_make_buffer_rsrc((void*)wqkv, 0, 448 * 2688, 0x00020000);
        const rsrc_t rsWO = __builtin_amdgcn_make_buffer_rsrc((void*)wo, 0, 448 * 896, 0x00020000);
        const rsrc_t rsW1 = __builtin_amdgcn_make_buffer_rsrc((void*)w1, 0, 448 * 3584, 0x00020000);
        const rsrc_t rsW2 = __builtin_amdgcn_make_buffer_rsrc((void*)w2, 0, 1792 * 896, 0x00020000);
        const unsigned stg0 = LDS_R1 + w * 6144, wt = sma<T_STRIDE>(j, WS_WT), wn = sma<T_STRIDE>(n, WS_WT);
        u32x2 xv[4];
        const unsigned vx = ((4 * g + 2 * m + t) * 32 + r) * 896 + (56 * n + 32 * u + 4 * hh) * 2;
        sfor<4>([&](auto i) { xv[i.value] = __builtin_amdgcn_raw_buffer_load_b64(rsX, vx + 16 * i.value, 0, 0); });
        const int row = lane >> 2, wq = min(w, 2); const unsigned c16 = (lane & 3) * 16, v896 = row * 896 + c16;
        int fk[2], i2[2]; bool isD[2];
        sfor<2>([&](auto i) {
            const int f = (i.value && g < 12) ? g + 16 : g, fq = (f * 37) >> 8, fm = f - 7 * fq, r0 = 112 * fq + (fm == 6 ? 48 : (fm >= 3 ? 8 : 0) + 16 * fm);
            fk[i.value] = f; isD[i.value] = fm == 6; i2[i.value] = 14 * g + 7 * m + min(2 * w + i.value, 6);
            const unsigned rr = row + ((fm == 6 && row >= 8) ? 48 : 0);
            dma(stg0 + i.value * 1024, rr * 2688 + c16, rsQKV, r0 * 2688 + wq * 896 + j * 56);
            dma(stg0 + (2 + i.value) * 1024, rr * 3584 + c16, rsW1, r0 * 3584 + j * 224 + w * 64);
            dma(stg0 + (4 + i.value) * 1024, v896, rsW2, (i2[i.value] >> 1) * (16 * 896) + n * 112 + (i2[i.value] & 1) * 64);
        });
        const int po = min(2 * g + m + 32 * u, 55), pq = po >= 28;
        const u32x4 ov = __builtin_amdgcn_raw_buffer_load_b128(rsWO, v896, pq * 64 + n * 112 + (po - 28 * pq) * (16 * 896), 0);
        tag = __hip_atomic_load((GAS u64*)ws, RLX_AGENT);
        if (w == 0) *(LAS unsigned*)(size_t)(LDS_WO + 2 * IMG + lane * 4) = 0u;
        SB();
        __builtin_amdgcn_s_waitcnt(0x0070);
        WAIT_VM0();
        TS(19);
        pin_v(cw);
        u32x4 fr[6];
        sfor<6>([&](auto i) { const bool D = i.value < 4 && isD[i.value & 1]; const unsigned a = stg0 + i.value * 1024 + trl + (D ? 512 : 256) * hh; fr[i.value] = trfrag(a, a + (D ? 256 : 512)); });
        sfor<4>([&](auto i) { resid[4 * i.value] = bflo(xv[i.value][0]); resid[4 * i.value + 1] = bfhi(xv[i.value][0]); resid[4 * i.value + 2] = bflo(xv[i.value][1]); resid[4 * i.value + 3] = bfhi(xv[i.value][1]); });
        tag += 1;
        publish_x(WS_X0);
        const unsigned v1 = w < 3 ? l16 : r < 16 ? (hh * 16 + r) * 16 : WS_BYTES;
        sfor<2>([&](auto i) {
            stg(fr[i.value], rsW, l16, wt + T_Q + (3 * fk[i.value] + wq) * 1024);
            stg(fr[2 + i.value], rsW, v1, wt + T_1 + w * IMG + fk[i.value] * (w < 3 ? 1024 : 512));
            stg(fr[4 + i.value], rsW, l16, wn + T_2 + i2[i.value] * 1024);
        });
        if (t == 0) stg(ov, rsW, l16, wn + T_O + po * 1024);
        float cq = 0.f, c1 = 0.f, cq2 = 0.f, c12 = 0.f;
        sfor<4>([&](auto k) { cq = dot2(fr[0][k.value], 0x3F803F80u, cq); cq2 = dot2(fr[1][k.value], 0x3F803F80u, cq2); c1 = dot2(fr[2][k.value], 0x3F803F80u, c1); c12 = dot2(fr[3][k.value], 0x3F803F80u, c12); });
        if (g < 12) { cq += cq2; c1 += c12; }
        cq = half_sum(cq); c1 = half_sum(c1);
        if (hh == 0) {
            __builtin_amdgcn_raw_buffer_store_b32(f2u(cq), rsW, r * 4, wt + T_CQ + (g * 3 + wq) * 128, 16);
            __builtin_amdgcn_raw_buffer_store_b32(f2u(c1), rsW, r * 4, wt + T_C1 + (g * 4 + w) * 128, 16);
        }
        TS(20);
        BAR_VM();
        if (w == 0) {
            const int q = lane >> 4, i = lane & 15;
            if (q < 3) __hip_atomic_store((GAS u64*)(ws + WS_FLAGS + (q == 0 ? ((10 * 16 + g) * 16 + i) * 256 + j * 8 : ((11 * 16 + i) * 16 + (q == 1 ? j : j ^ 8)) * 256 + (q - 1) * 128 + g * 8)), tag, RLX_AGENT);
            poll_at(lane < 32 ? ((11 * 16 + g) * 16 + j) * 256 + lane * 8 : ((10 * 16 + g) * 16 + j) * 256 + i * 8);
        }
        BAR();
    }

#pragma unroll 1
    for (; layer < 2; ++layer) {
        FRESH();
        const unsigned LB = sma<L_STRIDE>(layer, WS_DATA);
        TS(0);
        {
            u32x4 xf[28]; u32x2 st[8];
            const unsigned tb = sma<XT>(w, sma<4 * XT>(g, sma<WS_DATA + L_X2 - WS_X0>(layer, WS_X0))), wt = sma<T_STRIDE>(j, WS_WT);
            auto xload = [&](auto f) { ald<(f.value & 3) * 1024>(xf[f.value], l16, rsW, tb + (f.value >> 2) * 4096); };
            float cs[8], cq[8]; unsigned bias;
            sfor<8>([&](auto i) { def_v(cs[i.value]); def_v(cq[i.value]); }); def_v(bias);
            if (layer == 0) {
                const unsigned v1 = hh * 4096 + r * 4, vq = hh * 3072 + r * 4, s1 = wt + T_C1 + w * 128, sq = wt + T_CQ + min(w, 2) * 128;
                sfor<8>([&](auto i) { ald<i.value * 512>(cs[i.value], v1, rsW, s1); ald<i.value * 384>(cq[i.value], vq, rsW, sq); });
                ald_u16(bias, b1 + 112 * j + (w == 3 ? 96 + (r & 15) : 32 * w + r));
                stage(LDS_R1, T_Q, 0, 21, 0, 4);
            }
            sfor<12>([&](auto f) { xload(f); });
            if (layer == 0) {
                await<32>(bias); sfor<8>([&](auto i) { landed(cs[i.value]); landed(cq[i.value]); });
                const u32x4 g1 = make_waug(half_sum(((cs[0] + cs[1]) + (cs[2] + cs[3])) + ((cs[4] + cs[5]) + (cs[6] + cs[7]))), bias);
                const u32x4 gq = make_waug(half_sum(((cq[0] + cq[1]) + (cq[2] + cq[3])) + ((cq[4] + cq[5]) + (cq[6] + cq[7]))), 0u);
                if (hh == 0) { *(LAS u32x4*)(size_t)(LDS_AUG + w * 512 + r * 16) = g1; if (w < 3) *(LAS u32x4*)(size_t)(LDS_AUG + 2048 + w * 512 + r * 16) = gq; }
                bar_vm<12>();
            }
            SB();
            TS(1);
            unsigned wa = LDS_R1 + l16, wb = wa + 16 * 3072, ga = LDS_AUG + 2048 + r * 16, vs = hh * 2048 + r * 8, rb = LDS_HALF + w * 128 + hh * 16;
            pin_v(wa); pin_v(wb); pin_v(ga); pin_v(vs); pin_v(rb);
            auto wfrag = [&](int f, int i) { return f == 28 ? lds_frag(ga + i * 512) : f < 16 ? lds_frag(wa + (3 * f + i) * 1024) : lds_frag(wb + (3 * (f - 16) + i) * 1024); };
            f32x16 aq, ak, av; u32x4 wf[3][3], xa; XAug xg; f32x4 rs4[4];
            sfor<2>([&](auto f) { sfor<3>([&](auto i) { wf[f.value][i.value] = wfrag(f.value, i.value); }); });
            await<11>(xf[0]);
            sfor<28>([&](auto ff) {
                constexpr int f = ff.value;
                SB();
                if constexpr (f == 0) mfma0(aq, wf[0][0], xf[0]); else mfma_x(aq, wf[f % 3][0], xf[f], wf[f % 3][1], wf[f % 3][2]);
                SB();
                if constexpr (f == 15) BAR();
                if constexpr (f + 2 <= 28) sfor<3>([&](auto i) { wf[(f + 2) % 3][i.value] = wfrag(f + 2, i.value); });
                SB();
                if constexpr (f == 0) mfma0(ak, wf[0][1], xf[0]); else mfma(ak, wf[f % 3][1], xf[f]);
                SB();
                if constexpr (f < 8) sfor<2>([&](auto i) { xload(std::integral_constant<int, 12 + 2 * f + i.value>{}); });
                if constexpr (f >= 8 && f < 12) sfor<2>([&](auto i) { ald<(2 * (f - 8) + i.value) * 256>(st[2 * (f - 8) + i.value], vs, rsW, tb + 28672); });
                if constexpr (f >= 18 && f < 27) xaug_step<f - 18>(xg, st, xa, hh);
                if constexpr (f == 25) *(LAS float*)(size_t)(LDS_HALF + w * 128 + r * 4) = xg.rstd;
                if constexpr (f == 27) sfor<4>([&](auto i) { rs4[i.value] = lds_ld16(rb + 32 * i.value); });
                SB();
                if constexpr (f == 0) { mfma0(av, xf[0], wf[0][2]); TS(23); } else mfma(av, xf[f], wf[f % 3][2]);
                SB();
                if constexpr (f < 16) await<12 + 2 * (f < 11 ? f + 1 : 12) - (f + 2)>(xf[f + 1]);
                if constexpr (f == 16) { await<0>(xf[17]); sfor<10>([&](auto i) { landed(xf[18 + i.value]); }); sfor<8>([&](auto i) { landed(st[i.value]); }); }
                if constexpr (f == 8) TS(31); if constexpr (f == 14) TS(32); if constexpr (f == 15) TS(33); if constexpr (f == 18) TS(34); if constexpr (f == 22) TS(35);
            });
            SB();
            mfma_x(ak, wf[1][1], xa, wf[1][0], wf[1][2]); mfma(av, xa, wf[1][2]); mfma(aq, wf[1][0], xa);
            const float rstd = xg.rstd;
            TS(24);
            SETTLE3(aq, ak, av);
            const unsigned T = 4 * g + w, kvb = LB + L_KV + (j * 4 + (g >> 2)) * KVQ + (T & 15) * 4096, qo = LB + L_Q + (j * 16 + g) * QG + w * 2048;
            stg(pack8(ak, 0, rstd), rsW, l16, kvb); stg(pack8(ak, 1, rstd), rsW, l16 + 1024, kvb);
            sfor<16>([&](auto i) { av[i.value] *= rs4[i.value >> 2][i.value & 3]; });
            u32x4 v0 = pack8(av, 0, 1.f), v1 = pack8(av, 1, 1.f);
            if (r == 28) { v0 = (u32x4){0x3F803F80u, 0x3F803F80u, 0x3F803F80u, 0x3F803F80u}; v1 = v0; }
            stg(v0, rsW, l16 + 2048, kvb); stg(v1, rsW, l16 + 3072, kvb);
            const float qs = rstd * (0.18898223650461361f * 1.4426950408889634f);
            u32x4 q0 = pack8(aq, 0, qs), q1 = pack8(aq, 1, qs);
            if (hh) { q1[2] = 0u; q1[3] = 0u; }
            stg(q0, rsW, l16, qo); stg(q1, rsW, l16 + 1024, qo);
        }
        TS(2);
        publish(layer * 5 + 0, true);
        TS(3);
        FRESH();
        if (w == 0) poll(layer * 5 + 0);
        BAR();
        TS(4);
        FRESH();
        {
            u32x4 kv[4][4]; f32x16 O[4], S[2]; u32x4 p0, p1;
            sfor<16>([&](auto i) { def_v(kv[i.value >> 2][i.value & 3]); }); def_v(S[0]); def_v(S[1]); def_v(p0);
            unsigned kvs = sma<KVQ>(w, sma<4 * KVQ>(g, LB + L_KV));
            const unsigned qb = sma<QG>(j, sma<16 * QG>(g, LB + L_Q));
            acc_load2<224, 0>(l16, rsW, qb + w * 2048); kv_load2<0>(kv[0][0], kv[0][1], l16, rsW, kvs);
            acc_load2<232, 0>(l16, rsW, qb + (w ^ 1) * 2048); kv_load2<2048>(kv[0][2], kv[0][3], l16, rsW, kvs);
            acc_load2<240, 0>(l16, rsW, qb + (w ^ 2) * 2048); acc_load2<248, 0>(l16, rsW, qb + (w ^ 3) * 2048);
            kv_load(kv[1], l16, rsW, kvs + 4096);
            { float zero = 0.f; pin_v(zero);
              sfor<4>([&](auto c) { sfor<16>([&](auto i) { O[c.value][i.value] = zero; }); pin_v(O[c.value]); });
              p1 = (u32x4){f2u(zero), f2u(zero), f2u(zero), f2u(zero)}; kv[3][3] = p1; pin_v(p1); pin_v(kv[3][3]); }
            unsigned w2a = l16 + u * 1024, w2b = l16 + (u ^ 1) * 1024; const unsigned wn = sma<T_STRIDE>(n, WS_WT), w2s = sma<57344>(w, wn + T_2), w1s = sma<T_STRIDE>(j, WS_WT + T_1);
            kv_wait2<12>(kv[0][0], kv[0][1]);
            TS(22);
            mfmaz_qb<224>(S[0], kv[0][0]); mfma_qb<228>(S[0], kv[0][1]);
            kv_wait2<4>(kv[0][2], kv[0][3]);
            SETTLE1(S[0]);
#pragma unroll 1
            for (int it = 0; it < 4; ++it) {
                const int c15 = 1 - layer, c3 = c15 * it;
                SB();
                sfor<16>([&](auto ii) {
                    constexpr int i = ii.value, tau = i >> 2, c = i & 3, nx = (i + 1) & 15, ntau = nx >> 2, nc = nx & 3, pv = (i + 15) & 15;
                    f32x16& Sc = S[i & 1]; f32x16& Sn = S[nx & 1];
                    if constexpr (i == 3) kv_waitc<8, 30>(kv[1], c3);
                    if constexpr (i == 7) kv_wait<4>(kv[2]);
                    if constexpr (i == 11) {
                        kv_wait<8>(kv[3]);
                        const int q1 = min(4 * it + w, 12);
                        dma4(LDS_R1 + q1 * 4096, l16, rsW, w1s + q1 * 4096);
                        if (layer == 0) {
                            const int qo = min(4 * it + w, 13);
                            dma4(LDS_WO + qo * 4096, l16, rsW, wn + T_O + qo * 4096);
                            auto w2l = [&](auto q) { sfor<7>([&](auto k) { constexpr int kk = 7 * q.value + k.value;
                                acc_load1<8 * kk, (kk & 1) * 2048>(w2a, rsW, w2s + (kk >> 1) * 4096); acc_load1<8 * kk + 4, (kk & 1) * 2048>(w2b, rsW, w2s + (kk >> 1) * 4096); }); };
                            if (it == 0) w2l(std::integral_constant<int, 0>{}); else if (it == 1) w2l(std::integral_constant<int, 1>{});
                            else if (it == 2) w2l(std::integral_constant<int, 2>{}); else w2l(std::integral_constant<int, 3>{});
                        }
                        SB();
                    }
                    if constexpr (i == 15) kv_waitc<8, 26>(kv[0], c15);
                    auto ex = [&](int k, u32x4& p) {
                        const float e0 = __builtin_amdgcn_exp2f(Sc[4 * k]), e1 = __builtin_amdgcn_exp2f(Sc[4 * k + 1]), e2 = __builtin_amdgcn_exp2f(Sc[4 * k + 2]), e3 = __builtin_amdgcn_exp2f(Sc[4 * k + 3]);
                        p[2 * (k & 1)] = cvtpk(e0, e1); p[2 * (k & 1) + 1] = cvtpk(e2, e3);
                        pin_v(p);
                    };
                    mfmaz_qb<224 + 8 * nc>(Sn, kv[ntau][0]);
                    ex(0, p0); SB();
                    mfma_qb<228 + 8 * nc>(Sn, kv[ntau][1]);
                    ex(1, p0); SB();
                    mfma(O[pv & 3], kv[pv >> 2][3], p1);
                    if constexpr (i == 0 || i == 8) {
                        const unsigned so = kvs + (tau + 2) * 4096 + (i == 8 ? ((it + 1) >> 2) * WS_BYTES : 0u);
                        SB(); kv_load(kv[(tau + 2) & 3], l16, rsW, so); kv_load(kv[(tau + 3) & 3], l16, rsW, so + 4096); SB();
                    }
                    ex(2, p1); SB();
                    mfma(O[c], kv[tau][2], p0);
                    ex(3, p1); SB();
                });
                kvs += 16384;
            }
            mfma_n(O[3], kv[3][3], p1);
            kv_waitc<4, 22>(kv[1], 1 - layer);
            SETTLE4(O[0], O[1], O[2], O[3]);
            TS(5);
            FRESH();
            f32x16 o = O[0];
            reduce4(o, O[1], O[2], O[3]);
            float l = upper_half(o[12]);
            float amax = 0.f;
            sfor<16>([&](auto i) { amax = __builtin_fmaxf(amax, __builtin_fabsf(o[i.value])); });
            const bool ok = l >= 5.4e-20f && l <= 1.8e19f && amax <= 3.0e38f;
            if (!__all(ok)) {
                float mrun = -INFINITY;
                float zero = 0.f; pin_v(zero);
                sfor<16>([&](auto i) { o[i.value] = zero; });
                const u32x4 qa = ldg(rsW, l16, LB + L_Q + (g * 16 + j) * QG + w * 2048), qb2 = ldg(rsW, l16 + 1024, LB + L_Q + (g * 16 + j) * QG + w * 2048);
#pragma unroll 1
                for (int T = 0; T < 64; ++T) {
                    const unsigned so = LB + L_KV + (g * 4 + (T >> 4)) * KVQ + (T & 15) * 4096;
                    const u32x4 k0 = ldg(rsW, l16, so), k1 = ldg(rsW, l16 + 1024, so), v0 = ldg(rsW, l16 + 2048, so), v1 = ldg(rsW, l16 + 3072, so);
                    f32x16 s;
                    mfma0_safe(s, k0, qa); mfma_safe(s, k1, qb2);
                    float mt = s[0]; sfor<15>([&](auto i) { mt = __builtin_fmaxf(mt, s[i.value + 1]); });
                    const float mn = __builtin_fmaxf(mrun, half_max(mt)), al = __builtin_amdgcn_exp2f(mrun - mn);
                    mrun = mn;
                    sfor<16>([&](auto i) { o[i.value] *= al; s[i.value] = __builtin_amdgcn_exp2f(s[i.value] - mn); });
                    mfma_safe(o, v0, pack8(s, 0, 1.f)); mfma_safe(o, v1, pack8(s, 1, 1.f));
                }
                l = upper_half(o[12]);
            }
            const float rl = __builtin_amdgcn_rcpf(l);
            u32x4 o0 = pack8(o, 0, rl), o1 = pack8(o, 1, rl);
            if (hh) { o1[2] = 0u; o1[3] = 0u; }
            const unsigned ob = LB + L_O + (8 * j + 2 * w + (g >> 3)) * OH + (g & 7) * 2048;
            stg(o0, rsW, l16, ob); stg(o1, rsW, l16 + 1024, ob);
        }
        TS(6);
        publish(layer * 5 + 1, true);
        TS(7);
        FRESH();
        if (w == 0) poll(layer * 5 + 1);
        else stage(LDS_R1, T_1, 13, 21, 1, 3);
        BAR();
        TS(8);
        {
            u32x4 of[16];
            const unsigned ob = sma<OH>(w, sma<4 * OH>(m, sma<8 * OH>(g, LB + L_O)));
            auto oload = [&](auto s) { ald<(s.value & 3) * 1024>(of[s.value], l16, rsW, ob + (s.value >> 2) * 4096); };
            sfor<12>([&](auto s) { oload(s); });
            SB();
            const unsigned tA = LDS_WO + trl + 256 * hh + u * (8 * 28 * 64); unsigned tO = tA + u * IMG, tS = tA + (u ^ 1) * IMG;
            pin_v(tO); pin_v(tS);
            auto wfrag = [&](int img, int s) { const unsigned o = (img ? tS : tO) + (28 * (s >> 1) + 16 * (s & 1)) * 64; return trfrag(o, o + 512); };
            f32x16 a0, a1; u32x4 wf[3][2];
            sfor<2>([&](auto s) { wf[s.value][0] = wfrag(0, s.value); wf[s.value][1] = wfrag(1, s.value); });
            await<11>(of[0]);
            sfor<16>([&](auto ss) {
                constexpr int s = ss.value;
                SB();
                if constexpr (s == 0) mfma0(a1, wf[0][1], of[0]); else mfma_x(a1, wf[s % 3][1], of[s], wf[s % 3][0], wf[s % 3][0]);
                SB();
                if constexpr (s + 2 < 16) { wf[(s + 2) % 3][0] = wfrag(0, s + 2); wf[(s + 2) % 3][1] = wfrag(1, s + 2); }
                SB();
                if constexpr (s == 0) { mfma0(a0, wf[0][0], of[0]); TS(25); } else mfma(a0, wf[s % 3][0], of[s]);
                SB();
                if constexpr (s < 2) sfor<2>([&](auto i) { oload(std::integral_constant<int, 12 + 2 * s + i.value>{}); });
                if constexpr (s < 15) await<(s == 0 ? 14 : 16) - s - 2>(of[s + 1]);
            });
            SETTLE2(a0, a1);
            TS(26);
            sfor<4>([&](auto k) { lds_st16(LDS_SCR16 + w * 4096 + k.value * 1024 + l16, (f32x4){a1[4 * k.value], a1[4 * k.value + 1], a1[4 * k.value + 2], a1[4 * k.value + 3]}); });
            sfor<16>([&](auto i) { resid[i.value] += a0[i.value]; });
            BAR();
            sfor<4>([&](auto k) { f32x4 v = lds_ld16(LDS_SCR16 + (w ^ 1) * 4096 + k.value * 1024 + l16); sfor<4>([&](auto e) { resid[4 * k.value + e.value] += v[e.value]; }); });
            publish_x(LB + L_X1);
        }
        TS(9);
        publish(layer * 5 + 2, false);
        TS(10);
        FRESH();
        if (w == 0) poll(layer * 5 + 2);
        else stage(LDS_R1, T_1, 21, 25, 1, 3);
        BAR();
        TS(11);
        {
            u32x4 xf[28]; u32x2 st[8];
            const unsigned tb = LB + L_X1 + (4 * g + w) * XT;
            auto xload = [&](int f) { xf[f] = ldg(rsW, l16 + (f & 3) * 1024, tb + (f >> 2) * 4096); };
            sfor<12>([&](auto f) { xload(f.value); });
            SB();
            unsigned wA = LDS_R1 + l16, wC = wA + 2 * IMG, wH = LDS_HALF + (hh * 16 + (r & 15)) * 16, wG = LDS_AUG + r * 16;
            pin_v(wA); pin_v(wC); pin_v(wH); pin_v(wG);
            auto wfrag = [&](int img, int f) { return f == 28 ? lds_frag(wG + img * 512) : img == 3 ? lds_frag(wH + f * 512) : img == 2 ? lds_frag(wC + f * 1024) : lds_frag(wA + img * IMG + f * 1024); };
            f32x16 h0, h1, h2, h3; u32x4 wf[3][2], wp[8], xa; XAug xg;
            sfor<2>([&](auto f) { wf[f.value][0] = wfrag(0, f.value); wf[f.value][1] = wfrag(1, f.value); });
            sfor<28>([&](auto ff) {
                constexpr int f = ff.value;
                SB();
                if constexpr (f == 0) mfma0(h0, wf[0][0], xf[0]); else mfma_x(h0, wf[f % 3][0], xf[f], wf[f % 3][1], wf[f % 3][1]);
                SB();
                if constexpr (f + 2 <= 28) { wf[(f + 2) % 3][0] = wfrag(0, f + 2); wf[(f + 2) % 3][1] = wfrag(1, f + 2); }
                if constexpr (f == 27) { BAR(); sfor<4>([&](auto i) { wp[i.value] = wfrag(2 + (i.value & 1), i.value >> 1); }); }
                SB();
                if constexpr (f == 0) { mfma0(h1, wf[0][1], xf[0]); TS(27); } else mfma(h1, wf[f % 3][1], xf[f]);
                SB();
                if constexpr (f % 2 == 0 && f < 16) { xload(12 + f); xload(13 + f); }
                if constexpr (f >= 16 && f < 20) sfor<2>([&](auto i) { st[2 * (f - 16) + i.value] = __builtin_amdgcn_raw_buffer_load_b64(rsW, (8 * hh + 2 * (f - 16) + i.value) * 256 + r * 8, tb + 28672, 16); });
            });
            TS(28);
            sfor<4>([&](auto i) { wp[4 + i.value] = wfrag(2 + (i.value & 1), 2 + (i.value >> 1)); });
            sfor<8>([&](auto qq) {
                constexpr int q = qq.value;
                SB();
                if constexpr (q < 2) mfma0(q ? h3 : h2, wp[q], xf[0]); else mfma(q & 1 ? h3 : h2, wp[q], xf[q >> 1]);
                SB();
                xaug_step<q>(xg, st, xa, hh);
                if constexpr (q == 3) sfor<4>([&](auto i) { wp[i.value] = wfrag(2 + (i.value & 1), 4 + (i.value >> 1)); });
            });
            xaug_step<8>(xg, st, xa, hh);
            float c1 = xg.rstd * 1.4426950408889634f; pin_v(c1);
            sfor<4>([&](auto i) { wp[4 + i.value] = wfrag(2 + (i.value & 1), 6 + (i.value >> 1)); });
            SB();
            mfma_n(h0, wf[1][0], xa); mfma(h1, wf[1][1], xa);
            mfma(h2, wp[0], xf[4]); mfma(h3, wp[1], xf[4]); mfma(h2, wp[2], xf[5]); mfma(h3, wp[3], xf[5]);
            SB();
            const unsigned ab = LB + L_AF + (16 * g + 4 * w + (j >> 2)) * AQ + (j & 3) * 7168;
            u32x4 pk[4]; float mt[4], me[2], md[2];
            sfor<4>([&](auto i) { def_v(mt[i.value]); }); sfor<2>([&](auto i) { def_v(me[i.value]); def_v(md[i.value]); });
            auto mish_pk = [&](float a, float b2) {
                const f32x2 tt = (f32x2){a, b2} * c1, e = {__builtin_amdgcn_exp2f(tt[0]), __builtin_amdgcn_exp2f(tt[1])};
                const f32x2 d = __builtin_elementwise_fma(e, __builtin_elementwise_fma(e, (f32x2){-0.5f, -0.5f}, (f32x2){-1.f, -1.f}), (f32x2){-1.f, -1.f});
                const f32x2 y = __builtin_elementwise_fma(tt, (f32x2){__builtin_amdgcn_rcpf(d[0]), __builtin_amdgcn_rcpf(d[1])}, tt);
                return cvtpk(y[0], y[1]);
            };
            SETTLE1(h0);
            mt[0] = h0[0] * c1; pin_v(mt[0]);
            sfor<11>([&](auto pp) {
                constexpr int p = pp.value, f = 6 + 2 * p, cur = 4 * ((p + 1) & 1), nxt = 4 * (p & 1), n1 = 2 * p + 1, n2 = 2 * p + 2;
                sfor<4>([&](auto i) { constexpr int nf = f + 2 + (i.value >> 1); if constexpr (nf <= 28) wp[nxt + i.value] = wfrag(2 + (i.value & 1), nf); });
                SB();
                unsigned res;
                mfma4_mish<(p > 0)>(h2, h3, wp[cur], wp[cur + 1], wp[cur + 2], wp[cur + 3], xf[f], xf[f + 1], mt[(2 * p) & 3], mt[(2 * p + 1) & 3], mt[(2 * p + 2) & 3], mt[(2 * p + 3) & 3],
                                    me[0], me[1], md[0], md[1], n1 < 16 ? h0[n1 & 15] : h1[n1 & 15], n2 < 16 ? h0[n2 & 15] : h1[n2 & 15], c1, res);
                if constexpr (p > 0) { constexpr int pr = p - 1; if constexpr ((pr & 3) == 0) def_v(pk[pr >> 2]); pk[pr >> 2][pr & 3] = res; if constexpr ((pr & 3) == 3) stg(pk[pr >> 2], rsW, l16, ab + (pr >> 2) * 1024); }
                SB();
            });
            mfma_x(h2, wp[0], xa, wp[1], wp[1]); mfma(h3, wp[1], xa);
            TS(29);
            pk[2][2] = mish_pk(h1[4], h1[5]); pk[2][3] = mish_pk(h1[6], h1[7]);
            stg(pk[2], rsW, l16, ab + 2 * 1024);
            sfor<4>([&](auto k) { pk[3][k.value] = mish_pk(h1[8 + 2 * k.value], h1[9 + 2 * k.value]); });
            stg(pk[3], rsW, l16, ab + 3 * 1024);
            SETTLE1(h2);
            sfor<2>([&](auto s) { sfor<4>([&](auto k) { pk[s.value][k.value] = mish_pk(h2[8 * s.value + 2 * k.value], h2[8 * s.value + 2 * k.value + 1]); }); stg(pk[s.value], rsW, l16, ab + (4 + s.value) * 1024); });
            SETTLE1(h3);
            sfor<4>([&](auto k) { pk[2][k.value] = mish_pk(h3[2 * k.value], h3[2 * k.value + 1]); });
            stg(pk[2], rsW, l16, ab + 6 * 1024);
        }
        TS(12);
        publish(layer * 5 + 3, false);
        TS(13);
        FRESH();
        if (w == 0) poll(layer * 5 + 3);
        BAR();
        TS(14);
        {
            u32x4 af[28][2];
            const unsigned ab0 = sma<8 * AQ>(m, sma<16 * AQ>(g, sma<AQ>(w, LB + L_AF))), ab = sma<4 * AQ>(t, ab0), ab2 = sma<4 * AQ>(t ^ 1, ab0);
            auto aload = [&](auto k) { ald<(k.value & 3) * 1024>(af[k.value][0], l16, rsW, ab + (k.value >> 2) * 4096); ald<(k.value & 3) * 1024>(af[k.value][1], l16, rsW, ab2 + (k.value >> 2) * 4096); };
            sfor<6>([&](auto k) { aload(k); });
            SB();
            f32x16 y[4];
            const unsigned wqs = sma<WS_BYTES>(layer, sma<T_STRIDE>(j, WS_WT + T_Q));
            await<10>(af[0][1]); landed(af[0][0]);
            sfor<28>([&](auto kk) {
                constexpr int k = kk.value;
                SB();
                if constexpr (k == 0) mfma0_wa<0>(y[0], af[0][0]); else mfma_wa<8 * k>(y[0], af[k][0]);
                SB();
                if constexpr (k + 6 < 28) aload(std::integral_constant<int, k + 6>{});
                SB();
                if constexpr (k == 0) mfma0_wa<4>(y[1], af[0][0]); else mfma_wa<8 * k + 4>(y[1], af[k][0]);
                SB();
                if constexpr (k >= 24) {
                    const int q = min(4 * (k - 24) + w, 12); dma4(LDS_R1 + q * 4096, l16, rsW, wqs + q * 4096);
                }
                SB();
                if constexpr (k == 0) { mfma0_wa<0>(y[2], af[0][1]); mfma0_wa<4>(y[3], af[0][1]); TS(30); } else { mfma_wa<8 * k>(y[2], af[k][1]); mfma_wa<8 * k + 4>(y[3], af[k][1]); }
                SB();
                constexpr int issued = 2 * (k + 7 < 28 ? k + 7 : 28) + (k >= 24 ? 4 * (k - 23) : 0);
                if constexpr (k < 27) { await<issued - 2 * k - 4>(af[k + 1][1]); landed(af[k + 1][0]); }
            });
            SETTLE4(y[0], y[1], y[2], y[3]);
            TS(15);
            FRESH();
            f32x16 o = y[0];
            reduce4(o, y[1], y[2], y[3]);
            sfor<16>([&](auto i) { resid[i.value] = __builtin_fmaf(o[i.value], 0.6931471805599453f, resid[i.value]); });
        }
        if (layer == 0) {
            publish_x(LB + L_X2);
            TS(16);
            publish(4, false);
            TS(17);
            if (w == 0) poll(4);
            else stage(LDS_R1, T_Q, 13, 21, 1, 3);
            TSFLUSH();
            BAR();
        } else {
            const u32x4 p0 = pack8(resid, 0, 1.f), p1 = pack8(resid, 1, 1.f);
            GAS char* op = (GAS char*)out + ((4 * g + 2 * m + t) * 32 + r) * 896 + 112 * n + 64 * u + 16 * hh;
            auto a = __builtin_amdgcn_permlane32_swap(p0[0], p0[2], false, false), c = __builtin_amdgcn_permlane32_swap(p0[1], p0[3], false, false);
            *(GAS u32x4*)op = (u32x4){a[0], c[0], a[1], c[1]};
            a = __builtin_amdgcn_permlane32_swap(p1[0], p1[2], false, false); c = __builtin_amdgcn_permlane32_swap(p1[1], p1[3], false, false);
            if (u == 0 || hh == 0) *(GAS u32x4*)(op + 32) = (u32x4){a[0], c[0], a[1], c[1]};
            if (b == 0 && threadIdx.x == 0) __hip_atomic_store((GAS u64*)ws, tag, RLX_AGENT);
            WAIT_VM0();
            TS(16); RT(63); TSFLUSH();
        }
    }
}

extern "C" void kernel_launch(void* const* d_in, const int* in_sizes, int n_in, void* d_out, int out_size, void* d_ws, size_t ws_size, hipStream_t stream) {
    (void)hipFuncSetAttribute((const void*)mega, hipFuncAttributeMaxDynamicSharedMemorySize, LDS_BYTES);
    mega<<<256, 256, LDS_BYTES, stream>>>((const u16*)d_in[0], (const u16*)d_in[1], (const u16*)d_in[2], (const u16*)d_in[4], (const u16*)d_in[5], (const u16*)d_in[6],
                                          (u16*)d_out, (char*)d_ws);
}
```

```cpp
#include <hip/hip_runtime.h>
#include <utility>

#define DI __device__ __forceinline__
#define LAS __attribute__((address_space(3)))
#define GAS __attribute__((address_space(1)))
typedef float f32x16 __attribute__((ext_vector_type(16)));
typedef float f32x4 __attribute__((ext_vector_type(4)));
typedef float f32x2 __attribute__((ext_vector_type(2)));
typedef unsigned u32x4 __attribute__((ext_vector_type(4)));
typedef unsigned u32x2 __attribute__((ext_vector_type(2)));
typedef short v4i16 __attribute__((ext_vector_type(4)));
typedef __bf16 bf16x2 __attribute__((ext_vector_type(2)));
typedef __amdgpu_buffer_rsrc_t rsrc_t;
typedef unsigned long long u64;
typedef unsigned short u16;

constexpr unsigned WS_FLAGS = 4096;
constexpr unsigned WS_STAMP = 200u << 20;
constexpr unsigned WS_X0 = 1u << 20;
constexpr unsigned WS_WT = 4u << 20;
constexpr unsigned T_Q = 0, T_1 = 86016, T_O = T_1 + 100352, T_2 = T_O + 57344, T_CQ = T_2 + 229376, T_C1 = T_CQ + 6144, T_STRIDE = 491520;
constexpr unsigned WS_DATA = 12u << 20;
#define PADX 0
#define PADKV 0
#define PADQ 0
#define PADO 0
#define PADA 0
constexpr unsigned XT = 32768 + PADX;
constexpr unsigned KVQ = 65536 + PADKV;
constexpr unsigned QG = 8192 + PADQ;
constexpr unsigned OH = 16384 + PADO;
constexpr unsigned AQ = 28672 + PADA;
constexpr unsigned L_Q = 0, L_KV = 3u << 20, L_O = 8u << 20, L_X1 = 11u << 20, L_AF = 14u << 20, L_X2 = 22u << 20, L_STRIDE = 25u << 20;
constexpr unsigned WS_BYTES = WS_DATA + 2 * L_STRIDE;
constexpr unsigned IMG = 28672;
constexpr unsigned LDS_WO = 0;
constexpr unsigned LDS_R1 = 57600;
constexpr unsigned LDS_HALF = LDS_R1 + 3 * IMG;
constexpr unsigned LDS_AUG = 160256;
constexpr unsigned LDS_SCR48 = LDS_AUG - 49152;
constexpr unsigned LDS_SCR16 = LDS_AUG - 16384;
constexpr unsigned LDS_BYTES = 163840;
#define CODE_BYTES 23000

#define SB() __builtin_amdgcn_sched_barrier(0)
#define BAR() asm volatile("s_waitcnt lgkmcnt(0)\n\ts_barrier" ::: "memory")
#define BAR_VM() do { __builtin_amdgcn_s_waitcnt(0x0070); asm volatile("s_waitcnt vmcnt(0) lgkmcnt(0)\n\ts_barrier" ::: "memory"); } while (0)
#define WAIT_VM0() asm volatile("s_waitcnt vmcnt(0)" ::: "memory")
#define RLX_AGENT __ATOMIC_RELAXED, __HIP_MEMORY_SCOPE_AGENT

template <int N, class F> DI void sfor_impl(F&& f, std::integral_constant<int, N>) {}
template <class F, int... Is> DI void sfor_seq(F&& f, std::integer_sequence<int, Is...>) { (f(std::integral_constant<int, Is>{}), ...); }
template <int N, class F> DI void sfor(F&& f) { sfor_seq(f, std::make_integer_sequence<int, N>{}); }

DI void mfma(f32x16& c, const u32x4& a, const u32x4& b) { asm volatile("v_mfma_f32_32x32x16_bf16 %0, %1, %2, %0" : "+v"(c) : "v"(a), "v"(b)); }
DI void mfma0(f32x16& c, const u32x4& a, const u32x4& b) { asm volatile("v_mfma_f32_32x32x16_bf16 %0, %1, %2, 0" : "=&v"(c) : "v"(a), "v"(b)); }
template <int R> DI void mfma_qb(f32x16& c, const u32x4& a) { asm volatile("v_mfma_f32_32x32x16_bf16 %0, %1, a[%c2:%c3], %0" : "+v"(c) : "v"(a), "i"(R), "i"(R + 3)); }
template <int R> DI void mfma0_qb(f32x16& c, const u32x4& a) { asm volatile("v_mfma_f32_32x32x16_bf16 %0, %1, a[%c2:%c3], 0" : "=&v"(c) : "v"(a), "i"(R), "i"(R + 3)); }
template <int R> DI void mfma_wa(f32x16& c, const u32x4& b) { asm volatile("v_mfma_f32_32x32x16_bf16 %0, a[%c2:%c3], %1, %0" : "+v"(c) : "v"(b), "i"(R), "i"(R + 3)); }
template <int R> DI void mfma0_wa(f32x16& c, const u32x4& b) { asm volatile("v_mfma_f32_32x32x16_bf16 %0, a[%c2:%c3], %1, 0" : "=&v"(c) : "v"(b), "i"(R), "i"(R + 3)); }
template <int R> DI void mfmaz_qb(f32x16& c, const u32x4& a) { asm volatile("v_mfma_f32_32x32x16_bf16 %0, %1, a[%c2:%c3], 0" : "+v"(c) : "v"(a), "i"(R), "i"(R + 3)); }
template <int R, int OFF> DI void acc_load2(unsigned voff, __amdgpu_buffer_rsrc_t rs, unsigned soff) {
    asm volatile("buffer_load_dwordx4 a[%c3:%c4], %0, %1, %2 offen offset:%c7 sc1\n\tbuffer_load_dwordx4 a[%c5:%c6], %0, %1, %2 offen offset:%c8 sc1"
                 :: "v"(voff), "s"(rs), "s"(soff), "i"(R), "i"(R + 3), "i"(R + 4), "i"(R + 7), "i"(OFF), "i"(OFF + 1024) : "memory", "a255"); }
template <int R, int OFF> DI void acc_load1(unsigned voff, __amdgpu_buffer_rsrc_t rs, unsigned soff) {
    asm volatile("buffer_load_dwordx4 a[%c3:%c4], %0, %1, %2 offen offset:%c5 sc1" :: "v"(voff), "s"(rs), "s"(soff), "i"(R), "i"(R + 3), "i"(OFF) : "memory"); }
DI void mfma_n(f32x16& c, const u32x4& a, const u32x4& b) { asm volatile("s_nop 1\n\tv_mfma_f32_32x32x16_bf16 %0, %1, %2, %0" : "+v"(c) : "v"(a), "v"(b)); }
DI void mfma0_safe(f32x16& c, const u32x4& a, const u32x4& b) { asm volatile("s_nop 1\n\tv_mfma_f32_32x32x16_bf16 %0, %1, %2, 0\n\ts_nop 11" : "=&v"(c) : "v"(a), "v"(b)); }
DI void mfma_safe(f32x16& c, const u32x4& a, const u32x4& b) { asm volatile("s_nop 1\n\tv_mfma_f32_32x32x16_bf16 %0, %1, %2, %0\n\ts_nop 11" : "+v"(c) : "v"(a), "v"(b)); }
DI void SETTLE1(f32x16& a) { asm volatile("s_nop 11" : "+v"(a)); }
#define SETTLE2(a, b) asm volatile("s_nop 11" : "+v"(a), "+v"(b))
#define SETTLE3(a, b, c) asm volatile("s_nop 11" : "+v"(a), "+v"(b), "+v"(c))
#define SETTLE4(a, b, c, d) asm volatile("s_nop 11" : "+v"(a), "+v"(b), "+v"(c), "+v"(d))

DI unsigned cvtpk(float lo, float hi) { f32x2 v = {lo, hi}; bf16x2 b = __builtin_convertvector(v, bf16x2); return __builtin_bit_cast(unsigned, b); }
DI float bflo(unsigned u) { return __builtin_bit_cast(float, u << 16); }
DI float bfhi(unsigned u) { return __builtin_bit_cast(float, u & 0xffff0000u); }
DI float dot2(unsigned a, unsigned b, float c) { return __builtin_amdgcn_fdot2_f32_bf16(__builtin_bit_cast(bf16x2, a), __builtin_bit_cast(bf16x2, b), c, false); }
DI float u2f(unsigned u) { return __builtin_bit_cast(float, u); }
DI unsigned f2u(float f) { return __builtin_bit_cast(unsigned, f); }
DI float half_sum(float v) { auto r = __builtin_amdgcn_permlane32_swap(f2u(v), f2u(v), false, false); return u2f(r[0]) + u2f(r[1]); }
DI float half_max(float v) { auto r = __builtin_amdgcn_permlane32_swap(f2u(v), f2u(v), false, false); return __builtin_fmaxf(u2f(r[0]), u2f(r[1])); }
DI float upper_half(float v) { auto r = __builtin_amdgcn_permlane32_swap(f2u(v), f2u(v), false, false); return u2f(r[1]); }
#define LDAUX 16
DI u32x4 ldg(rsrc_t rs, unsigned voff, unsigned soff) { return __builtin_amdgcn_raw_buffer_load_b128(rs, voff, soff, LDAUX); }
DI void stg(u32x4 v, rsrc_t rs, unsigned voff, unsigned soff) { __builtin_amdgcn_raw_buffer_store_b128(v, rs, voff, soff, 16); asm volatile("s_nop 1" : "+v"(v) :: "memory"); }
DI void dma(unsigned lds, unsigned voff, rsrc_t rs, unsigned soff) {
    asm volatile("s_nop 4\n\ts_mov_b32 m0, %0\n\ts_nop 0\n\tbuffer_load_dwordx4 %1, %2, %3 offen lds"
                 :: "s"(lds), "v"(voff), "s"(rs), "s"(__builtin_amdgcn_readfirstlane(soff)) : "memory"); }
DI void dma4(unsigned lds, unsigned voff, rsrc_t rs, unsigned soff) {
    asm volatile("s_mov_b32 m0, %0\n\ts_nop 0\n\tbuffer_load_dwordx4 %1, %2, %3 offen sc1 lds\n\tbuffer_load_dwordx4 %1, %2, %3 offen offset:1024 sc1 lds\n\t"
                 "buffer_load_dwordx4 %1, %2, %3 offen offset:2048 sc1 lds\n\tbuffer_load_dwordx4 %1, %2, %3 offen offset:3072 sc1 lds" :: "s"(lds), "v"(voff), "s"(rs), "s"(soff) : "memory"); }
template <unsigned C> DI unsigned sma(unsigned a, unsigned b) { unsigned r; asm("s_mul_i32 %0, %1, %2\n\ts_add_i32 %0, %0, %3" : "=&s"(r) : "s"(a), "i"(C), "s"(b) : "scc"); return r; }
DI void pin_v(u32x4& x) { asm volatile("" : "+v"(x)); }
DI void pin_v(unsigned& x) { asm volatile("" : "+v"(x)); }
DI void pin_v(float& x) { asm volatile("" : "+v"(x)); }
template <class T> DI void def_v(T& x) { asm volatile("" : "=v"(x)); }
DI void pin_v(f32x16& x) { asm volatile("" : "+v"(x)); }
template <int O0> DI void kv_load2(u32x4& d0, u32x4& d1, unsigned voff, rsrc_t rs, unsigned soff) {
    asm volatile("buffer_load_dwordx4 %0, %2, %3, %4 offen offset:%c5 sc1\n\tbuffer_load_dwordx4 %1, %2, %3, %4 offen offset:%c6 sc1"
                 : "+v"(d0), "+v"(d1) : "v"(voff), "s"(rs), "s"(soff), "i"(O0), "i"(O0 + 1024) : "memory"); }
DI void kv_load(u32x4 (&d)[4], unsigned voff, rsrc_t rs, unsigned soff) {
    asm volatile("buffer_load_dwordx4 %0, %4, %5, %6 offen sc1\n\tbuffer_load_dwordx4 %1, %4, %5, %6 offen offset:1024 sc1\n\t"
                 "buffer_load_dwordx4 %2, %4, %5, %6 offen offset:2048 sc1\n\tbuffer_load_dwordx4 %3, %4, %5, %6 offen offset:3072 sc1"
                 : "+v"(d[0]), "+v"(d[1]), "+v"(d[2]), "+v"(d[3]) : "v"(voff), "s"(rs), "s"(soff) : "memory"); }
template <int N> DI void kv_wait(u32x4 (&d)[4]) {
    asm volatile("s_waitcnt vmcnt(%c4)\n\t; KVW %0 %1 %2 %3" : "+v"(d[0]), "+v"(d[1]), "+v"(d[2]), "+v"(d[3]) : "i"(N) : "memory"); }
template <int N> DI void kv_wait2(u32x4& a, u32x4& b) { asm volatile("s_waitcnt vmcnt(%c2)\n\t; KVW %0 %1" : "+v"(a), "+v"(b) : "i"(N) : "memory"); }
template <int NS, int NB> DI void kv_waitc(u32x4 (&d)[4], int cond) {
    asm volatile("s_cmp_lg_u32 %4, 0\n\ts_cbranch_scc1 1f\n\ts_waitcnt vmcnt(%c5)\n1:\n\ts_waitcnt vmcnt(%c6)\n\t; KVW %0 %1 %2 %3"
                 : "+v"(d[0]), "+v"(d[1]), "+v"(d[2]), "+v"(d[3]) : "s"(cond), "i"(NS), "i"(NB) : "memory", "scc"); }
template <int OFF> DI void ald(u32x4& d, unsigned voff, rsrc_t rs, unsigned soff) {
    asm volatile("buffer_load_dwordx4 %0, %1, %2, %3 offen offset:%c4 sc1" : "=v"(d) : "v"(voff), "s"(rs), "s"(soff), "i"(OFF) : "memory"); }
template <int OFF> DI void ald(u32x2& d, unsigned voff, rsrc_t rs, unsigned soff) {
    asm volatile("buffer_load_dwordx2 %0, %1, %2, %3 offen offset:%c4 sc1" : "=v"(d) : "v"(voff), "s"(rs), "s"(soff), "i"(OFF) : "memory"); }
template <int N, class T> DI void await(T& d) { asm volatile("s_waitcnt vmcnt(%c1)\n\t; KVW %0" : "+v"(d) : "i"(N) : "memory"); }
template <class T> DI void landed(T& d) { asm volatile("; KVW %0" : "+v"(d) :: "memory"); }
template <int N> DI void bar_vm() { asm volatile("s_waitcnt vmcnt(%c0) lgkmcnt(0)\n\ts_barrier" :: "i"(N) : "memory"); }
DI void mfma_x(f32x16& c, const u32x4& a, const u32x4& b, const u32x4& d1, const u32x4& d2) { asm volatile("v_mfma_f32_32x32x16_bf16 %0, %1, %2, %0" : "+v"(c) : "v"(a), "v"(b), "v"(d1), "v"(d2)); }
template <bool CVT> DI void mfma4_mish(f32x16& c2, f32x16& c3, const u32x4& w0, const u32x4& w1, const u32x4& w2, const u32x4& w3, const u32x4& b0, const u32x4& b1,
                                       float& t0, float& t1, float& t2, float& t3, float& e0, float& e1, float& d0, float& d1, float xa, float xb, float c1, unsigned& r) {
#define MISH4_HEAD "v_mfma_f32_32x32x16_bf16 %0, %11, %15, %0\n\tv_exp_f32 %6, %2\n\tv_fma_f32 %9, %7, %9, -1.0\n\tv_fmac_f32 %4, %4, %8\n\t" \
                   "v_mfma_f32_32x32x16_bf16 %1, %12, %15, %1\n\tv_rcp_f32 %9, %9\n\tv_fma_f32 %8, %6, -0.5, -1.0\n\tv_mul_f32 %3, %19, %17\n\t" \
                   "v_mfma_f32_32x32x16_bf16 %0, %13, %16, %0\n\tv_exp_f32 %7, %3\n\tv_fma_f32 %8, %6, %8, -1.0\n\tv_fmac_f32 %5, %5, %9\n\t" \
                   "v_mfma_f32_32x32x16_bf16 %1, %14, %16, %1\n\tv_rcp_f32 %8, %8\n\tv_fma_f32 %9, %7, -0.5, -1.0\n\t"
    if constexpr (CVT) asm volatile(MISH4_HEAD "v_cvt_pk_bf16_f32 %10, %4, %5\n\tv_mul_f32 %4, %19, %18"
        : "+v"(c2), "+v"(c3), "+v"(t0), "+v"(t1), "+v"(t2), "+v"(t3), "+v"(e0), "+v"(e1), "+v"(d0), "+v"(d1), "=&v"(r)
        : "v"(w0), "v"(w1), "v"(w2), "v"(w3), "v"(b0), "v"(b1), "v"(xa), "v"(xb), "v"(c1));
    else asm volatile(MISH4_HEAD "v_mul_f32 %4, %19, %18"
        : "+v"(c2), "+v"(c3), "+v"(t0), "+v"(t1), "+v"(t2), "+v"(t3), "+v"(e0), "+v"(e1), "+v"(d0), "+v"(d1), "=&v"(r)
        : "v"(w0), "v"(w1), "v"(w2), "v"(w3), "v"(b0), "v"(b1), "v"(xa), "v"(xb), "v"(c1));
}
template <int OFF> DI void ald(float& d, unsigned voff, rsrc_t rs, unsigned soff) {
    asm volatile("buffer_load_dword %0, %1, %2, %3 offen offset:%c4 sc1 ; CORR" : "+v"(d) : "v"(voff), "s"(rs), "s"(soff), "i"(OFF) : "memory"); }
DI void ald_u16(unsigned& d, const void* p) { asm volatile("global_load_ushort %0, %1, off" : "+v"(d) : "v"(p) : "memory"); }
DI float addp(float a, float b) { float r = a + b; asm volatile("" : "+v"(r)); return r; }
struct XAug { float s1, s2, mean, var, rstd, mh, sd; unsigned mhb, d0, d1, d2; };
template <int STEP> DI void xaug_step(XAug& a, const u32x2 (&st)[8], u32x4& xa, int hh) {
    if constexpr (STEP == 0) { a.s1 = addp(addp(u2f(st[0][0]), u2f(st[1][0])), addp(u2f(st[2][0]), u2f(st[3][0]))); a.mean = addp(u2f(st[4][0]), u2f(st[5][0])); a.s1 = addp(a.s1, a.mean); }
    if constexpr (STEP == 1) { a.s1 = addp(a.s1, addp(u2f(st[6][0]), u2f(st[7][0]))); a.s2 = addp(addp(u2f(st[0][1]), u2f(st[1][1])), addp(u2f(st[2][1]), u2f(st[3][1]))); }
    if constexpr (STEP == 2) { a.s2 = addp(addp(a.s2, addp(u2f(st[4][1]), u2f(st[5][1]))), addp(u2f(st[6][1]), u2f(st[7][1]))); }
    if constexpr (STEP == 3) { a.s1 = half_sum(a.s1); pin_v(a.s1); }
    if constexpr (STEP == 4) { a.s2 = half_sum(a.s2); pin_v(a.s2); }
    if constexpr (STEP == 5) { a.mean = a.s1 * (-1.f / 448.f); pin_v(a.mean); a.var = a.s2 * (1.f / 448.f); pin_v(a.var); a.var = __builtin_fmaxf(__builtin_fmaf(-a.mean, a.mean, a.var), 0.f) + 1e-5f; pin_v(a.var); }
    if constexpr (STEP == 6) { a.rstd = __builtin_amdgcn_rsqf(a.var); a.mhb = cvtpk(a.mean, 0.f); a.mh = bflo(a.mhb); a.sd = a.var * a.rstd; pin_v(a.rstd); pin_v(a.mhb); pin_v(a.mh); pin_v(a.sd); }
    if constexpr (STEP == 7) { a.d0 = cvtpk(a.mh, a.mean - a.mh); a.d1 = cvtpk(a.mh, a.sd); a.d2 = cvtpk(a.sd - bfhi(a.d1), 0.f); pin_v(a.d0); pin_v(a.d1); pin_v(a.d2); }
    if constexpr (STEP == 8) { xa = hh == 0 ? (u32x4){a.d0, a.d1, a.d2, 0u} : (u32x4){0u, 0u, 0u, 0u}; pin_v(xa); }
}
DI u32x4 lds_frag(unsigned a) { return *(LAS u32x4*)(size_t)a; }
DI u32x4 trfrag(unsigned a1, unsigned a2) {
    v4i16 lo = __builtin_amdgcn_ds_read_tr16_b64_v4i16((LAS v4i16*)(size_t)a1), hi = __builtin_amdgcn_ds_read_tr16_b64_v4i16((LAS v4i16*)(size_t)a2);
    u32x2 l = __builtin_bit_cast(u32x2, lo), h = __builtin_bit_cast(u32x2, hi);
    return (u32x4){l[0], l[1], h[0], h[1]}; }
DI void lds_st16(unsigned a, f32x4 v) { *(LAS f32x4*)(size_t)a = v; }
DI f32x4 lds_ld16(unsigned a) { return *(LAS f32x4*)(size_t)a; }
constexpr int xrow(int f) { return (f % 7 == 6) ? 112 * (f / 7) + 48 : 112 * (f / 7) + (f % 7 >= 3 ? 56 : 0) + 16 * ((f % 7) % 3); }
DI u32x4 pack8(const f32x16& v, int s, float k) {
    return (u32x4){cvtpk(v[8 * s] * k, v[8 * s + 1] * k), cvtpk(v[8 * s + 2] * k, v[8 * s + 3] * k), cvtpk(v[8 * s + 4] * k, v[8 * s + 5] * k), cvtpk(v[8 * s + 6] * k, v[8 * s + 7] * k)}; }
DI unsigned hi_lo(float v, float& hi) { unsigned h = cvtpk(v, 0.f); hi = bflo(h); return h & 0xffffu; }

#define TS(i) do {} while (0)
#define RT(i) do {} while (0)
#define TSFLUSH() do {} while (0)
#define SHARED_FLAGS
#define FFN1_ST 16
#define FFN1_XA 27
#define PRESLEEP 8

__global__ __launch_bounds__(256, 1) void mega(const u16* __restrict__ x, const u16* __restrict__ wqkv, const u16* __restrict__ wo, const u16* __restrict__ w1,
                                               const u16* __restrict__ b1, const u16* __restrict__ w2, u16* __restrict__ out, char* __restrict__ ws) {
    const u64 t0_ = __builtin_amdgcn_s_memrealtime();
    int lane = threadIdx.x & 63, w = __builtin_amdgcn_readfirstlane(threadIdx.x >> 6);
    const int b = blockIdx.x;
    int g = (b & 7) * 2 + (b >> 7), j = (b >> 3) & 15;
    int r, hh, m, n, t, u; unsigned trl, l16;
    int layer = 0;
#define FRESH() do { asm volatile("" : "+s"(g), "+s"(j), "+s"(w), "+s"(layer), "+v"(lane)); r = lane & 31; hh = lane >> 5; \
        asm volatile("s_lshr_b32 %0, %4, 3\n\ts_and_b32 %1, %4, 7\n\ts_lshr_b32 %2, %5, 1\n\ts_and_b32 %3, %5, 1" : "=&s"(m), "=&s"(n), "=&s"(t), "=&s"(u) : "s"(j), "s"(w) : "scc"); \
        trl = ((lane & 15) >> 2) * 64 + ((lane >> 4) & 1) * 32 + (lane & 3) * 8; l16 = lane * 16; } while (0)
    const rsrc_t rsW = __builtin_amdgcn_make_buffer_rsrc((void*)ws, 0, WS_BYTES, 0x00020000);
    u64 tag;
    bool dead = false; u64 pend_;
    f32x16 resid;
    unsigned tsv = 0;

    auto publish = [&](int S, bool cross) {
        BAR_VM();
        if (w == 0 && lane == 0) __hip_atomic_store((GAS u64*)(ws + WS_FLAGS + ((S * 16 + (cross ? j : g)) * 16 + (cross ? g : j)) * 256), tag, RLX_AGENT);
    };
    auto poll_at = [&](unsigned off) {
        GAS u64* f = (GAS u64*)(ws + WS_FLAGS + off);
        if (PRESLEEP) __builtin_amdgcn_s_sleep(PRESLEEP);
        if (!dead) {
            for (int spin = 0;; ++spin) {
                u64 v = __hip_atomic_load(f, RLX_AGENT);
                if (__all(v == tag)) break;
                if (spin > 400000) { dead = true; break; }
                __builtin_amdgcn_s_sleep(1);
            }
        }
    };
    auto poll = [&](int S) { poll_at(((S * 16 + g) * 16 + (lane & 15)) * 256); };
    auto stage = [&](unsigned lds, unsigned src, int g0, int g1, int w0, int nw) {
        const unsigned sb = sma<T_STRIDE>(j, WS_WT + src);
        for (int q = g0 + w - w0; q < g1; q += nw) dma4(lds + q * 4096, l16, rsW, sb + q * 4096);
    };
    auto publish_x = [&](unsigned base) {
        const unsigned tb = base + (4 * g + 2 * m + t) * XT, f0 = 7 * (n >> 1) + 3 * (n & 1);
        u32x4 p0 = pack8(resid, 0, 1.f), p1 = pack8(resid, 1, 1.f);
        if (u == 0) { stg(p0, rsW, l16, tb + f0 * 1024); stg(p1, rsW, l16, tb + f0 * 1024 + 1024); }
        else {
            stg(p0, rsW, l16, tb + f0 * 1024 + 2048);
            auto a = __builtin_amdgcn_permlane32_swap(p1[0], p1[0], false, false), c = __builtin_amdgcn_permlane32_swap(p1[1], p1[1], false, false);
            if (hh == (n & 1)) stg((u32x4){a[0], c[0], a[1], c[1]}, rsW, l16, tb + (7 * (n >> 1) + 6) * 1024);
            p1[2] = 0u; p1[3] = 0u;
        }
        float s1 = 0.f, s2 = 0.f, s3 = 0.f, s4 = 0.f;
        sfor<4>([&](auto i) { s1 = dot2(p0[i.value], 0x3F803F80u, s1); s2 = dot2(p0[i.value], p0[i.value], s2); s3 = dot2(p1[i.value], 0x3F803F80u, s3); s4 = dot2(p1[i.value], p1[i.value], s4); });
        s1 = half_sum(s1 + s3); s2 = half_sum(s2 + s4);
        if (hh == 0) __builtin_amdgcn_raw_buffer_store_b64((u32x2){f2u(s1), f2u(s2)}, rsW, r * 8, tb + 28672 + (2 * n + u) * 256, 16);
    };
    auto reduce4 = [&](f32x16& o, const f32x16& v1, const f32x16& v2, const f32x16& v3) {
        unsigned sw = LDS_SCR48 + w * 12288 + l16; pin_v(sw);
        sfor<4>([&](auto k) { lds_st16(sw + k.value * 1024, (f32x4){v1[4 * k.value], v1[4 * k.value + 1], v1[4 * k.value + 2], v1[4 * k.value + 3]}); });
        sfor<4>([&](auto k) { lds_st16(sw + 4096 + k.value * 1024, (f32x4){v2[4 * k.value], v2[4 * k.value + 1], v2[4 * k.value + 2], v2[4 * k.value + 3]}); });
        sfor<4>([&](auto k) { lds_st16(sw + 8192 + k.value * 1024, (f32x4){v3[4 * k.value], v3[4 * k.value + 1], v3[4 * k.value + 2], v3[4 * k.value + 3]}); });
        BAR();
        sfor<3>([&](auto d) { unsigned a = LDS_SCR48 + (w ^ (d.value + 1)) * 12288 + d.value * 4096 + l16; pin_v(a);
            sfor<4>([&](auto k) { f32x4 v = lds_ld16(a + k.value * 1024); sfor<4>([&](auto e) { o[4 * k.value + e.value] += v[e.value]; }); }); });
    };
    auto make_waug = [&](float cs, unsigned bias16) {
        float ch; const unsigned chb = hi_lo(cs, ch);
        return (u32x4){chb | (chb << 16), (cvtpk(cs - ch, 0.f) & 0xffffu) | (bias16 << 16), bias16, 0u};
    };

    {
        unsigned cw = *(GAS unsigned*)((GAS char*)__builtin_amdgcn_s_getpc() + min((unsigned)((b >> 3) * 256 + threadIdx.x) * 4u, (unsigned)CODE_BYTES));
        SB();
        FRESH();
        TS(18); RT(62);
        const rsrc_t rsX = __builtin_amdgcn_make_buffer_rsrc((void*)x, 0, 2048 * 896, 0x00020000);
        const rsrc_t rsQKV = __builtin_amdgcn_make_buffer_rsrc((void*)wqkv, 0, 448 * 2688, 0x00020000);
        const rsrc_t rsWO = __builtin_amdgcn_make_buffer_rsrc((void*)wo, 0, 448 * 896, 0x00020000);
        const rsrc_t rsW1 = __builtin_amdgcn_make_buffer_rsrc((void*)w1, 0, 448 * 3584, 0x00020000);
        const rsrc_t rsW2 = __builtin_amdgcn_make_buffer_rsrc((void*)w2, 0, 1792 * 896, 0x00020000);
        const unsigned stg0 = LDS_R1 + w * 6144, wt = sma<T_STRIDE>(j, WS_WT), wn = sma<T_STRIDE>(n, WS_WT);
        u32x2 xv[4];
        const unsigned vx = ((4 * g + 2 * m + t) * 32 + r) * 896 + (56 * n + 32 * u + 4 * hh) * 2;
        sfor<4>([&](auto i) { xv[i.value] = __builtin_amdgcn_raw_buffer_load_b64(rsX, vx + 16 * i.value, 0, 0); });
        const int row = lane >> 2, wq = min(w, 2); const unsigned c16 = (lane & 3) * 16, v896 = row * 896 + c16;
        int fk[2], i2[2]; bool isD[2];
        sfor<2>([&](auto i) {
            const int f = (i.value && g < 12) ? g + 16 : g, fq = (f * 37) >> 8, fm = f - 7 * fq, r0 = 112 * fq + (fm == 6 ? 48 : (fm >= 3 ? 8 : 0) + 16 * fm);
            fk[i.value] = f; isD[i.value] = fm == 6; i2[i.value] = 14 * g + 7 * m + min(2 * w + i.value, 6);
            const unsigned rr = row + ((fm == 6 && row >= 8) ? 48 : 0);
            dma(stg0 + i.value * 1024, rr * 2688 + c16, rsQKV, r0 * 2688 + wq * 896 + j * 56);
            dma(stg0 + (2 + i.value) * 1024, rr * 3584 + c16, rsW1, r0 * 3584 + j * 224 + w * 64);
            dma(stg0 + (4 + i.value) * 1024, v896, rsW2, (i2[i.value] >> 1) * (16 * 896) + n * 112 + (i2[i.value] & 1) * 64);
        });
        const int po = min(2 * g + m + 32 * u, 55), pq = po >= 28;
        const u32x4 ov = __builtin_amdgcn_raw_buffer_load_b128(rsWO, v896, pq * 64 + n * 112 + (po - 28 * pq) * (16 * 896), 0);
        tag = __hip_atomic_load((GAS u64*)ws, RLX_AGENT);
        const u64 pe_ = __hip_atomic_load((GAS u64*)(ws + 512), RLX_AGENT);
        if (threadIdx.x == 0) __hip_atomic_store((GAS u64*)(ws + (900u << 10)) + b, t0_, RLX_AGENT);
        pend_ = pe_;
        if (w == 0) *(LAS unsigned*)(size_t)(LDS_WO + 2 * IMG + lane * 4) = 0u;
        SB();
        __builtin_amdgcn_s_waitcnt(0x0070);
        WAIT_VM0();
        TS(19);
        pin_v(cw);
        u32x4 fr[6];
        sfor<6>([&](auto i) { const bool D = i.value < 4 && isD[i.value & 1]; const unsigned a = stg0 + i.value * 1024 + trl + (D ? 512 : 256) * hh; fr[i.value] = trfrag(a, a + (D ? 256 : 512)); });
        sfor<4>([&](auto i) { resid[4 * i.value] = bflo(xv[i.value][0]); resid[4 * i.value + 1] = bfhi(xv[i.value][0]); resid[4 * i.value + 2] = bflo(xv[i.value][1]); resid[4 * i.value + 3] = bfhi(xv[i.value][1]); });
        tag += 1;
        publish_x(WS_X0);
        const unsigned v1 = w < 3 ? l16 : r < 16 ? (hh * 16 + r) * 16 : WS_BYTES;
        sfor<2>([&](auto i) {
            stg(fr[i.value], rsW, l16, wt + T_Q + (3 * fk[i.value] + wq) * 1024);
            stg(fr[2 + i.value], rsW, v1, wt + T_1 + w * IMG + fk[i.value] * (w < 3 ? 1024 : 512));
            stg(fr[4 + i.value], rsW, l16, wn + T_2 + i2[i.value] * 1024);
        });
        if (t == 0) stg(ov, rsW, l16, wn + T_O + po * 1024);
        float cq = 0.f, c1 = 0.f, cq2 = 0.f, c12 = 0.f;
        sfor<4>([&](auto k) { cq = dot2(fr[0][k.value], 0x3F803F80u, cq); cq2 = dot2(fr[1][k.value], 0x3F803F80u, cq2); c1 = dot2(fr[2][k.value], 0x3F803F80u, c1); c12 = dot2(fr[3][k.value], 0x3F803F80u, c12); });
        if (g < 12) { cq += cq2; c1 += c12; }
        cq = half_sum(cq); c1 = half_sum(c1);
        if (hh == 0) {
            __builtin_amdgcn_raw_buffer_store_b32(f2u(cq), rsW, r * 4, wt + T_CQ + (g * 3 + wq) * 128, 16);
            __builtin_amdgcn_raw_buffer_store_b32(f2u(c1), rsW, r * 4, wt + T_C1 + (g * 4 + w) * 128, 16);
        }
        TS(20);
        BAR_VM();
        if (w == 0) {
            const int q = lane >> 4, i = lane & 15;
            if (q < 3) __hip_atomic_store((GAS u64*)(ws + WS_FLAGS + (q == 0 ? ((10 * 16 + g) * 16 + i) * 256 + j * 8 : ((11 * 16 + i) * 16 + (q == 1 ? j : j ^ 8)) * 256 + (q - 1) * 128 + g * 8)), tag, RLX_AGENT);
            poll_at(lane < 32 ? ((11 * 16 + g) * 16 + j) * 256 + lane * 8 : ((10 * 16 + g) * 16 + j) * 256 + i * 8);
        }
        BAR();
    }

#pragma unroll 1
    for (; layer < 2; ++layer) {
        FRESH();
        const unsigned LB = sma<L_STRIDE>(layer, WS_DATA);
        TS(0);
        {
            u32x4 xf[28]; u32x2 st[8];
            const unsigned tb = sma<XT>(w, sma<4 * XT>(g, sma<WS_DATA + L_X2 - WS_X0>(layer, WS_X0))), wt = sma<T_STRIDE>(j, WS_WT);
            auto xload = [&](auto f) { ald<(f.value & 3) * 1024>(xf[f.value], l16, rsW, tb + (f.value >> 2) * 4096); };
            float cs[8], cq[8]; unsigned bias;
            sfor<8>([&](auto i) { def_v(cs[i.value]); def_v(cq[i.value]); }); def_v(bias);
            if (layer == 0) {
                const unsigned v1 = hh * 4096 + r * 4, vq = hh * 3072 + r * 4, s1 = wt + T_C1 + w * 128, sq = wt + T_CQ + min(w, 2) * 128;
                sfor<8>([&](auto i) { ald<i.value * 512>(cs[i.value], v1, rsW, s1); ald<i.value * 384>(cq[i.value], vq, rsW, sq); });
                ald_u16(bias, b1 + 112 * j + (w == 3 ? 96 + (r & 15) : 32 * w + r));
                stage(LDS_R1, T_Q, 0, 21, 0, 4);
            }
            sfor<12>([&](auto f) { xload(f); });
            if (layer == 0) {
                await<32>(bias); sfor<8>([&](auto i) { landed(cs[i.value]); landed(cq[i.value]); });
                const u32x4 g1 = make_waug(half_sum(((cs[0] + cs[1]) + (cs[2] + cs[3])) + ((cs[4] + cs[5]) + (cs[6] + cs[7]))), bias);
                const u32x4 gq = make_waug(half_sum(((cq[0] + cq[1]) + (cq[2] + cq[3])) + ((cq[4] + cq[5]) + (cq[6] + cq[7]))), 0u);
                if (hh == 0) { *(LAS u32x4*)(size_t)(LDS_AUG + w * 512 + r * 16) = g1; if (w < 3) *(LAS u32x4*)(size_t)(LDS_AUG + 2048 + w * 512 + r * 16) = gq; }
                bar_vm<12>();
            }
            SB();
            TS(1);
            unsigned wa = LDS_R1 + l16, wb = wa + 16 * 3072, ga = LDS_AUG + 2048 + r * 16, vs = hh * 2048 + r * 8, rb = LDS_HALF + w * 128 + hh * 16;
            pin_v(wa); pin_v(wb); pin_v(ga); pin_v(vs); pin_v(rb);
            auto wfrag = [&](int f, int i) { return f == 28 ? lds_frag(ga + i * 512) : f < 16 ? lds_frag(wa + (3 * f + i) * 1024) : lds_frag(wb + (3 * (f - 16) + i) * 1024); };
            f32x16 aq, ak, av; u32x4 wf[3][3], xa; XAug xg; f32x4 rs4[4];
            sfor<2>([&](auto f) { sfor<3>([&](auto i) { wf[f.value][i.value] = wfrag(f.value, i.value); }); });
            await<11>(xf[0]);
            sfor<28>([&](auto ff) {
                constexpr int f = ff.value;
                SB();
                if constexpr (f == 0) mfma0(aq, wf[0][0], xf[0]); else mfma_x(aq, wf[f % 3][0], xf[f], wf[f % 3][1], wf[f % 3][2]);
                SB();
                if constexpr (f == 15) BAR();
                if constexpr (f + 2 <= 28) sfor<3>([&](auto i) { wf[(f + 2) % 3][i.value] = wfrag(f + 2, i.value); });
                SB();
                if constexpr (f == 0) mfma0(ak, wf[0][1], xf[0]); else mfma(ak, wf[f % 3][1], xf[f]);
                SB();
                if constexpr (f < 8) sfor<2>([&](auto i) { xload(std::integral_constant<int, 12 + 2 * f + i.value>{}); });
                if constexpr (f >= 8 && f < 12) sfor<2>([&](auto i) { ald<(2 * (f - 8) + i.value) * 256>(st[2 * (f - 8) + i.value], vs, rsW, tb + 28672); });
                if constexpr (f >= 18 && f < 27) xaug_step<f - 18>(xg, st, xa, hh);
                if constexpr (f == 25) *(LAS float*)(size_t)(LDS_HALF + w * 128 + r * 4) = xg.rstd;
                if constexpr (f == 27) sfor<4>([&](auto i) { rs4[i.value] = lds_ld16(rb + 32 * i.value); });
                SB();
                if constexpr (f == 0) { mfma0(av, xf[0], wf[0][2]); TS(23); } else mfma(av, xf[f], wf[f % 3][2]);
                SB();
                if constexpr (f < 16) await<12 + 2 * (f < 11 ? f + 1 : 12) - (f + 2)>(xf[f + 1]);
                if constexpr (f == 16) { await<0>(xf[17]); sfor<10>([&](auto i) { landed(xf[18 + i.value]); }); sfor<8>([&](auto i) { landed(st[i.value]); }); }
                if constexpr (f == 8) TS(31); if constexpr (f == 14) TS(32); if constexpr (f == 15) TS(33); if constexpr (f == 18) TS(34); if constexpr (f == 22) TS(35);
            });
            SB();
            mfma_x(ak, wf[1][1], xa, wf[1][0], wf[1][2]); mfma(av, xa, wf[1][2]); mfma(aq, wf[1][0], xa);
            const float rstd = xg.rstd;
            TS(24);
            SETTLE3(aq, ak, av);
            const unsigned T = 4 * g + w, kvb = LB + L_KV + (j * 4 + (g >> 2)) * KVQ + (T & 15) * 4096, qo = LB + L_Q + (j * 16 + g) * QG + w * 2048;
            stg(pack8(ak, 0, rstd), rsW, l16, kvb); stg(pack8(ak, 1, rstd), rsW, l16 + 1024, kvb);
            sfor<16>([&](auto i) { av[i.value] *= rs4[i.value >> 2][i.value & 3]; });
            u32x4 v0 = pack8(av, 0, 1.f), v1 = pack8(av, 1, 1.f);
            if (r == 28) { v0 = (u32x4){0x3F803F80u, 0x3F803F80u, 0x3F803F80u, 0x3F803F80u}; v1 = v0; }
            stg(v0, rsW, l16 + 2048, kvb); stg(v1, rsW, l16 + 3072, kvb);
            const float qs = rstd * (0.18898223650461361f * 1.4426950408889634f);
            u32x4 q0 = pack8(aq, 0, qs), q1 = pack8(aq, 1, qs);
            if (hh) { q1[2] = 0u; q1[3] = 0u; }
            stg(q0, rsW, l16, qo); stg(q1, rsW, l16 + 1024, qo);
        }
        TS(2);
        publish(layer * 5 + 0, true);
        TS(3);
        FRESH();
        if (w == 0) poll(layer * 5 + 0);
        BAR();
        TS(4);
        FRESH();
        {
            u32x4 kv[4][4]; f32x16 O[4], S[2]; u32x4 p0, p1;
            sfor<16>([&](auto i) { def_v(kv[i.value >> 2][i.value & 3]); }); def_v(S[0]); def_v(S[1]); def_v(p0);
            unsigned kvs = sma<KVQ>(w, sma<4 * KVQ>(g, LB + L_KV));
            const unsigned qb = sma<QG>(j, sma<16 * QG>(g, LB + L_Q));
            acc_load2<224, 0>(l16, rsW, qb + w * 2048); kv_load2<0>(kv[0][0], kv[0][1], l16, rsW, kvs);
            acc_load2<232, 0>(l16, rsW, qb + (w ^ 1) * 2048); kv_load2<2048>(kv[0][2], kv[0][3], l16, rsW, kvs);
            acc_load2<240, 0>(l16, rsW, qb + (w ^ 2) * 2048); acc_load2<248, 0>(l16, rsW, qb + (w ^ 3) * 2048);
            kv_load(kv[1], l16, rsW, kvs + 4096);
            { float zero = 0.f; pin_v(zero);
              sfor<4>([&](auto c) { sfor<16>([&](auto i) { O[c.value][i.value] = zero; }); pin_v(O[c.value]); });
              p1 = (u32x4){f2u(zero), f2u(zero), f2u(zero), f2u(zero)}; kv[3][3] = p1; pin_v(p1); pin_v(kv[3][3]); }
            unsigned w2a = l16 + u * 1024, w2b = l16 + (u ^ 1) * 1024; const unsigned wn = sma<T_STRIDE>(n, WS_WT), w2s = sma<57344>(w, wn + T_2), w1s = sma<T_STRIDE>(j, WS_WT + T_1);
            kv_wait2<12>(kv[0][0], kv[0][1]);
            TS(22);
            mfmaz_qb<224>(S[0], kv[0][0]); mfma_qb<228>(S[0], kv[0][1]);
            kv_wait2<4>(kv[0][2], kv[0][3]);
            SETTLE1(S[0]);
#pragma unroll 1
            for (int it = 0; it < 4; ++it) {
                const int c15 = 1 - layer, c3 = c15 * it;
                SB();
                sfor<16>([&](auto ii) {
                    constexpr int i = ii.value, tau = i >> 2, c = i & 3, nx = (i + 1) & 15, ntau = nx >> 2, nc = nx & 3, pv = (i + 15) & 15;
                    f32x16& Sc = S[i & 1]; f32x16& Sn = S[nx & 1];
                    if constexpr (i == 3) kv_waitc<8, 30>(kv[1], c3);
                    if constexpr (i == 7) kv_wait<4>(kv[2]);
                    if constexpr (i == 11) {
                        kv_wait<8>(kv[3]);
                        const int q1 = min(4 * it + w, 12);
                        dma4(LDS_R1 + q1 * 4096, l16, rsW, w1s + q1 * 4096);
                        if (layer == 0) {
                            const int qo = min(4 * it + w, 13);
                            dma4(LDS_WO + qo * 4096, l16, rsW, wn + T_O + qo * 4096);
                            auto w2l = [&](auto q) { sfor<7>([&](auto k) { constexpr int kk = 7 * q.value + k.value;
                                acc_load1<8 * kk, (kk & 1) * 2048>(w2a, rsW, w2s + (kk >> 1) * 4096); acc_load1<8 * kk + 4, (kk & 1) * 2048>(w2b, rsW, w2s + (kk >> 1) * 4096); }); };
                            if (it == 0) w2l(std::integral_constant<int, 0>{}); else if (it == 1) w2l(std::integral_constant<int, 1>{});
                            else if (it == 2) w2l(std::integral_constant<int, 2>{}); else w2l(std::integral_constant<int, 3>{});
                        }
                        SB();
                    }
                    if constexpr (i == 15) kv_waitc<8, 26>(kv[0], c15);
                    auto ex = [&](int k, u32x4& p) {
                        const float e0 = __builtin_amdgcn_exp2f(Sc[4 * k]), e1 = __builtin_amdgcn_exp2f(Sc[4 * k + 1]), e2 = __builtin_amdgcn_exp2f(Sc[4 * k + 2]), e3 = __builtin_amdgcn_exp2f(Sc[4 * k + 3]);
                        p[2 * (k & 1)] = cvtpk(e0, e1); p[2 * (k & 1) + 1] = cvtpk(e2, e3);
                        pin_v(p);
                    };
                    mfmaz_qb<224 + 8 * nc>(Sn, kv[ntau][0]);
                    ex(0, p0); SB();
                    mfma_qb<228 + 8 * nc>(Sn, kv[ntau][1]);
                    ex(1, p0); SB();
                    mfma(O[pv & 3], kv[pv >> 2][3], p1);
                    if constexpr (i == 0 || i == 8) {
                        const unsigned so = kvs + (tau + 2) * 4096 + (i == 8 ? ((it + 1) >> 2) * WS_BYTES : 0u);
                        SB(); kv_load(kv[(tau + 2) & 3], l16, rsW, so); kv_load(kv[(tau + 3) & 3], l16, rsW, so + 4096); SB();
                    }
                    ex(2, p1); SB();
                    mfma(O[c], kv[tau][2], p0);
                    ex(3, p1); SB();
                });
                kvs += 16384;
            }
            mfma_n(O[3], kv[3][3], p1);
            kv_waitc<4, 22>(kv[1], 1 - layer);
            SETTLE4(O[0], O[1], O[2], O[3]);
            TS(5);
            FRESH();
            f32x16 o = O[0];
            reduce4(o, O[1], O[2], O[3]);
            float l = upper_half(o[12]);
            float amax = 0.f;
            sfor<16>([&](auto i) { amax = __builtin_fmaxf(amax, __builtin_fabsf(o[i.value])); });
            const bool ok = l >= 5.4e-20f && l <= 1.8e19f && amax <= 3.0e38f;
            if (!__all(ok)) {
                float mrun = -INFINITY;
                float zero = 0.f; pin_v(zero);
                sfor<16>([&](auto i) { o[i.value] = zero; });
                const u32x4 qa = ldg(rsW, l16, LB + L_Q + (g * 16 + j) * QG + w * 2048), qb2 = ldg(rsW, l16 + 1024, LB + L_Q + (g * 16 + j) * QG + w * 2048);
#pragma unroll 1
                for (int T = 0; T < 64; ++T) {
                    const unsigned so = LB + L_KV + (g * 4 + (T >> 4)) * KVQ + (T & 15) * 4096;
                    const u32x4 k0 = ldg(rsW, l16, so), k1 = ldg(rsW, l16 + 1024, so), v0 = ldg(rsW, l16 + 2048, so), v1 = ldg(rsW, l16 + 3072, so);
                    f32x16 s;
                    mfma0_safe(s, k0, qa); mfma_safe(s, k1, qb2);
                    float mt = s[0]; sfor<15>([&](auto i) { mt = __builtin_fmaxf(mt, s[i.value + 1]); });
                    const float mn = __builtin_fmaxf(mrun, half_max(mt)), al = __builtin_amdgcn_exp2f(mrun - mn);
                    mrun = mn;
                    sfor<16>([&](auto i) { o[i.value] *= al; s[i.value] = __builtin_amdgcn_exp2f(s[i.value] - mn); });
                    mfma_safe(o, v0, pack8(s, 0, 1.f)); mfma_safe(o, v1, pack8(s, 1, 1.f));
                }
                l = upper_half(o[12]);
            }
            const float rl = __builtin_amdgcn_rcpf(l);
            u32x4 o0 = pack8(o, 0, rl), o1 = pack8(o, 1, rl);
            if (hh) { o1[2] = 0u; o1[3] = 0u; }
            const unsigned ob = LB + L_O + (8 * j + 2 * w + (g >> 3)) * OH + (g & 7) * 2048;
            stg(o0, rsW, l16, ob); stg(o1, rsW, l16 + 1024, ob);
        }
        TS(6);
        publish(layer * 5 + 1, true);
        TS(7);
        FRESH();
        if (w == 0) poll(layer * 5 + 1);
        else stage(LDS_R1, T_1, 13, 21, 1, 3);
        BAR();
        TS(8);
        {
            u32x4 of[16];
            const unsigned ob = sma<OH>(w, sma<4 * OH>(m, sma<8 * OH>(g, LB + L_O)));
            auto oload = [&](auto s) { ald<(s.value & 3) * 1024>(of[s.value], l16, rsW, ob + (s.value >> 2) * 4096); };
            sfor<12>([&](auto s) { oload(s); });
            SB();
            const unsigned tA = LDS_WO + trl + 256 * hh + u * (8 * 28 * 64); unsigned tO = tA + u * IMG, tS = tA + (u ^ 1) * IMG;
            pin_v(tO); pin_v(tS);
            auto wfrag = [&](int img, int s) { const unsigned o = (img ? tS : tO) + (28 * (s >> 1) + 16 * (s & 1)) * 64; return trfrag(o, o + 512); };
            f32x16 a0, a1; u32x4 wf[3][2];
            sfor<2>([&](auto s) { wf[s.value][0] = wfrag(0, s.value); wf[s.value][1] = wfrag(1, s.value); });
            await<11>(of[0]);
            sfor<16>([&](auto ss) {
                constexpr int s = ss.value;
                SB();
                if constexpr (s == 0) mfma0(a1, wf[0][1], of[0]); else mfma_x(a1, wf[s % 3][1], of[s], wf[s % 3][0], wf[s % 3][0]);
                SB();
                if constexpr (s + 2 < 16) { wf[(s + 2) % 3][0] = wfrag(0, s + 2); wf[(s + 2) % 3][1] = wfrag(1, s + 2); }
                SB();
                if constexpr (s == 0) { mfma0(a0, wf[0][0], of[0]); TS(25); } else mfma(a0, wf[s % 3][0], of[s]);
                SB();
                if constexpr (s < 2) sfor<2>([&](auto i) { oload(std::integral_constant<int, 12 + 2 * s + i.value>{}); });
                if constexpr (s < 15) await<(s == 0 ? 14 : 16) - s - 2>(of[s + 1]);
            });
            SETTLE2(a0, a1);
            TS(26);
            sfor<4>([&](auto k) { lds_st16(LDS_SCR16 + w * 4096 + k.value * 1024 + l16, (f32x4){a1[4 * k.value], a1[4 * k.value + 1], a1[4 * k.value + 2], a1[4 * k.value + 3]}); });
            sfor<16>([&](auto i) { resid[i.value] += a0[i.value]; });
            BAR();
            sfor<4>([&](auto k) { f32x4 v = lds_ld16(LDS_SCR16 + (w ^ 1) * 4096 + k.value * 1024 + l16); sfor<4>([&](auto e) { resid[4 * k.value + e.value] += v[e.value]; }); });
            publish_x(LB + L_X1);
        }
        TS(9);
        publish(layer * 5 + 2, false);
        TS(10);
        FRESH();
        if (w == 0) poll(layer * 5 + 2);
        else stage(LDS_R1, T_1, 21, 25, 1, 3);
        BAR();
        TS(11);
        {
            u32x4 xf[28]; u32x2 st[8];
            const unsigned tb = LB + L_X1 + (4 * g + w) * XT;
            auto xload = [&](int f) { xf[f] = ldg(rsW, l16 + (f & 3) * 1024, tb + (f >> 2) * 4096); };
            sfor<12>([&](auto f) { xload(f.value); });
            SB();
            unsigned wA = LDS_R1 + l16, wC = wA + 2 * IMG, wH = LDS_HALF + (hh * 16 + (r & 15)) * 16, wG = LDS_AUG + r * 16;
            pin_v(wA); pin_v(wC); pin_v(wH); pin_v(wG);
            auto wfrag = [&](int img, int f) { return f == 28 ? lds_frag(wG + img * 512) : img == 3 ? lds_frag(wH + f * 512) : img == 2 ? lds_frag(wC + f * 1024) : lds_frag(wA + img * IMG + f * 1024); };
            f32x16 h0, h1, h2, h3; u32x4 wf[3][2], wp[8], xa; XAug xg;
            sfor<2>([&](auto f) { wf[f.value][0] = wfrag(0, f.value); wf[f.value][1] = wfrag(1, f.value); });
            sfor<28>([&](auto ff) {
                constexpr int f = ff.value;
                SB();
                if constexpr (f == 0) mfma0(h0, wf[0][0], xf[0]); else mfma_x(h0, wf[f % 3][0], xf[f], wf[f % 3][1], wf[f % 3][1]);
                SB();
                if constexpr (f + 2 <= 28) { wf[(f + 2) % 3][0] = wfrag(0, f + 2); wf[(f + 2) % 3][1] = wfrag(1, f + 2); }
                if constexpr (f == 27) { BAR(); sfor<4>([&](auto i) { wp[i.value] = wfrag(2 + (i.value & 1), i.value >> 1); }); }
                SB();
                if constexpr (f == 0) { mfma0(h1, wf[0][1], xf[0]); TS(27); } else mfma(h1, wf[f % 3][1], xf[f]);
                SB();
                if constexpr (f % 2 == 0 && f < 16) { xload(12 + f); xload(13 + f); }
                if constexpr (f >= 16 && f < 20) sfor<2>([&](auto i) { st[2 * (f - 16) + i.value] = __builtin_amdgcn_raw_buffer_load_b64(rsW, (8 * hh + 2 * (f - 16) + i.value) * 256 + r * 8, tb + 28672, 16); });
            });
            TS(28);
            sfor<4>([&](auto i) { wp[4 + i.value] = wfrag(2 + (i.value & 1), 2 + (i.value >> 1)); });
            sfor<8>([&](auto qq) {
                constexpr int q = qq.value;
                SB();
                if constexpr (q < 2) mfma0(q ? h3 : h2, wp[q], xf[0]); else mfma(q & 1 ? h3 : h2, wp[q], xf[q >> 1]);
                SB();
                xaug_step<q>(xg, st, xa, hh);
                if constexpr (q == 3) sfor<4>([&](auto i) { wp[i.value] = wfrag(2 + (i.value & 1), 4 + (i.value >> 1)); });
            });
            xaug_step<8>(xg, st, xa, hh);
            float c1 = xg.rstd * 1.4426950408889634f; pin_v(c1);
            sfor<4>([&](auto i) { wp[4 + i.value] = wfrag(2 + (i.value & 1), 6 + (i.value >> 1)); });
            SB();
            mfma_n(h0, wf[1][0], xa); mfma(h1, wf[1][1], xa);
            mfma(h2, wp[0], xf[4]); mfma(h3, wp[1], xf[4]); mfma(h2, wp[2], xf[5]); mfma(h3, wp[3], xf[5]);
            SB();
            const unsigned ab = LB + L_AF + (16 * g + 4 * w + (j >> 2)) * AQ + (j & 3) * 7168;
            u32x4 pk[4]; float mt[4], me[2], md[2];
            sfor<4>([&](auto i) { def_v(mt[i.value]); }); sfor<2>([&](auto i) { def_v(me[i.value]); def_v(md[i.value]); });
            auto mish_pk = [&](float a, float b2) {
                const f32x2 tt = (f32x2){a, b2} * c1, e = {__builtin_amdgcn_exp2f(tt[0]), __builtin_amdgcn_exp2f(tt[1])};
                const f32x2 d = __builtin_elementwise_fma(e, __builtin_elementwise_fma(e, (f32x2){-0.5f, -0.5f}, (f32x2){-1.f, -1.f}), (f32x2){-1.f, -1.f});
                const f32x2 y = __builtin_elementwise_fma(tt, (f32x2){__builtin_amdgcn_rcpf(d[0]), __builtin_amdgcn_rcpf(d[1])}, tt);
                return cvtpk(y[0], y[1]);
            };
            SETTLE1(h0);
            mt[0] = h0[0] * c1; pin_v(mt[0]);
            sfor<11>([&](auto pp) {
                constexpr int p = pp.value, f = 6 + 2 * p, cur = 4 * ((p + 1) & 1), nxt = 4 * (p & 1), n1 = 2 * p + 1, n2 = 2 * p + 2;
                sfor<4>([&](auto i) { constexpr int nf = f + 2 + (i.value >> 1); if constexpr (nf <= 28) wp[nxt + i.value] = wfrag(2 + (i.value & 1), nf); });
                SB();
                unsigned res;
                mfma4_mish<(p > 0)>(h2, h3, wp[cur], wp[cur + 1], wp[cur + 2], wp[cur + 3], xf[f], xf[f + 1], mt[(2 * p) & 3], mt[(2 * p + 1) & 3], mt[(2 * p + 2) & 3], mt[(2 * p + 3) & 3],
                                    me[0], me[1], md[0], md[1], n1 < 16 ? h0[n1 & 15] : h1[n1 & 15], n2 < 16 ? h0[n2 & 15] : h1[n2 & 15], c1, res);
                if constexpr (p > 0) { constexpr int pr = p - 1; if constexpr ((pr & 3) == 0) def_v(pk[pr >> 2]); pk[pr >> 2][pr & 3] = res; if constexpr ((pr & 3) == 3) stg(pk[pr >> 2], rsW, l16, ab + (pr >> 2) * 1024); }
                SB();
            });
            mfma_x(h2, wp[0], xa, wp[1], wp[1]); mfma(h3, wp[1], xa);
            TS(29);
            pk[2][2] = mish_pk(h1[4], h1[5]); pk[2][3] = mish_pk(h1[6], h1[7]);
            stg(pk[2], rsW, l16, ab + 2 * 1024);
            sfor<4>([&](auto k) { pk[3][k.value] = mish_pk(h1[8 + 2 * k.value], h1[9 + 2 * k.value]); });
            stg(pk[3], rsW, l16, ab + 3 * 1024);
            SETTLE1(h2);
            sfor<2>([&](auto s) { sfor<4>([&](auto k) { pk[s.value][k.value] = mish_pk(h2[8 * s.value + 2 * k.value], h2[8 * s.value + 2 * k.value + 1]); }); stg(pk[s.value], rsW, l16, ab + (4 + s.value) * 1024); });
            SETTLE1(h3);
            sfor<4>([&](auto k) { pk[2][k.value] = mish_pk(h3[2 * k.value], h3[2 * k.value + 1]); });
            stg(pk[2], rsW, l16, ab + 6 * 1024);
        }
        TS(12);
        publish(layer * 5 + 3, false);
        TS(13);
        FRESH();
        if (w == 0) poll(layer * 5 + 3);
        BAR();
        TS(14);
        {
            u32x4 af[28][2];
            const unsigned ab0 = sma<8 * AQ>(m, sma<16 * AQ>(g, sma<AQ>(w, LB + L_AF))), ab = sma<4 * AQ>(t, ab0), ab2 = sma<4 * AQ>(t ^ 1, ab0);
            auto aload = [&](auto k) { ald<(k.value & 3) * 1024>(af[k.value][0], l16, rsW, ab + (k.value >> 2) * 4096); ald<(k.value & 3) * 1024>(af[k.value][1], l16, rsW, ab2 + (k.value >> 2) * 4096); };
            sfor<6>([&](auto k) { aload(k); });
            SB();
            f32x16 y[4];
            const unsigned wqs = sma<WS_BYTES>(layer, sma<T_STRIDE>(j, WS_WT + T_Q));
            await<10>(af[0][1]); landed(af[0][0]);
            sfor<28>([&](auto kk) {
                constexpr int k = kk.value;
                SB();
                if constexpr (k == 0) mfma0_wa<0>(y[0], af[0][0]); else mfma_wa<8 * k>(y[0], af[k][0]);
                SB();
                if constexpr (k + 6 < 28) aload(std::integral_constant<int, k + 6>{});
                SB();
                if constexpr (k == 0) mfma0_wa<4>(y[1], af[0][0]); else mfma_wa<8 * k + 4>(y[1], af[k][0]);
                SB();
                if constexpr (k >= 24) {
                    const int q = min(4 * (k - 24) + w, 12); dma4(LDS_R1 + q * 4096, l16, rsW, wqs + q * 4096);
                }
                SB();
                if constexpr (k == 0) { mfma0_wa<0>(y[2], af[0][1]); mfma0_wa<4>(y[3], af[0][1]); TS(30); } else { mfma_wa<8 * k>(y[2], af[k][1]); mfma_wa<8 * k + 4>(y[3], af[k][1]); }
                SB();
                constexpr int issued = 2 * (k + 7 < 28 ? k + 7 : 28) + (k >= 24 ? 4 * (k - 23) : 0);
                if constexpr (k < 27) { await<issued - 2 * k - 4>(af[k + 1][1]); landed(af[k + 1][0]); }
            });
            SETTLE4(y[0], y[1], y[2], y[3]);
            TS(15);
            FRESH();
            f32x16 o = y[0];
            reduce4(o, y[1], y[2], y[3]);
            sfor<16>([&](auto i) { resid[i.value] = __builtin_fmaf(o[i.value], 0.6931471805599453f, resid[i.value]); });
        }
        if (layer == 0) {
            publish_x(LB + L_X2);
            TS(16);
            publish(4, false);
            TS(17);
            if (w == 0) poll(4);
            else stage(LDS_R1, T_Q, 13, 21, 1, 3);
            TSFLUSH();
            BAR();
        } else {
            const u32x4 p0 = pack8(resid, 0, 1.f), p1 = pack8(resid, 1, 1.f);
            GAS char* op = (GAS char*)out + ((4 * g + 2 * m + t) * 32 + r) * 896 + 112 * n + 64 * u + 16 * hh;
            auto a = __builtin_amdgcn_permlane32_swap(p0[0], p0[2], false, false), c = __builtin_amdgcn_permlane32_swap(p0[1], p0[3], false, false);
            *(GAS u32x4*)op = (u32x4){a[0], c[0], a[1], c[1]};
            a = __builtin_amdgcn_permlane32_swap(p1[0], p1[2], false, false); c = __builtin_amdgcn_permlane32_swap(p1[1], p1[3], false, false);
            if (u == 0 || hh == 0) *(GAS u32x4*)(op + 32) = (u32x4){a[0], c[0], a[1], c[1]};
            if (b == 0 && threadIdx.x == 0) __hip_atomic_store((GAS u64*)ws, tag, RLX_AGENT);
            WAIT_VM0();
            u64 mn_ = ~0ull, mx_ = 0;
            for (int i = 0; i < 4; ++i) { u64 v = __hip_atomic_load((GAS u64*)(ws + (900u << 10)) + i * 64 + lane, RLX_AGENT); mn_ = v < mn_ ? v : mn_; mx_ = v > mx_ ? v : mx_; }
            for (int o = 32; o; o >>= 1) { u64 a = __shfl_xor(mn_, o), c = __shfl_xor(mx_, o); mn_ = a < mn_ ? a : mn_; mx_ = c > mx_ ? c : mx_; }
            u64 gap_ = mn_ - pend_; if (gap_ > 2000) gap_ = 0;
            const u64 end_ = mn_ + 7000 + 0 * (mx_ - mn_) + 0 * gap_;
            while (__builtin_amdgcn_s_memrealtime() < end_) __builtin_amdgcn_s_sleep(1);
            if (b == 0 && threadIdx.x == 0) __hip_atomic_store((GAS u64*)(ws + 512), end_, RLX_AGENT);
        }
    }
}

extern "C" void kernel_launch(void* const* d_in, const int* in_sizes, int n_in, void* d_out, int out_size, void* d_ws, size_t ws_size, hipStream_t stream) {
    (void)hipFuncSetAttribute((const void*)mega, hipFuncAttributeMaxDynamicSharedMemorySize, LDS_BYTES);
    mega<<<256, 256, LDS_BYTES, stream>>>((const u16*)d_in[0], (const u16*)d_in[1], (const u16*)d_in[2], (const u16*)d_in[4], (const u16*)d_in[5], (const u16*)d_in[6],
                                          (u16*)d_out, (char*)d_ws);
}
```

```cpp
#include <hip/hip_runtime.h>
#include <utility>

#define DI __device__ __forceinline__
#define LAS __attribute__((address_space(3)))
#define GAS __attribute__((address_space(1)))
typedef float f32x16 __attribute__((ext_vector_type(16)));
typedef float f32x4 __attribute__((ext_vector_type(4)));
typedef float f32x2 __attribute__((ext_vector_type(2)));
typedef unsigned u32x4 __attribute__((ext_vector_type(4)));
typedef unsigned u32x16 __attribute__((ext_vector_type(16)));
typedef unsigned u32x2 __attribute__((ext_vector_type(2)));
typedef short v4i16 __attribute__((ext_vector_type(4)));
typedef __bf16 bf16x2 __attribute__((ext_vector_type(2)));
typedef __amdgpu_buffer_rsrc_t rsrc_t;
typedef unsigned long long u64;
typedef unsigned short u16;

constexpr unsigned WS_FLAGS = 4096;
constexpr unsigned WS_X0 = 1u << 20;
constexpr unsigned WS_WT = 4u << 20;
constexpr unsigned T_Q = 0, T_1 = 86016, T_O = T_1 + 100352, T_2 = T_O + 57344, T_CQ = T_2 + 229376, T_C1 = T_CQ + 6144, T_STRIDE = 491520;
constexpr unsigned WS_DATA = 12u << 20;
constexpr unsigned XT = 32768;
constexpr unsigned KVQ = 65536;
constexpr unsigned QG = 8192;
constexpr unsigned OH = 16384;
constexpr unsigned AQ = 28672;
constexpr unsigned L_Q = 0, L_KV = 3u << 20, L_O = 8u << 20, L_X1 = 11u << 20, L_AF = 14u << 20, L_X2 = 22u << 20, L_STRIDE = 25u << 20;
constexpr unsigned WS_BYTES = WS_DATA + 2 * L_STRIDE;
constexpr unsigned IMG = 28672;
constexpr unsigned LDS_WO = 0;
constexpr unsigned LDS_R1 = 57600;
constexpr unsigned LDS_HALF = LDS_R1 + 3 * IMG;
constexpr unsigned LDS_Q = LDS_HALF + 1024;
constexpr unsigned LDS_AUG = 160256;
constexpr unsigned LDS_SCR48 = LDS_AUG - 49152;
constexpr unsigned LDS_SCR16 = LDS_AUG - 16384;
constexpr unsigned LDS_BYTES = 163840;

constexpr int CODE_BYTES = 39700;
constexpr int PRE_5 = 14;
constexpr int PRE_2 = 18;
constexpr int PRE_IN = 14;
constexpr int PGAP = 1;
constexpr int LDAUX = 16;
constexpr int WQA = 9;
constexpr int QB2 = 1;
constexpr int QC1 = 5;
constexpr int QC2 = 8;
constexpr int PRE_X = 22;

#define SB() __builtin_amdgcn_sched_barrier(0)
#define BAR() asm volatile("s_waitcnt lgkmcnt(0)\n\ts_barrier" ::: "memory")
#define BAR_VM() do { __builtin_amdgcn_s_waitcnt(0x0070); asm volatile("s_waitcnt vmcnt(0) lgkmcnt(0)\n\ts_barrier" ::: "memory"); } while (0)

template <class F, int... Is> DI void sfor_seq(F&& f, std::integer_sequence<int, Is...>) { (f(std::integral_constant<int, Is>{}), ...); }
template <int N, class F> DI void sfor(F&& f) { sfor_seq(f, std::make_integer_sequence<int, N>{}); }

DI void mfma(f32x16& c, const u32x4& a, const u32x4& b) { asm volatile("v_mfma_f32_32x32x16_bf16 %0, %1, %2, %0" : "+v"(c) : "v"(a), "v"(b)); }
DI void mfma0(f32x16& c, const u32x4& a, const u32x4& b) { asm volatile("v_mfma_f32_32x32x16_bf16 %0, %1, %2, 0" : "=&v"(c) : "v"(a), "v"(b)); }
template <int R> DI void mfma_wa(f32x16& c, const u32x4& b) { asm volatile("v_mfma_f32_32x32x16_bf16 %0, a[%c2:%c3], %1, %0" : "+v"(c) : "v"(b), "i"(R), "i"(R + 3)); }
template <int R> DI void mfma0_wa(f32x16& c, const u32x4& b) { asm volatile("v_mfma_f32_32x32x16_bf16 %0, a[%c2:%c3], %1, 0" : "=&v"(c) : "v"(b), "i"(R), "i"(R + 3)); }
DI void mfma0_safe(f32x16& c, const u32x4& a, const u32x4& b) { asm volatile("s_nop 1\n\tv_mfma_f32_32x32x16_bf16 %0, %1, %2, 0\n\ts_nop 11" : "=&v"(c) : "v"(a), "v"(b)); }
DI void mfma_safe(f32x16& c, const u32x4& a, const u32x4& b) { asm volatile("s_nop 1\n\tv_mfma_f32_32x32x16_bf16 %0, %1, %2, %0\n\ts_nop 11" : "+v"(c) : "v"(a), "v"(b)); }
#define SETTLE2(a, b) asm volatile("s_nop 11" : "+v"(a), "+v"(b))
#define SETTLE3(a, b, c) asm volatile("s_nop 11" : "+v"(a), "+v"(b), "+v"(c))

DI unsigned cvtpk(float lo, float hi) { f32x2 v = {lo, hi}; bf16x2 b = __builtin_convertvector(v, bf16x2); return __builtin_bit_cast(unsigned, b); }
DI float bflo(unsigned u) { return __builtin_bit_cast(float, u << 16); }
DI float bfhi(unsigned u) { return __builtin_bit_cast(float, u & 0xffff0000u); }
DI float dot2(unsigned a, unsigned b, float c) { return __builtin_amdgcn_fdot2_f32_bf16(__builtin_bit_cast(bf16x2, a), __builtin_bit_cast(bf16x2, b), c, false); }
DI float u2f(unsigned u) { return __builtin_bit_cast(float, u); }
DI unsigned f2u(float f) { return __builtin_bit_cast(unsigned, f); }
DI float half_sum(float v) { auto r = __builtin_amdgcn_permlane32_swap(f2u(v), f2u(v), false, false); return u2f(r[0]) + u2f(r[1]); }
DI float half_max(float v) { auto r = __builtin_amdgcn_permlane32_swap(f2u(v), f2u(v), false, false); return __builtin_fmaxf(u2f(r[0]), u2f(r[1])); }
DI float upper_half(float v) { auto r = __builtin_amdgcn_permlane32_swap(f2u(v), f2u(v), false, false); return u2f(r[1]); }
DI u32x4 ldg(rsrc_t rs, unsigned voff, unsigned soff) { return __builtin_amdgcn_raw_buffer_load_b128(rs, voff, soff, LDAUX); }
DI void stg(u32x4 v, rsrc_t rs, unsigned voff, unsigned soff) { __builtin_amdgcn_raw_buffer_store_b128(v, rs, voff, soff, 16); asm volatile("s_nop 1" :: "v"(v) : "memory"); }
DI void dma_nt(unsigned lds, unsigned voff, rsrc_t rs, unsigned soff) {
    asm volatile("s_mov_b32 m0, %0\n\ts_nop 0\n\tbuffer_load_dwordx4 %1, %2, %3 offen nt lds" :: "s"(lds), "v"(voff), "s"(rs), "s"(soff) : "memory"); }
DI void dma4(unsigned lds, unsigned voff, rsrc_t rs, unsigned soff) {
    asm volatile("s_mov_b32 m0, %0\n\ts_nop 0\n\tbuffer_load_dwordx4 %1, %2, %3 offen sc1 lds\n\tbuffer_load_dwordx4 %1, %2, %3 offen offset:1024 sc1 lds\n\t"
                 "buffer_load_dwordx4 %1, %2, %3 offen offset:2048 sc1 lds\n\tbuffer_load_dwordx4 %1, %2, %3 offen offset:3072 sc1 lds" :: "s"(lds), "v"(voff), "s"(rs), "s"(soff) : "memory"); }
template <unsigned C> DI unsigned sma(unsigned a, unsigned b) { unsigned r; asm("s_mul_i32 %0, %1, %2\n\ts_add_i32 %0, %0, %3" : "=&s"(r) : "s"(a), "i"(C), "s"(b) : "scc"); return r; }
DI void pin_v(u32x4& x) { asm volatile("" : "+v"(x)); }
DI void pin_v(unsigned& x) { asm volatile("" : "+v"(x)); }
DI void pin_v(float& x) { asm volatile("" : "+v"(x)); }
template <class T> DI void def_v(T& x) { asm volatile("" : "=v"(x)); }
DI void pin_v(f32x16& x) { asm volatile("" : "+v"(x)); }
template <int OFF> DI void ald(u32x4& d, unsigned voff, rsrc_t rs, unsigned soff) {
    asm volatile("buffer_load_dwordx4 %0, %1, %2, %3 offen offset:%c4 sc1" : "=v"(d) : "v"(voff), "s"(rs), "s"(soff), "i"(OFF) : "memory"); }
template <int OFF> DI void ald(u32x2& d, unsigned voff, rsrc_t rs, unsigned soff) {
    asm volatile("buffer_load_dwordx2 %0, %1, %2, %3 offen offset:%c4 sc1" : "=v"(d) : "v"(voff), "s"(rs), "s"(soff), "i"(OFF) : "memory"); }
template <int N, class T> DI void await(T& d) { asm volatile("s_waitcnt vmcnt(%c1)" : "+v"(d) : "i"(N) : "memory"); }
template <class T> DI void landed(T& d) { asm volatile("" : "+v"(d) :: "memory"); }
DI void asm_settle(f32x16& a, f32x16& b) { asm volatile("s_nop 4" : "+v"(a), "+v"(b)); }
template <bool F> DI void pin16(u32x16& x) { if constexpr (F) asm volatile("; first" : "+v"(x)); else asm volatile("; later" : "+v"(x)); }
DI void pin_s(unsigned& x) { asm volatile("" : "+s"(x)); }
template <int N, int PRE> DI void window(u32x2 tag, unsigned vflag, unsigned sflag, unsigned vpoll, unsigned spoll, rsrc_t rs, int w, int& left) {
    u32x2 v; def_v(v);
    asm volatile("s_waitcnt vmcnt(%c9) lgkmcnt(0)\n\ts_barrier\n\ts_cmp_lg_u32 %8, 0\n\ts_cbranch_scc1 2f\n\t"
                 "buffer_store_dwordx2 %2, %3, %7, %4 offen sc1\n\ts_sleep %c10\n"
                 "1:\n\tbuffer_load_dwordx2 %1, %5, %7, %6 offen sc1\n\ts_waitcnt vmcnt(0)\n\tv_cmp_ne_u64 vcc, %1, %2\n\ts_nop 0\n\ts_cbranch_vccz 2f\n\t"
                 "s_sleep %c11\n\ts_sub_i32 %0, %0, 1\n\ts_cmp_gt_i32 %0, 0\n\ts_cbranch_scc1 1b\n2:"
                 : "+s"(left) : "v"(v), "v"(tag), "v"(vflag), "s"(sflag), "v"(vpoll), "s"(spoll), "s"(rs), "s"(w), "i"(N), "i"(PRE), "i"(PGAP) : "memory", "scc", "vcc");
}
DI void poll_only(u32x2 tag, unsigned vpoll, unsigned spoll, rsrc_t rs, int w, int& left) {
    u32x2 v; def_v(v);
    asm volatile("s_cmp_lg_u32 %6, 0\n\ts_cbranch_scc1 2f\n"
                 "1:\n\tbuffer_load_dwordx2 %1, %3, %5, %4 offen sc1\n\ts_waitcnt vmcnt(0)\n\tv_cmp_ne_u64 vcc, %1, %2\n\ts_nop 0\n\ts_cbranch_vccz 2f\n\t"
                 "s_sleep 1\n\ts_sub_i32 %0, %0, 1\n\ts_cmp_gt_i32 %0, 0\n\ts_cbranch_scc1 1b\n2:"
                 : "+s"(left) : "v"(v), "v"(tag), "v"(vpoll), "s"(spoll), "s"(rs), "s"(w) : "memory", "scc", "vcc");
}
template <int N> DI void bar_vm() { asm volatile("s_waitcnt vmcnt(%c0) lgkmcnt(0)\n\ts_barrier" :: "i"(N) : "memory"); }
DI void mfma_x(f32x16& c, const u32x4& a, const u32x4& b, const u32x4& d1, const u32x4& d2) { asm volatile("v_mfma_f32_32x32x16_bf16 %0, %1, %2, %0" : "+v"(c) : "v"(a), "v"(b), "v"(d1), "v"(d2)); }
template <bool CVT> DI void mfma4_mish(f32x16& c2, f32x16& c3, const u32x4& w0, const u32x4& w1, const u32x4& w2, const u32x4& w3, const u32x4& b0, const u32x4& b1, const u32x4& b2, const u32x4& b3,
                                       float& t0, float& t1, float& t2, float& t3, float& e0, float& e1, float& d0, float& d1, float xa, float xb, float c1, unsigned& r) {
#define MISH4_HEAD "v_mfma_f32_32x32x16_bf16 %0, %11, %15, %0\n\tv_exp_f32 %6, %2\n\tv_fma_f32 %9, %7, %9, -1.0\n\tv_fmac_f32 %4, %4, %8\n\tv_rcp_f32 %9, %9\n\t" \
                   "v_mfma_f32_32x32x16_bf16 %1, %12, %16, %1\n\tv_mul_f32 %3, %21, %19\n\tv_fma_f32 %8, %6, -0.5, -1.0\n\tv_exp_f32 %7, %3\n\tv_fma_f32 %8, %6, %8, -1.0\n\tv_fmac_f32 %5, %5, %9\n\t" \
                   "v_mfma_f32_32x32x16_bf16 %0, %13, %17, %0\n\tv_rcp_f32 %8, %8\n\tv_fma_f32 %9, %7, -0.5, -1.0\n\t"
#define MISH4_TAIL "v_mul_f32 %4, %21, %20\n\tv_mfma_f32_32x32x16_bf16 %1, %14, %18, %1"
    if constexpr (CVT) asm volatile(MISH4_HEAD "v_cvt_pk_bf16_f32 %10, %4, %5\n\t" MISH4_TAIL
        : "+v"(c2), "+v"(c3), "+v"(t0), "+v"(t1), "+v"(t2), "+v"(t3), "+v"(e0), "+v"(e1), "+v"(d0), "+v"(d1), "=&v"(r)
        : "v"(w0), "v"(w1), "v"(w2), "v"(w3), "v"(b0), "v"(b1), "v"(b2), "v"(b3), "v"(xa), "v"(xb), "v"(c1));
    else asm volatile(MISH4_HEAD MISH4_TAIL
        : "+v"(c2), "+v"(c3), "+v"(t0), "+v"(t1), "+v"(t2), "+v"(t3), "+v"(e0), "+v"(e1), "+v"(d0), "+v"(d1), "=&v"(r)
        : "v"(w0), "v"(w1), "v"(w2), "v"(w3), "v"(b0), "v"(b1), "v"(b2), "v"(b3), "v"(xa), "v"(xb), "v"(c1));
}
template <int OFF> DI void ald(float& d, unsigned voff, rsrc_t rs, unsigned soff) {
    asm volatile("buffer_load_dword %0, %1, %2, %3 offen offset:%c4 sc1" : "+v"(d) : "v"(voff), "s"(rs), "s"(soff), "i"(OFF) : "memory"); }
template <int OFF> DI void ald_plain(u32x2& d, unsigned voff, rsrc_t rs) {
    asm volatile("buffer_load_dwordx2 %0, %1, %2, 0 offen offset:%c3" : "=v"(d) : "v"(voff), "s"(rs), "i"(OFF) : "memory"); }
DI void ald_x4(u32x4& d, unsigned voff, rsrc_t rs, unsigned soff) { asm volatile("buffer_load_dwordx4 %0, %1, %2, %3 offen nt" : "=v"(d) : "v"(voff), "s"(rs), "s"(soff) : "memory"); }
struct XAug { float a, b, c, d, e, f, g, h, rstd, c1; };
template <int STEP> DI void xaug_step(XAug& x, const u32x2 (&st)[8], u32x4& xa) {
    if constexpr (STEP == 0) asm volatile("v_add_f32 %0, %4, %5\n\tv_add_f32 %1, %6, %7\n\tv_add_f32 %2, %8, %9\n\tv_add_f32 %3, %10, %11" : "=&v"(x.a), "=&v"(x.b), "=&v"(x.c), "=&v"(x.d)
        : "v"(st[0][0]), "v"(st[1][0]), "v"(st[2][0]), "v"(st[3][0]), "v"(st[4][0]), "v"(st[5][0]), "v"(st[6][0]), "v"(st[7][0]));
    if constexpr (STEP == 1) asm volatile("v_add_f32 %0, %4, %5\n\tv_add_f32 %1, %6, %7\n\tv_add_f32 %2, %8, %9\n\tv_add_f32 %3, %10, %11" : "=&v"(x.e), "=&v"(x.f), "=&v"(x.g), "=&v"(x.h)
        : "v"(st[0][1]), "v"(st[1][1]), "v"(st[2][1]), "v"(st[3][1]), "v"(st[4][1]), "v"(st[5][1]), "v"(st[6][1]), "v"(st[7][1]));
    if constexpr (STEP == 2) asm volatile("v_add_f32 %0, %0, %4\n\tv_add_f32 %1, %1, %5\n\tv_add_f32 %2, %2, %6\n\tv_add_f32 %3, %3, %7" : "+v"(x.a), "+v"(x.c), "+v"(x.e), "+v"(x.g) : "v"(x.b), "v"(x.d), "v"(x.f), "v"(x.h));
    if constexpr (STEP == 3) asm volatile("v_add_f32 %0, %0, %4\n\tv_add_f32 %1, %1, %5\n\tv_mov_b32 %2, %0\n\tv_mov_b32 %3, %1\n\ts_nop 0" : "+v"(x.a), "+v"(x.e), "=&v"(x.b), "=&v"(x.f) : "v"(x.c), "v"(x.g));
    if constexpr (STEP == 4) asm volatile("s_nop 0\n\tv_permlane32_swap_b32 %0, %2\n\tv_permlane32_swap_b32 %1, %3\n\tv_add_f32 %0, %0, %2\n\tv_add_f32 %1, %1, %3" : "+v"(x.a), "+v"(x.e), "+v"(x.b), "+v"(x.f));
    if constexpr (STEP == 5) asm volatile("v_mul_f32 %0, 0xbb124925, %0\n\tv_mul_f32 %1, 0x3b124925, %1\n\tv_fma_f32 %1, -%0, %0, %1\n\tv_max_f32 %1, 0, %1\n\tv_add_f32 %1, 0x3727c5ac, %1" : "+v"(x.a), "+v"(x.e));
    if constexpr (STEP == 6) asm volatile("v_rsq_f32 %0, %4\n\tv_cvt_pk_bf16_f32 %1, %3, 0\n\tv_lshlrev_b32 %1, 16, %1\n\tv_mul_f32 %2, %4, %0" : "=&v"(x.rstd), "=&v"(x.b), "=&v"(x.c) : "v"(x.a), "v"(x.e));
    if constexpr (STEP == 7) asm volatile("v_cvt_pk_bf16_f32 %1, %4, %2\n\tv_sub_f32 %0, %3, %4\n\tv_cvt_pk_bf16_f32 %0, %4, %0\n\tv_and_b32 %3, 0xffff0000, %1\n\tv_sub_f32 %2, %2, %3" : "=&v"(x.d), "=&v"(x.f), "+v"(x.c), "+v"(x.a) : "v"(x.b));
    if constexpr (STEP == 8) { unsigned d0, d1, d2;
        asm volatile("v_cvt_pk_bf16_f32 %2, %6, 0\n\tv_mul_f32 %3, 0x3fb8aa3b, %7\n\tv_cndmask_b32 %0, 0, %4, %8\n\tv_cndmask_b32 %1, 0, %5, %8\n\tv_cndmask_b32 %2, 0, %2, %8"
                     : "=&v"(d0), "=&v"(d1), "=&v"(d2), "=&v"(x.c1) : "v"(x.d), "v"(x.f), "v"(x.c), "v"(x.rstd), "s"(0xffffffffull));
        xa = (u32x4){d0, d1, d2, 0u}; }
}
DI u32x4 lds_frag(unsigned a) { return *(LAS u32x4*)(size_t)a; }
DI u32x4 trfrag(unsigned a1, unsigned a2) {
    v4i16 lo = __builtin_amdgcn_ds_read_tr16_b64_v4i16((LAS v4i16*)(size_t)a1), hi = __builtin_amdgcn_ds_read_tr16_b64_v4i16((LAS v4i16*)(size_t)a2);
    u32x2 l = __builtin_bit_cast(u32x2, lo), h = __builtin_bit_cast(u32x2, hi);
    return (u32x4){l[0], l[1], h[0], h[1]}; }
DI void lds_st16(unsigned a, f32x4 v) { *(LAS f32x4*)(size_t)a = v; }
DI f32x4 lds_ld16(unsigned a) { return *(LAS f32x4*)(size_t)a; }
constexpr int xrow(int f) { return (f % 7 == 6) ? 112 * (f / 7) + 48 : 112 * (f / 7) + (f % 7 >= 3 ? 56 : 0) + 16 * ((f % 7) % 3); }
DI u32x4 pack8(const f32x16& v, int s, float k) {
    return (u32x4){cvtpk(v[8 * s] * k, v[8 * s + 1] * k), cvtpk(v[8 * s + 2] * k, v[8 * s + 3] * k), cvtpk(v[8 * s + 4] * k, v[8 * s + 5] * k), cvtpk(v[8 * s + 6] * k, v[8 * s + 7] * k)}; }
DI unsigned hi_lo(float v, float& hi) { unsigned h = cvtpk(v, 0.f); hi = bflo(h); return h & 0xffffu; }

constexpr int F2D = 5;
#define ATTN_ASM \
    "s_add_u32 %[kvo], %[kvo], 4096\n\t" \
    "s_and_b32 %[kvo], %[kvo], 0xffff\n\t" \
    "s_add_u32 %[t0], %[kvs], %[kvo]\n\t" \
    "buffer_load_dwordx4 v[112:115], %[l16], %[rs], %[t0] offen offset:0 sc1\n\t" \
    "buffer_load_dwordx4 v[116:119], %[l16], %[rs], %[t0] offen offset:1024 sc1\n\t" \
    "buffer_load_dwordx4 v[120:123], %[l16], %[rs], %[t0] offen offset:2048 sc1\n\t" \
    "buffer_load_dwordx4 v[124:127], %[l16], %[rs], %[t0] offen offset:3072 sc1\n\t" \
    "ds_read_b128 a[224:227], %[q0]\n\t" \
    "ds_read_b128 a[228:231], %[q0] offset:1024\n\t" \
    "ds_read_b128 a[232:235], %[q1]\n\t" \
    "ds_read_b128 a[236:239], %[q1] offset:1024\n\t" \
    "ds_read_b128 a[240:243], %[q2]\n\t" \
    "ds_read_b128 a[244:247], %[q2] offset:1024\n\t" \
    "ds_read_b128 a[248:251], %[q3]\n\t" \
    "ds_read_b128 a[252:255], %[q3] offset:1024\n\t" \
    "s_add_u32 %[kvo], %[kvo], 4096\n\t" \
    "s_and_b32 %[kvo], %[kvo], 0xffff\n\t" \
    "s_add_u32 %[t0], %[kvs], %[kvo]\n\t" \
    "buffer_load_dwordx4 v[128:131], %[l16], %[rs], %[t0] offen offset:0 sc1\n\t" \
    "buffer_load_dwordx4 v[132:135], %[l16], %[rs], %[t0] offen offset:1024 sc1\n\t" \
    "buffer_load_dwordx4 v[136:139], %[l16], %[rs], %[t0] offen offset:2048 sc1\n\t" \
    "buffer_load_dwordx4 v[140:143], %[l16], %[rs], %[t0] offen offset:3072 sc1\n\t" \
    "s_add_u32 %[kvo], %[kvo], 4096\n\t" \
    "s_and_b32 %[kvo], %[kvo], 0xffff\n\t" \
    "s_add_u32 %[t0], %[kvs], %[kvo]\n\t" \
    "buffer_load_dwordx4 v[144:147], %[l16], %[rs], %[t0] offen offset:0 sc1\n\t" \
    "buffer_load_dwordx4 v[148:151], %[l16], %[rs], %[t0] offen offset:1024 sc1\n\t" \
    "buffer_load_dwordx4 v[152:155], %[l16], %[rs], %[t0] offen offset:2048 sc1\n\t" \
    "buffer_load_dwordx4 v[156:159], %[l16], %[rs], %[t0] offen offset:3072 sc1\n\t" \
    "s_waitcnt lgkmcnt(6)\n\t" \
    "v_mfma_f32_32x32x16_bf16 v[64:79], v[96:99], a[224:227], 0\n\t" \
    "v_mfma_f32_32x32x16_bf16 v[64:79], v[100:103], a[228:231], v[64:79]\n\t" \
    "v_mov_b64 v[0:1], 0\n\t" \
    "v_mov_b64 v[2:3], 0\n\t" \
    "v_mov_b64 v[4:5], 0\n\t" \
    "v_mov_b64 v[6:7], 0\n\t" \
    "v_mov_b64 v[8:9], 0\n\t" \
    "v_mov_b64 v[10:11], 0\n\t" \
    "v_mov_b64 v[12:13], 0\n\t" \
    "v_mov_b64 v[14:15], 0\n\t" \
    "v_mov_b64 v[16:17], 0\n\t" \
    "v_mov_b64 v[18:19], 0\n\t" \
    "v_mov_b64 v[20:21], 0\n\t" \
    "v_mov_b64 v[22:23], 0\n\t" \
    "v_mov_b64 v[24:25], 0\n\t" \
    "v_mov_b64 v[26:27], 0\n\t" \
    "v_mov_b64 v[28:29], 0\n\t" \
    "v_mov_b64 v[30:31], 0\n\t" \
    "v_mov_b64 v[32:33], 0\n\t" \
    "v_mov_b64 v[34:35], 0\n\t" \
    "v_mov_b64 v[36:37], 0\n\t" \
    "v_mov_b64 v[38:39], 0\n\t" \
    "v_mov_b64 v[40:41], 0\n\t" \
    "v_mov_b64 v[42:43], 0\n\t" \
    "v_mov_b64 v[44:45], 0\n\t" \
    "v_mov_b64 v[46:47], 0\n\t" \
    "v_mov_b64 v[48:49], 0\n\t" \
    "v_mov_b64 v[50:51], 0\n\t" \
    "v_mov_b64 v[52:53], 0\n\t" \
    "v_mov_b64 v[54:55], 0\n\t" \
    "v_mov_b64 v[56:57], 0\n\t" \
    "v_mov_b64 v[58:59], 0\n\t" \
    "v_mov_b64 v[60:61], 0\n\t" \
    "v_mov_b64 v[62:63], 0\n\t" \
    "s_mov_b32 %[it], 0\n\t" \
    "s_waitcnt lgkmcnt(0)\n\t" \
    "s_barrier\n\t" \
    "v_exp_f32 v168, v64\n\t" \
    "v_exp_f32 v169, v65\n\t" \
    "v_exp_f32 v170, v66\n\t" \
    "v_exp_f32 v171, v67\n\t" \
    "v_mfma_f32_32x32x16_bf16 v[80:95], v[96:99], a[232:235], 0\n\t" \
    "v_cvt_pk_bf16_f32 v160, v168, v169\n\t" \
    "v_cvt_pk_bf16_f32 v161, v170, v171\n\t" \
    "v_exp_f32 v168, v68\n\t" \
    "v_exp_f32 v169, v69\n\t" \
    "v_exp_f32 v170, v70\n\t" \
    "v_exp_f32 v171, v71\n\t" \
    "v_mfma_f32_32x32x16_bf16 v[80:95], v[100:103], a[236:239], v[80:95]\n\t" \
    "v_cvt_pk_bf16_f32 v162, v168, v169\n\t" \
    "v_cvt_pk_bf16_f32 v163, v170, v171\n\t" \
    "v_exp_f32 v168, v72\n\t" \
    "v_exp_f32 v169, v73\n\t" \
    "v_exp_f32 v170, v74\n\t" \
    "v_exp_f32 v171, v75\n\t" \
    "s_nop 0\n\t" \
    "v_cvt_pk_bf16_f32 v164, v168, v169\n\t" \
    "v_cvt_pk_bf16_f32 v165, v170, v171\n\t" \
    "v_exp_f32 v168, v76\n\t" \
    "v_exp_f32 v169, v77\n\t" \
    "v_exp_f32 v170, v78\n\t" \
    "v_exp_f32 v171, v79\n\t" \
    "v_mfma_f32_32x32x16_bf16 v[0:15], v[104:107], v[160:163], v[0:15]\n\t" \
    "v_cvt_pk_bf16_f32 v166, v168, v169\n\t" \
    "v_cvt_pk_bf16_f32 v167, v170, v171\n\t" \
    "v_exp_f32 v168, v80\n\t" \
    "v_exp_f32 v169, v81\n\t" \
    "v_exp_f32 v170, v82\n\t" \
    "v_exp_f32 v171, v83\n\t" \
    "v_mfma_f32_32x32x16_bf16 v[64:79], v[96:99], a[240:243], 0\n\t" \
    "v_cvt_pk_bf16_f32 v160, v168, v169\n\t" \
    "v_cvt_pk_bf16_f32 v161, v170, v171\n\t" \
    "v_exp_f32 v168, v84\n\t" \
    "v_exp_f32 v169, v85\n\t" \
    "v_exp_f32 v170, v86\n\t" \
    "v_exp_f32 v171, v87\n\t" \
    "v_mfma_f32_32x32x16_bf16 v[64:79], v[100:103], a[244:247], v[64:79]\n\t" \
    "v_cvt_pk_bf16_f32 v162, v168, v169\n\t" \
    "v_cvt_pk_bf16_f32 v163, v170, v171\n\t" \
    "v_exp_f32 v168, v88\n\t" \
    "v_exp_f32 v169, v89\n\t" \
    "v_exp_f32 v170, v90\n\t" \
    "v_exp_f32 v171, v91\n\t" \
    "v_mfma_f32_32x32x16_bf16 v[0:15], v[108:111], v[164:167], v[0:15]\n\t" \
    "v_cvt_pk_bf16_f32 v164, v168, v169\n\t" \
    "v_cvt_pk_bf16_f32 v165, v170, v171\n\t" \
    "v_exp_f32 v168, v92\n\t" \
    "v_exp_f32 v169, v93\n\t" \
    "v_exp_f32 v170, v94\n\t" \
    "v_exp_f32 v171, v95\n\t" \
    "v_mfma_f32_32x32x16_bf16 v[16:31], v[104:107], v[160:163], v[16:31]\n\t" \
    "v_cvt_pk_bf16_f32 v166, v168, v169\n\t" \
    "v_cvt_pk_bf16_f32 v167, v170, v171\n\t" \
    "v_exp_f32 v168, v64\n\t" \
    "v_exp_f32 v169, v65\n\t" \
    "v_exp_f32 v170, v66\n\t" \
    "v_exp_f32 v171, v67\n\t" \
    "v_mfma_f32_32x32x16_bf16 v[80:95], v[96:99], a[248:251], 0\n\t" \
    "v_cvt_pk_bf16_f32 v160, v168, v169\n\t" \
    "v_cvt_pk_bf16_f32 v161, v170, v171\n\t" \
    "v_exp_f32 v168, v68\n\t" \
    "v_exp_f32 v169, v69\n\t" \
    "v_exp_f32 v170, v70\n\t" \
    "v_exp_f32 v171, v71\n\t" \
    "v_mfma_f32_32x32x16_bf16 v[80:95], v[100:103], a[252:255], v[80:95]\n\t" \
    "v_cvt_pk_bf16_f32 v162, v168, v169\n\t" \
    "v_cvt_pk_bf16_f32 v163, v170, v171\n\t" \
    "v_exp_f32 v168, v72\n\t" \
    "v_exp_f32 v169, v73\n\t" \
    "v_exp_f32 v170, v74\n\t" \
    "v_exp_f32 v171, v75\n\t" \
    "v_mfma_f32_32x32x16_bf16 v[16:31], v[108:111], v[164:167], v[16:31]\n\t" \
    "v_cvt_pk_bf16_f32 v164, v168, v169\n\t" \
    "v_cvt_pk_bf16_f32 v165, v170, v171\n\t" \
    "v_exp_f32 v168, v76\n\t" \
    "v_exp_f32 v169, v77\n\t" \
    "v_exp_f32 v170, v78\n\t" \
    "v_exp_f32 v171, v79\n\t" \
    "v_mfma_f32_32x32x16_bf16 v[32:47], v[104:107], v[160:163], v[32:47]\n\t" \
    "v_cvt_pk_bf16_f32 v166, v168, v169\n\t" \
    "v_cvt_pk_bf16_f32 v167, v170, v171\n\t" \
    "s_cmp_eq_u32 %[layer], 0\n\t" \
    "s_cbranch_scc1 .Lattn_first%=\n\t" \
    "s_branch .Lattn_entry%=\n\t" \
    ".Lattn_loop%=:\n\t" \
    "v_exp_f32 v168, v64\n\t" \
    "v_exp_f32 v169, v65\n\t" \
    "v_exp_f32 v170, v66\n\t" \
    "v_exp_f32 v171, v67\n\t" \
    "v_mfma_f32_32x32x16_bf16 v[80:95], v[96:99], a[232:235], 0\n\t" \
    "v_cvt_pk_bf16_f32 v160, v168, v169\n\t" \
    "v_cvt_pk_bf16_f32 v161, v170, v171\n\t" \
    "v_exp_f32 v168, v68\n\t" \
    "v_exp_f32 v169, v69\n\t" \
    "v_exp_f32 v170, v70\n\t" \
    "v_exp_f32 v171, v71\n\t" \
    "v_mfma_f32_32x32x16_bf16 v[80:95], v[100:103], a[236:239], v[80:95]\n\t" \
    "v_cvt_pk_bf16_f32 v162, v168, v169\n\t" \
    "v_cvt_pk_bf16_f32 v163, v170, v171\n\t" \
    "v_exp_f32 v168, v72\n\t" \
    "v_exp_f32 v169, v73\n\t" \
    "v_exp_f32 v170, v74\n\t" \
    "v_exp_f32 v171, v75\n\t" \
    "v_mfma_f32_32x32x16_bf16 v[48:63], v[156:159], v[164:167], v[48:63]\n\t" \
    "v_cvt_pk_bf16_f32 v164, v168, v169\n\t" \
    "v_cvt_pk_bf16_f32 v165, v170, v171\n\t" \
    "v_exp_f32 v168, v76\n\t" \
    "v_exp_f32 v169, v77\n\t" \
    "v_exp_f32 v170, v78\n\t" \
    "v_exp_f32 v171, v79\n\t" \
    "v_mfma_f32_32x32x16_bf16 v[0:15], v[104:107], v[160:163], v[0:15]\n\t" \
    "v_cvt_pk_bf16_f32 v166, v168, v169\n\t" \
    "v_cvt_pk_bf16_f32 v167, v170, v171\n\t" \
    "v_exp_f32 v168, v80\n\t" \
    "v_exp_f32 v169, v81\n\t" \
    "v_exp_f32 v170, v82\n\t" \
    "v_exp_f32 v171, v83\n\t" \
    "v_mfma_f32_32x32x16_bf16 v[64:79], v[96:99], a[240:243], 0\n\t" \
    "v_cvt_pk_bf16_f32 v160, v168, v169\n\t" \
    "v_cvt_pk_bf16_f32 v161, v170, v171\n\t" \
    "v_exp_f32 v168, v84\n\t" \
    "v_exp_f32 v169, v85\n\t" \
    "v_exp_f32 v170, v86\n\t" \
    "v_exp_f32 v171, v87\n\t" \
    "v_mfma_f32_32x32x16_bf16 v[64:79], v[100:103], a[244:247], v[64:79]\n\t" \
    "v_cvt_pk_bf16_f32 v162, v168, v169\n\t" \
    "v_cvt_pk_bf16_f32 v163, v170, v171\n\t" \
    "v_exp_f32 v168, v88\n\t" \
    "v_exp_f32 v169, v89\n\t" \
    "v_exp_f32 v170, v90\n\t" \
    "v_exp_f32 v171, v91\n\t" \
    "v_mfma_f32_32x32x16_bf16 v[0:15], v[108:111], v[164:167], v[0:15]\n\t" \
    "v_cvt_pk_bf16_f32 v164, v168, v169\n\t" \
    "v_cvt_pk_bf16_f32 v165, v170, v171\n\t" \
    "v_exp_f32 v168, v92\n\t" \
    "v_exp_f32 v169, v93\n\t" \
    "v_exp_f32 v170, v94\n\t" \
    "v_exp_f32 v171, v95\n\t" \
    "v_mfma_f32_32x32x16_bf16 v[16:31], v[104:107], v[160:163], v[16:31]\n\t" \
    "v_cvt_pk_bf16_f32 v166, v168, v169\n\t" \
    "v_cvt_pk_bf16_f32 v167, v170, v171\n\t" \
    "v_exp_f32 v168, v64\n\t" \
    "v_exp_f32 v169, v65\n\t" \
    "v_exp_f32 v170, v66\n\t" \
    "v_exp_f32 v171, v67\n\t" \
    "v_mfma_f32_32x32x16_bf16 v[80:95], v[96:99], a[248:251], 0\n\t" \
    "v_cvt_pk_bf16_f32 v160, v168, v169\n\t" \
    "v_cvt_pk_bf16_f32 v161, v170, v171\n\t" \
    "v_exp_f32 v168, v68\n\t" \
    "v_exp_f32 v169, v69\n\t" \
    "v_exp_f32 v170, v70\n\t" \
    "v_exp_f32 v171, v71\n\t" \
    "v_mfma_f32_32x32x16_bf16 v[80:95], v[100:103], a[252:255], v[80:95]\n\t" \
    "v_cvt_pk_bf16_f32 v162, v168, v169\n\t" \
    "v_cvt_pk_bf16_f32 v163, v170, v171\n\t" \
    "v_exp_f32 v168, v72\n\t" \
    "v_exp_f32 v169, v73\n\t" \
    "v_exp_f32 v170, v74\n\t" \
    "v_exp_f32 v171, v75\n\t" \
    "v_mfma_f32_32x32x16_bf16 v[16:31], v[108:111], v[164:167], v[16:31]\n\t" \
    "s_add_u32 %[kvo], %[kvo], 4096\n\t" \
    "s_and_b32 %[kvo], %[kvo], 0xffff\n\t" \
    "s_add_u32 %[t0], %[kvs], %[kvo]\n\t" \
    "buffer_load_dwordx4 v[128:131], %[l16], %[rs], %[t0] offen offset:0 sc1\n\t" \
    "buffer_load_dwordx4 v[132:135], %[l16], %[rs], %[t0] offen offset:1024 sc1\n\t" \
    "buffer_load_dwordx4 v[136:139], %[l16], %[rs], %[t0] offen offset:2048 sc1\n\t" \
    "buffer_load_dwordx4 v[140:143], %[l16], %[rs], %[t0] offen offset:3072 sc1\n\t" \
    "s_add_u32 %[kvo], %[kvo], 4096\n\t" \
    "s_and_b32 %[kvo], %[kvo], 0xffff\n\t" \
    "s_add_u32 %[t0], %[kvs], %[kvo]\n\t" \
    "buffer_load_dwordx4 v[144:147], %[l16], %[rs], %[t0] offen offset:0 sc1\n\t" \
    "buffer_load_dwordx4 v[148:151], %[l16], %[rs], %[t0] offen offset:1024 sc1\n\t" \
    "buffer_load_dwordx4 v[152:155], %[l16], %[rs], %[t0] offen offset:2048 sc1\n\t" \
    "buffer_load_dwordx4 v[156:159], %[l16], %[rs], %[t0] offen offset:3072 sc1\n\t" \
    "v_cvt_pk_bf16_f32 v164, v168, v169\n\t" \
    "v_cvt_pk_bf16_f32 v165, v170, v171\n\t" \
    "v_exp_f32 v168, v76\n\t" \
    "v_exp_f32 v169, v77\n\t" \
    "v_exp_f32 v170, v78\n\t" \
    "v_exp_f32 v171, v79\n\t" \
    "v_mfma_f32_32x32x16_bf16 v[32:47], v[104:107], v[160:163], v[32:47]\n\t" \
    "v_cvt_pk_bf16_f32 v166, v168, v169\n\t" \
    "v_cvt_pk_bf16_f32 v167, v170, v171\n\t" \
    ".Lattn_entry%=:\n\t" \
    "s_waitcnt vmcnt(8)\n\t" \
    "v_exp_f32 v168, v80\n\t" \
    "v_exp_f32 v169, v81\n\t" \
    "v_exp_f32 v170, v82\n\t" \
    "v_exp_f32 v171, v83\n\t" \
    "v_mfma_f32_32x32x16_bf16 v[64:79], v[112:115], a[224:227], 0\n\t" \
    "v_cvt_pk_bf16_f32 v160, v168, v169\n\t" \
    "v_cvt_pk_bf16_f32 v161, v170, v171\n\t" \
    "v_exp_f32 v168, v84\n\t" \
    "v_exp_f32 v169, v85\n\t" \
    "v_exp_f32 v170, v86\n\t" \
    "v_exp_f32 v171, v87\n\t" \
    "v_mfma_f32_32x32x16_bf16 v[64:79], v[116:119], a[228:231], v[64:79]\n\t" \
    "v_cvt_pk_bf16_f32 v162, v168, v169\n\t" \
    "v_cvt_pk_bf16_f32 v163, v170, v171\n\t" \
    "v_exp_f32 v168, v88\n\t" \
    "v_exp_f32 v169, v89\n\t" \
    "v_exp_f32 v170, v90\n\t" \
    "v_exp_f32 v171, v91\n\t" \
    "v_mfma_f32_32x32x16_bf16 v[32:47], v[108:111], v[164:167], v[32:47]\n\t" \
    "v_cvt_pk_bf16_f32 v164, v168, v169\n\t" \
    "v_cvt_pk_bf16_f32 v165, v170, v171\n\t" \
    "v_exp_f32 v168, v92\n\t" \
    "v_exp_f32 v169, v93\n\t" \
    "v_exp_f32 v170, v94\n\t" \
    "v_exp_f32 v171, v95\n\t" \
    "v_mfma_f32_32x32x16_bf16 v[48:63], v[104:107], v[160:163], v[48:63]\n\t" \
    "v_cvt_pk_bf16_f32 v166, v168, v169\n\t" \
    "v_cvt_pk_bf16_f32 v167, v170, v171\n\t" \
    "v_exp_f32 v168, v64\n\t" \
    "v_exp_f32 v169, v65\n\t" \
    "v_exp_f32 v170, v66\n\t" \
    "v_exp_f32 v171, v67\n\t" \
    "v_mfma_f32_32x32x16_bf16 v[80:95], v[112:115], a[232:235], 0\n\t" \
    "v_cvt_pk_bf16_f32 v160, v168, v169\n\t" \
    "v_cvt_pk_bf16_f32 v161, v170, v171\n\t" \
    "v_exp_f32 v168, v68\n\t" \
    "v_exp_f32 v169, v69\n\t" \
    "v_exp_f32 v170, v70\n\t" \
    "v_exp_f32 v171, v71\n\t" \
    "v_mfma_f32_32x32x16_bf16 v[80:95], v[116:119], a[236:239], v[80:95]\n\t" \
    "v_cvt_pk_bf16_f32 v162, v168, v169\n\t" \
    "v_cvt_pk_bf16_f32 v163, v170, v171\n\t" \
    "v_exp_f32 v168, v72\n\t" \
    "v_exp_f32 v169, v73\n\t" \
    "v_exp_f32 v170, v74\n\t" \
    "v_exp_f32 v171, v75\n\t" \
    "v_mfma_f32_32x32x16_bf16 v[48:63], v[108:111], v[164:167], v[48:63]\n\t" \
    "v_cvt_pk_bf16_f32 v164, v168, v169\n\t" \
    "v_cvt_pk_bf16_f32 v165, v170, v171\n\t" \
    "v_exp_f32 v168, v76\n\t" \
    "v_exp_f32 v169, v77\n\t" \
    "v_exp_f32 v170, v78\n\t" \
    "v_exp_f32 v171, v79\n\t" \
    "v_mfma_f32_32x32x16_bf16 v[0:15], v[120:123], v[160:163], v[0:15]\n\t" \
    "v_cvt_pk_bf16_f32 v166, v168, v169\n\t" \
    "v_cvt_pk_bf16_f32 v167, v170, v171\n\t" \
    "v_exp_f32 v168, v80\n\t" \
    "v_exp_f32 v169, v81\n\t" \
    "v_exp_f32 v170, v82\n\t" \
    "v_exp_f32 v171, v83\n\t" \
    "v_mfma_f32_32x32x16_bf16 v[64:79], v[112:115], a[240:243], 0\n\t" \
    "v_cvt_pk_bf16_f32 v160, v168, v169\n\t" \
    "v_cvt_pk_bf16_f32 v161, v170, v171\n\t" \
    "v_exp_f32 v168, v84\n\t" \
    "v_exp_f32 v169, v85\n\t" \
    "v_exp_f32 v170, v86\n\t" \
    "v_exp_f32 v171, v87\n\t" \
    "v_mfma_f32_32x32x16_bf16 v[64:79], v[116:119], a[244:247], v[64:79]\n\t" \
    "v_cvt_pk_bf16_f32 v162, v168, v169\n\t" \
    "v_cvt_pk_bf16_f32 v163, v170, v171\n\t" \
    "v_exp_f32 v168, v88\n\t" \
    "v_exp_f32 v169, v89\n\t" \
    "v_exp_f32 v170, v90\n\t" \
    "v_exp_f32 v171, v91\n\t" \
    "v_mfma_f32_32x32x16_bf16 v[0:15], v[124:127], v[164:167], v[0:15]\n\t" \
    "v_cvt_pk_bf16_f32 v164, v168, v169\n\t" \
    "v_cvt_pk_bf16_f32 v165, v170, v171\n\t" \
    "v_exp_f32 v168, v92\n\t" \
    "v_exp_f32 v169, v93\n\t" \
    "v_exp_f32 v170, v94\n\t" \
    "v_exp_f32 v171, v95\n\t" \
    "v_mfma_f32_32x32x16_bf16 v[16:31], v[120:123], v[160:163], v[16:31]\n\t" \
    "v_cvt_pk_bf16_f32 v166, v168, v169\n\t" \
    "v_cvt_pk_bf16_f32 v167, v170, v171\n\t" \
    "v_exp_f32 v168, v64\n\t" \
    "v_exp_f32 v169, v65\n\t" \
    "v_exp_f32 v170, v66\n\t" \
    "v_exp_f32 v171, v67\n\t" \
    "v_mfma_f32_32x32x16_bf16 v[80:95], v[112:115], a[248:251], 0\n\t" \
    "s_min_u32 %[t0], %[wq], 49152\n\t" \
    "s_add_u32 %[t1], %[t0], %[w1s]\n\t" \
    "s_add_u32 %[t0], %[t0], 57600\n\t" \
    "s_mov_b32 m0, %[t0]\n\t" \
    "s_nop 0\n\t" \
    "buffer_load_dwordx4 %[l16], %[rs], %[t1] offen offset:0 sc1 lds\n\t" \
    "buffer_load_dwordx4 %[l16], %[rs], %[t1] offen offset:1024 sc1 lds\n\t" \
    "buffer_load_dwordx4 %[l16], %[rs], %[t1] offen offset:2048 sc1 lds\n\t" \
    "buffer_load_dwordx4 %[l16], %[rs], %[t1] offen offset:3072 sc1 lds\n\t" \
    "s_add_u32 %[wq], %[wq], 16384\n\t" \
    "buffer_load_dwordx4 v[172:175], %[l16], %[rs], %[ww] offen offset:0 sc1\n\t" \
    "buffer_load_dwordx4 v[172:175], %[l16], %[rs], %[ww] offen offset:1024 sc1\n\t" \
    "buffer_load_dwordx4 v[172:175], %[l16], %[rs], %[ww] offen offset:2048 sc1\n\t" \
    "s_add_u32 %[ww], %[ww], 3072\n\t" \
    "v_cvt_pk_bf16_f32 v160, v168, v169\n\t" \
    "v_cvt_pk_bf16_f32 v161, v170, v171\n\t" \
    "v_exp_f32 v168, v68\n\t" \
    "v_exp_f32 v169, v69\n\t" \
    "v_exp_f32 v170, v70\n\t" \
    "v_exp_f32 v171, v71\n\t" \
    "v_mfma_f32_32x32x16_bf16 v[80:95], v[116:119], a[252:255], v[80:95]\n\t" \
    "v_cvt_pk_bf16_f32 v162, v168, v169\n\t" \
    "v_cvt_pk_bf16_f32 v163, v170, v171\n\t" \
    "v_exp_f32 v168, v72\n\t" \
    "v_exp_f32 v169, v73\n\t" \
    "v_exp_f32 v170, v74\n\t" \
    "v_exp_f32 v171, v75\n\t" \
    "v_mfma_f32_32x32x16_bf16 v[16:31], v[124:127], v[164:167], v[16:31]\n\t" \
    "v_cvt_pk_bf16_f32 v164, v168, v169\n\t" \
    "v_cvt_pk_bf16_f32 v165, v170, v171\n\t" \
    "v_exp_f32 v168, v76\n\t" \
    "v_exp_f32 v169, v77\n\t" \
    "v_exp_f32 v170, v78\n\t" \
    "v_exp_f32 v171, v79\n\t" \
    "v_mfma_f32_32x32x16_bf16 v[32:47], v[120:123], v[160:163], v[32:47]\n\t" \
    "v_cvt_pk_bf16_f32 v166, v168, v169\n\t" \
    "v_cvt_pk_bf16_f32 v167, v170, v171\n\t" \
    "s_waitcnt vmcnt(11)\n\t" \
    "v_exp_f32 v168, v80\n\t" \
    "v_exp_f32 v169, v81\n\t" \
    "v_exp_f32 v170, v82\n\t" \
    "v_exp_f32 v171, v83\n\t" \
    "v_mfma_f32_32x32x16_bf16 v[64:79], v[128:131], a[224:227], 0\n\t" \
    "v_cvt_pk_bf16_f32 v160, v168, v169\n\t" \
    "v_cvt_pk_bf16_f32 v161, v170, v171\n\t" \
    "v_exp_f32 v168, v84\n\t" \
    "v_exp_f32 v169, v85\n\t" \
    "v_exp_f32 v170, v86\n\t" \
    "v_exp_f32 v171, v87\n\t" \
    "v_mfma_f32_32x32x16_bf16 v[64:79], v[132:135], a[228:231], v[64:79]\n\t" \
    "v_cvt_pk_bf16_f32 v162, v168, v169\n\t" \
    "v_cvt_pk_bf16_f32 v163, v170, v171\n\t" \
    "v_exp_f32 v168, v88\n\t" \
    "v_exp_f32 v169, v89\n\t" \
    "v_exp_f32 v170, v90\n\t" \
    "v_exp_f32 v171, v91\n\t" \
    "v_mfma_f32_32x32x16_bf16 v[32:47], v[124:127], v[164:167], v[32:47]\n\t" \
    "v_cvt_pk_bf16_f32 v164, v168, v169\n\t" \
    "v_cvt_pk_bf16_f32 v165, v170, v171\n\t" \
    "v_exp_f32 v168, v92\n\t" \
    "v_exp_f32 v169, v93\n\t" \
    "v_exp_f32 v170, v94\n\t" \
    "v_exp_f32 v171, v95\n\t" \
    "v_mfma_f32_32x32x16_bf16 v[48:63], v[120:123], v[160:163], v[48:63]\n\t" \
    "v_cvt_pk_bf16_f32 v166, v168, v169\n\t" \
    "v_cvt_pk_bf16_f32 v167, v170, v171\n\t" \
    "v_exp_f32 v168, v64\n\t" \
    "v_exp_f32 v169, v65\n\t" \
    "v_exp_f32 v170, v66\n\t" \
    "v_exp_f32 v171, v67\n\t" \
    "v_mfma_f32_32x32x16_bf16 v[80:95], v[128:131], a[232:235], 0\n\t" \
    "v_cvt_pk_bf16_f32 v160, v168, v169\n\t" \
    "v_cvt_pk_bf16_f32 v161, v170, v171\n\t" \
    "v_exp_f32 v168, v68\n\t" \
    "v_exp_f32 v169, v69\n\t" \
    "v_exp_f32 v170, v70\n\t" \
    "v_exp_f32 v171, v71\n\t" \
    "v_mfma_f32_32x32x16_bf16 v[80:95], v[132:135], a[236:239], v[80:95]\n\t" \
    "v_cvt_pk_bf16_f32 v162, v168, v169\n\t" \
    "v_cvt_pk_bf16_f32 v163, v170, v171\n\t" \
    "v_exp_f32 v168, v72\n\t" \
    "v_exp_f32 v169, v73\n\t" \
    "v_exp_f32 v170, v74\n\t" \
    "v_exp_f32 v171, v75\n\t" \
    "v_mfma_f32_32x32x16_bf16 v[48:63], v[124:127], v[164:167], v[48:63]\n\t" \
    "v_cvt_pk_bf16_f32 v164, v168, v169\n\t" \
    "v_cvt_pk_bf16_f32 v165, v170, v171\n\t" \
    "v_exp_f32 v168, v76\n\t" \
    "v_exp_f32 v169, v77\n\t" \
    "v_exp_f32 v170, v78\n\t" \
    "v_exp_f32 v171, v79\n\t" \
    "v_mfma_f32_32x32x16_bf16 v[0:15], v[136:139], v[160:163], v[0:15]\n\t" \
    "v_cvt_pk_bf16_f32 v166, v168, v169\n\t" \
    "v_cvt_pk_bf16_f32 v167, v170, v171\n\t" \
    "v_exp_f32 v168, v80\n\t" \
    "v_exp_f32 v169, v81\n\t" \
    "v_exp_f32 v170, v82\n\t" \
    "v_exp_f32 v171, v83\n\t" \
    "v_mfma_f32_32x32x16_bf16 v[64:79], v[128:131], a[240:243], 0\n\t" \
    "v_cvt_pk_bf16_f32 v160, v168, v169\n\t" \
    "v_cvt_pk_bf16_f32 v161, v170, v171\n\t" \
    "v_exp_f32 v168, v84\n\t" \
    "v_exp_f32 v169, v85\n\t" \
    "v_exp_f32 v170, v86\n\t" \
    "v_exp_f32 v171, v87\n\t" \
    "v_mfma_f32_32x32x16_bf16 v[64:79], v[132:135], a[244:247], v[64:79]\n\t" \
    "v_cvt_pk_bf16_f32 v162, v168, v169\n\t" \
    "v_cvt_pk_bf16_f32 v163, v170, v171\n\t" \
    "v_exp_f32 v168, v88\n\t" \
    "v_exp_f32 v169, v89\n\t" \
    "v_exp_f32 v170, v90\n\t" \
    "v_exp_f32 v171, v91\n\t" \
    "v_mfma_f32_32x32x16_bf16 v[0:15], v[140:143], v[164:167], v[0:15]\n\t" \
    "v_cvt_pk_bf16_f32 v164, v168, v169\n\t" \
    "v_cvt_pk_bf16_f32 v165, v170, v171\n\t" \
    "v_exp_f32 v168, v92\n\t" \
    "v_exp_f32 v169, v93\n\t" \
    "v_exp_f32 v170, v94\n\t" \
    "v_exp_f32 v171, v95\n\t" \
    "v_mfma_f32_32x32x16_bf16 v[16:31], v[136:139], v[160:163], v[16:31]\n\t" \
    "v_cvt_pk_bf16_f32 v166, v168, v169\n\t" \
    "v_cvt_pk_bf16_f32 v167, v170, v171\n\t" \
    "v_exp_f32 v168, v64\n\t" \
    "v_exp_f32 v169, v65\n\t" \
    "v_exp_f32 v170, v66\n\t" \
    "v_exp_f32 v171, v67\n\t" \
    "v_mfma_f32_32x32x16_bf16 v[80:95], v[128:131], a[248:251], 0\n\t" \
    "v_cvt_pk_bf16_f32 v160, v168, v169\n\t" \
    "v_cvt_pk_bf16_f32 v161, v170, v171\n\t" \
    "v_exp_f32 v168, v68\n\t" \
    "v_exp_f32 v169, v69\n\t" \
    "v_exp_f32 v170, v70\n\t" \
    "v_exp_f32 v171, v71\n\t" \
    "v_mfma_f32_32x32x16_bf16 v[80:95], v[132:135], a[252:255], v[80:95]\n\t" \
    "v_cvt_pk_bf16_f32 v162, v168, v169\n\t" \
    "v_cvt_pk_bf16_f32 v163, v170, v171\n\t" \
    "v_exp_f32 v168, v72\n\t" \
    "v_exp_f32 v169, v73\n\t" \
    "v_exp_f32 v170, v74\n\t" \
    "v_exp_f32 v171, v75\n\t" \
    "v_mfma_f32_32x32x16_bf16 v[16:31], v[140:143], v[164:167], v[16:31]\n\t" \
    "s_cmp_eq_u32 %[it], 3\n\t" \
    "s_cselect_b32 %[kvs], %[oob], %[kvs]\n\t" \
    "s_add_u32 %[kvo], %[kvo], 4096\n\t" \
    "s_and_b32 %[kvo], %[kvo], 0xffff\n\t" \
    "s_add_u32 %[t0], %[kvs], %[kvo]\n\t" \
    "buffer_load_dwordx4 v[96:99], %[l16], %[rs], %[t0] offen offset:0 sc1\n\t" \
    "buffer_load_dwordx4 v[100:103], %[l16], %[rs], %[t0] offen offset:1024 sc1\n\t" \
    "buffer_load_dwordx4 v[104:107], %[l16], %[rs], %[t0] offen offset:2048 sc1\n\t" \
    "buffer_load_dwordx4 v[108:111], %[l16], %[rs], %[t0] offen offset:3072 sc1\n\t" \
    "s_add_u32 %[kvo], %[kvo], 4096\n\t" \
    "s_and_b32 %[kvo], %[kvo], 0xffff\n\t" \
    "s_add_u32 %[t0], %[kvs], %[kvo]\n\t" \
    "buffer_load_dwordx4 v[112:115], %[l16], %[rs], %[t0] offen offset:0 sc1\n\t" \
    "buffer_load_dwordx4 v[116:119], %[l16], %[rs], %[t0] offen offset:1024 sc1\n\t" \
    "buffer_load_dwordx4 v[120:123], %[l16], %[rs], %[t0] offen offset:2048 sc1\n\t" \
    "buffer_load_dwordx4 v[124:127], %[l16], %[rs], %[t0] offen offset:3072 sc1\n\t" \
    "v_cvt_pk_bf16_f32 v164, v168, v169\n\t" \
    "v_cvt_pk_bf16_f32 v165, v170, v171\n\t" \
    "v_exp_f32 v168, v76\n\t" \
    "v_exp_f32 v169, v77\n\t" \
    "v_exp_f32 v170, v78\n\t" \
    "v_exp_f32 v171, v79\n\t" \
    "v_mfma_f32_32x32x16_bf16 v[32:47], v[136:139], v[160:163], v[32:47]\n\t" \
    "v_cvt_pk_bf16_f32 v166, v168, v169\n\t" \
    "v_cvt_pk_bf16_f32 v167, v170, v171\n\t" \
    "s_waitcnt vmcnt(15)\n\t" \
    "v_exp_f32 v168, v80\n\t" \
    "v_exp_f32 v169, v81\n\t" \
    "v_exp_f32 v170, v82\n\t" \
    "v_exp_f32 v171, v83\n\t" \
    "v_mfma_f32_32x32x16_bf16 v[64:79], v[144:147], a[224:227], 0\n\t" \
    "v_cvt_pk_bf16_f32 v160, v168, v169\n\t" \
    "v_cvt_pk_bf16_f32 v161, v170, v171\n\t" \
    "v_exp_f32 v168, v84\n\t" \
    "v_exp_f32 v169, v85\n\t" \
    "v_exp_f32 v170, v86\n\t" \
    "v_exp_f32 v171, v87\n\t" \
    "v_mfma_f32_32x32x16_bf16 v[64:79], v[148:151], a[228:231], v[64:79]\n\t" \
    "v_cvt_pk_bf16_f32 v162, v168, v169\n\t" \
    "v_cvt_pk_bf16_f32 v163, v170, v171\n\t" \
    "v_exp_f32 v168, v88\n\t" \
    "v_exp_f32 v169, v89\n\t" \
    "v_exp_f32 v170, v90\n\t" \
    "v_exp_f32 v171, v91\n\t" \
    "v_mfma_f32_32x32x16_bf16 v[32:47], v[140:143], v[164:167], v[32:47]\n\t" \
    "v_cvt_pk_bf16_f32 v164, v168, v169\n\t" \
    "v_cvt_pk_bf16_f32 v165, v170, v171\n\t" \
    "v_exp_f32 v168, v92\n\t" \
    "v_exp_f32 v169, v93\n\t" \
    "v_exp_f32 v170, v94\n\t" \
    "v_exp_f32 v171, v95\n\t" \
    "v_mfma_f32_32x32x16_bf16 v[48:63], v[136:139], v[160:163], v[48:63]\n\t" \
    "v_cvt_pk_bf16_f32 v166, v168, v169\n\t" \
    "v_cvt_pk_bf16_f32 v167, v170, v171\n\t" \
    "v_exp_f32 v168, v64\n\t" \
    "v_exp_f32 v169, v65\n\t" \
    "v_exp_f32 v170, v66\n\t" \
    "v_exp_f32 v171, v67\n\t" \
    "v_mfma_f32_32x32x16_bf16 v[80:95], v[144:147], a[232:235], 0\n\t" \
    "v_cvt_pk_bf16_f32 v160, v168, v169\n\t" \
    "v_cvt_pk_bf16_f32 v161, v170, v171\n\t" \
    "v_exp_f32 v168, v68\n\t" \
    "v_exp_f32 v169, v69\n\t" \
    "v_exp_f32 v170, v70\n\t" \
    "v_exp_f32 v171, v71\n\t" \
    "v_mfma_f32_32x32x16_bf16 v[80:95], v[148:151], a[236:239], v[80:95]\n\t" \
    "v_cvt_pk_bf16_f32 v162, v168, v169\n\t" \
    "v_cvt_pk_bf16_f32 v163, v170, v171\n\t" \
    "v_exp_f32 v168, v72\n\t" \
    "v_exp_f32 v169, v73\n\t" \
    "v_exp_f32 v170, v74\n\t" \
    "v_exp_f32 v171, v75\n\t" \
    "v_mfma_f32_32x32x16_bf16 v[48:63], v[140:143], v[164:167], v[48:63]\n\t" \
    "v_cvt_pk_bf16_f32 v164, v168, v169\n\t" \
    "v_cvt_pk_bf16_f32 v165, v170, v171\n\t" \
    "v_exp_f32 v168, v76\n\t" \
    "v_exp_f32 v169, v77\n\t" \
    "v_exp_f32 v170, v78\n\t" \
    "v_exp_f32 v171, v79\n\t" \
    "v_mfma_f32_32x32x16_bf16 v[0:15], v[152:155], v[160:163], v[0:15]\n\t" \
    "v_cvt_pk_bf16_f32 v166, v168, v169\n\t" \
    "v_cvt_pk_bf16_f32 v167, v170, v171\n\t" \
    "v_exp_f32 v168, v80\n\t" \
    "v_exp_f32 v169, v81\n\t" \
    "v_exp_f32 v170, v82\n\t" \
    "v_exp_f32 v171, v83\n\t" \
    "v_mfma_f32_32x32x16_bf16 v[64:79], v[144:147], a[240:243], 0\n\t" \
    "v_cvt_pk_bf16_f32 v160, v168, v169\n\t" \
    "v_cvt_pk_bf16_f32 v161, v170, v171\n\t" \
    "v_exp_f32 v168, v84\n\t" \
    "v_exp_f32 v169, v85\n\t" \
    "v_exp_f32 v170, v86\n\t" \
    "v_exp_f32 v171, v87\n\t" \
    "v_mfma_f32_32x32x16_bf16 v[64:79], v[148:151], a[244:247], v[64:79]\n\t" \
    "v_cvt_pk_bf16_f32 v162, v168, v169\n\t" \
    "v_cvt_pk_bf16_f32 v163, v170, v171\n\t" \
    "v_exp_f32 v168, v88\n\t" \
    "v_exp_f32 v169, v89\n\t" \
    "v_exp_f32 v170, v90\n\t" \
    "v_exp_f32 v171, v91\n\t" \
    "v_mfma_f32_32x32x16_bf16 v[0:15], v[156:159], v[164:167], v[0:15]\n\t" \
    "v_cvt_pk_bf16_f32 v164, v168, v169\n\t" \
    "v_cvt_pk_bf16_f32 v165, v170, v171\n\t" \
    "v_exp_f32 v168, v92\n\t" \
    "v_exp_f32 v169, v93\n\t" \
    "v_exp_f32 v170, v94\n\t" \
    "v_exp_f32 v171, v95\n\t" \
    "v_mfma_f32_32x32x16_bf16 v[16:31], v[152:155], v[160:163], v[16:31]\n\t" \
    "v_cvt_pk_bf16_f32 v166, v168, v169\n\t" \
    "v_cvt_pk_bf16_f32 v167, v170, v171\n\t" \
    "s_cmp_eq_u32 %[it], 3\n\t" \
    "s_cbranch_scc1 .Lattn_tail%=\n\t" \
    "v_exp_f32 v168, v64\n\t" \
    "v_exp_f32 v169, v65\n\t" \
    "v_exp_f32 v170, v66\n\t" \
    "v_exp_f32 v171, v67\n\t" \
    "v_mfma_f32_32x32x16_bf16 v[80:95], v[144:147], a[248:251], 0\n\t" \
    "v_cvt_pk_bf16_f32 v160, v168, v169\n\t" \
    "v_cvt_pk_bf16_f32 v161, v170, v171\n\t" \
    "v_exp_f32 v168, v68\n\t" \
    "v_exp_f32 v169, v69\n\t" \
    "v_exp_f32 v170, v70\n\t" \
    "v_exp_f32 v171, v71\n\t" \
    "v_mfma_f32_32x32x16_bf16 v[80:95], v[148:151], a[252:255], v[80:95]\n\t" \
    "v_cvt_pk_bf16_f32 v162, v168, v169\n\t" \
    "v_cvt_pk_bf16_f32 v163, v170, v171\n\t" \
    "v_exp_f32 v168, v72\n\t" \
    "v_exp_f32 v169, v73\n\t" \
    "v_exp_f32 v170, v74\n\t" \
    "v_exp_f32 v171, v75\n\t" \
    "v_mfma_f32_32x32x16_bf16 v[16:31], v[156:159], v[164:167], v[16:31]\n\t" \
    "v_cvt_pk_bf16_f32 v164, v168, v169\n\t" \
    "v_cvt_pk_bf16_f32 v165, v170, v171\n\t" \
    "v_exp_f32 v168, v76\n\t" \
    "v_exp_f32 v169, v77\n\t" \
    "v_exp_f32 v170, v78\n\t" \
    "v_exp_f32 v171, v79\n\t" \
    "v_mfma_f32_32x32x16_bf16 v[32:47], v[152:155], v[160:163], v[32:47]\n\t" \
    "v_cvt_pk_bf16_f32 v166, v168, v169\n\t" \
    "v_cvt_pk_bf16_f32 v167, v170, v171\n\t" \
    "s_waitcnt vmcnt(4)\n\t" \
    "v_exp_f32 v168, v80\n\t" \
    "v_exp_f32 v169, v81\n\t" \
    "v_exp_f32 v170, v82\n\t" \
    "v_exp_f32 v171, v83\n\t" \
    "v_mfma_f32_32x32x16_bf16 v[64:79], v[96:99], a[224:227], 0\n\t" \
    "v_cvt_pk_bf16_f32 v160, v168, v169\n\t" \
    "v_cvt_pk_bf16_f32 v161, v170, v171\n\t" \
    "v_exp_f32 v168, v84\n\t" \
    "v_exp_f32 v169, v85\n\t" \
    "v_exp_f32 v170, v86\n\t" \
    "v_exp_f32 v171, v87\n\t" \
    "v_mfma_f32_32x32x16_bf16 v[64:79], v[100:103], a[228:231], v[64:79]\n\t" \
    "v_cvt_pk_bf16_f32 v162, v168, v169\n\t" \
    "v_cvt_pk_bf16_f32 v163, v170, v171\n\t" \
    "v_exp_f32 v168, v88\n\t" \
    "v_exp_f32 v169, v89\n\t" \
    "v_exp_f32 v170, v90\n\t" \
    "v_exp_f32 v171, v91\n\t" \
    "v_mfma_f32_32x32x16_bf16 v[32:47], v[156:159], v[164:167], v[32:47]\n\t" \
    "v_cvt_pk_bf16_f32 v164, v168, v169\n\t" \
    "v_cvt_pk_bf16_f32 v165, v170, v171\n\t" \
    "v_exp_f32 v168, v92\n\t" \
    "v_exp_f32 v169, v93\n\t" \
    "v_exp_f32 v170, v94\n\t" \
    "v_exp_f32 v171, v95\n\t" \
    "v_mfma_f32_32x32x16_bf16 v[48:63], v[152:155], v[160:163], v[48:63]\n\t" \
    "v_cvt_pk_bf16_f32 v166, v168, v169\n\t" \
    "v_cvt_pk_bf16_f32 v167, v170, v171\n\t" \
    "s_add_u32 %[it], %[it], 1\n\t" \
    "s_branch .Lattn_loop%=\n\t" \
    ".Lattn_first%=:\n\t" \
    "s_waitcnt vmcnt(8)\n\t" \
    "v_exp_f32 v168, v80\n\t" \
    "v_exp_f32 v169, v81\n\t" \
    "v_exp_f32 v170, v82\n\t" \
    "v_exp_f32 v171, v83\n\t" \
    "v_mfma_f32_32x32x16_bf16 v[64:79], v[112:115], a[224:227], 0\n\t" \
    "v_cvt_pk_bf16_f32 v160, v168, v169\n\t" \
    "v_cvt_pk_bf16_f32 v161, v170, v171\n\t" \
    "v_exp_f32 v168, v84\n\t" \
    "v_exp_f32 v169, v85\n\t" \
    "v_exp_f32 v170, v86\n\t" \
    "v_exp_f32 v171, v87\n\t" \
    "v_mfma_f32_32x32x16_bf16 v[64:79], v[116:119], a[228:231], v[64:79]\n\t" \
    "v_cvt_pk_bf16_f32 v162, v168, v169\n\t" \
    "v_cvt_pk_bf16_f32 v163, v170, v171\n\t" \
    "v_exp_f32 v168, v88\n\t" \
    "v_exp_f32 v169, v89\n\t" \
    "v_exp_f32 v170, v90\n\t" \
    "v_exp_f32 v171, v91\n\t" \
    "v_mfma_f32_32x32x16_bf16 v[32:47], v[108:111], v[164:167], v[32:47]\n\t" \
    "v_cvt_pk_bf16_f32 v164, v168, v169\n\t" \
    "v_cvt_pk_bf16_f32 v165, v170, v171\n\t" \
    "v_exp_f32 v168, v92\n\t" \
    "v_exp_f32 v169, v93\n\t" \
    "v_exp_f32 v170, v94\n\t" \
    "v_exp_f32 v171, v95\n\t" \
    "v_mfma_f32_32x32x16_bf16 v[48:63], v[104:107], v[160:163], v[48:63]\n\t" \
    "v_cvt_pk_bf16_f32 v166, v168, v169\n\t" \
    "v_cvt_pk_bf16_f32 v167, v170, v171\n\t" \
    "v_exp_f32 v168, v64\n\t" \
    "v_exp_f32 v169, v65\n\t" \
    "v_exp_f32 v170, v66\n\t" \
    "v_exp_f32 v171, v67\n\t" \
    "v_mfma_f32_32x32x16_bf16 v[80:95], v[112:115], a[232:235], 0\n\t" \
    "buffer_load_dword v176, %[vc1], %[rs], %[sc1] offen offset:0 sc1\n\t" \
    "buffer_load_dword v177, %[vc1], %[rs], %[sc1] offen offset:512 sc1\n\t" \
    "buffer_load_dword v178, %[vc1], %[rs], %[sc1] offen offset:1024 sc1\n\t" \
    "buffer_load_dword v179, %[vc1], %[rs], %[sc1] offen offset:1536 sc1\n\t" \
    "buffer_load_dword v180, %[vc1], %[rs], %[sc1] offen offset:2048 sc1\n\t" \
    "buffer_load_dword v181, %[vc1], %[rs], %[sc1] offen offset:2560 sc1\n\t" \
    "buffer_load_dword v182, %[vc1], %[rs], %[sc1] offen offset:3072 sc1\n\t" \
    "buffer_load_dword v183, %[vc1], %[rs], %[sc1] offen offset:3584 sc1\n\t" \
    "global_load_ushort v186, %[b1p], off\n\t" \
    "v_cvt_pk_bf16_f32 v160, v168, v169\n\t" \
    "v_cvt_pk_bf16_f32 v161, v170, v171\n\t" \
    "v_exp_f32 v168, v68\n\t" \
    "v_exp_f32 v169, v69\n\t" \
    "v_exp_f32 v170, v70\n\t" \
    "v_exp_f32 v171, v71\n\t" \
    "v_mfma_f32_32x32x16_bf16 v[80:95], v[116:119], a[236:239], v[80:95]\n\t" \
    "v_cvt_pk_bf16_f32 v162, v168, v169\n\t" \
    "v_cvt_pk_bf16_f32 v163, v170, v171\n\t" \
    "v_exp_f32 v168, v72\n\t" \
    "v_exp_f32 v169, v73\n\t" \
    "v_exp_f32 v170, v74\n\t" \
    "v_exp_f32 v171, v75\n\t" \
    "v_mfma_f32_32x32x16_bf16 v[48:63], v[108:111], v[164:167], v[48:63]\n\t" \
    "v_cvt_pk_bf16_f32 v164, v168, v169\n\t" \
    "v_cvt_pk_bf16_f32 v165, v170, v171\n\t" \
    "v_exp_f32 v168, v76\n\t" \
    "v_exp_f32 v169, v77\n\t" \
    "v_exp_f32 v170, v78\n\t" \
    "v_exp_f32 v171, v79\n\t" \
    "v_mfma_f32_32x32x16_bf16 v[0:15], v[120:123], v[160:163], v[0:15]\n\t" \
    "v_cvt_pk_bf16_f32 v166, v168, v169\n\t" \
    "v_cvt_pk_bf16_f32 v167, v170, v171\n\t" \
    "v_exp_f32 v168, v80\n\t" \
    "v_exp_f32 v169, v81\n\t" \
    "v_exp_f32 v170, v82\n\t" \
    "v_exp_f32 v171, v83\n\t" \
    "v_mfma_f32_32x32x16_bf16 v[64:79], v[112:115], a[240:243], 0\n\t" \
    "v_cvt_pk_bf16_f32 v160, v168, v169\n\t" \
    "v_cvt_pk_bf16_f32 v161, v170, v171\n\t" \
    "v_exp_f32 v168, v84\n\t" \
    "v_exp_f32 v169, v85\n\t" \
    "v_exp_f32 v170, v86\n\t" \
    "v_exp_f32 v171, v87\n\t" \
    "v_mfma_f32_32x32x16_bf16 v[64:79], v[116:119], a[244:247], v[64:79]\n\t" \
    "v_cvt_pk_bf16_f32 v162, v168, v169\n\t" \
    "v_cvt_pk_bf16_f32 v163, v170, v171\n\t" \
    "v_exp_f32 v168, v88\n\t" \
    "v_exp_f32 v169, v89\n\t" \
    "v_exp_f32 v170, v90\n\t" \
    "v_exp_f32 v171, v91\n\t" \
    "v_mfma_f32_32x32x16_bf16 v[0:15], v[124:127], v[164:167], v[0:15]\n\t" \
    "v_cvt_pk_bf16_f32 v164, v168, v169\n\t" \
    "v_cvt_pk_bf16_f32 v165, v170, v171\n\t" \
    "v_exp_f32 v168, v92\n\t" \
    "v_exp_f32 v169, v93\n\t" \
    "v_exp_f32 v170, v94\n\t" \
    "v_exp_f32 v171, v95\n\t" \
    "v_mfma_f32_32x32x16_bf16 v[16:31], v[120:123], v[160:163], v[16:31]\n\t" \
    "v_cvt_pk_bf16_f32 v166, v168, v169\n\t" \
    "v_cvt_pk_bf16_f32 v167, v170, v171\n\t" \
    "v_exp_f32 v168, v64\n\t" \
    "v_exp_f32 v169, v65\n\t" \
    "v_exp_f32 v170, v66\n\t" \
    "v_exp_f32 v171, v67\n\t" \
    "v_mfma_f32_32x32x16_bf16 v[80:95], v[112:115], a[248:251], 0\n\t" \
    "s_add_u32 %[t0], %[w2s], 0\n\t" \
    "buffer_load_dwordx4 a[0:3], %[w2a], %[rs], %[t0] offen offset:0 sc1\n\t" \
    "buffer_load_dwordx4 a[4:7], %[w2b], %[rs], %[t0] offen offset:0 sc1\n\t" \
    "buffer_load_dwordx4 a[8:11], %[w2a], %[rs], %[t0] offen offset:2048 sc1\n\t" \
    "buffer_load_dwordx4 a[12:15], %[w2b], %[rs], %[t0] offen offset:2048 sc1\n\t" \
    "s_add_u32 %[t0], %[w2s], 4096\n\t" \
    "buffer_load_dwordx4 a[16:19], %[w2a], %[rs], %[t0] offen offset:0 sc1\n\t" \
    "buffer_load_dwordx4 a[20:23], %[w2b], %[rs], %[t0] offen offset:0 sc1\n\t" \
    "buffer_load_dwordx4 a[24:27], %[w2a], %[rs], %[t0] offen offset:2048 sc1\n\t" \
    "buffer_load_dwordx4 a[28:31], %[w2b], %[rs], %[t0] offen offset:2048 sc1\n\t" \
    "v_cvt_pk_bf16_f32 v160, v168, v169\n\t" \
    "v_cvt_pk_bf16_f32 v161, v170, v171\n\t" \
    "v_exp_f32 v168, v68\n\t" \
    "v_exp_f32 v169, v69\n\t" \
    "v_exp_f32 v170, v70\n\t" \
    "v_exp_f32 v171, v71\n\t" \
    "v_mfma_f32_32x32x16_bf16 v[80:95], v[116:119], a[252:255], v[80:95]\n\t" \
    "v_cvt_pk_bf16_f32 v162, v168, v169\n\t" \
    "v_cvt_pk_bf16_f32 v163, v170, v171\n\t" \
    "v_exp_f32 v168, v72\n\t" \
    "v_exp_f32 v169, v73\n\t" \
    "v_exp_f32 v170, v74\n\t" \
    "v_exp_f32 v171, v75\n\t" \
    "v_mfma_f32_32x32x16_bf16 v[16:31], v[124:127], v[164:167], v[16:31]\n\t" \
    "v_cvt_pk_bf16_f32 v164, v168, v169\n\t" \
    "v_cvt_pk_bf16_f32 v165, v170, v171\n\t" \
    "v_exp_f32 v168, v76\n\t" \
    "v_exp_f32 v169, v77\n\t" \
    "v_exp_f32 v170, v78\n\t" \
    "v_exp_f32 v171, v79\n\t" \
    "v_mfma_f32_32x32x16_bf16 v[32:47], v[120:123], v[160:163], v[32:47]\n\t" \
    "v_cvt_pk_bf16_f32 v166, v168, v169\n\t" \
    "v_cvt_pk_bf16_f32 v167, v170, v171\n\t" \
    "s_waitcnt vmcnt(21)\n\t" \
    "v_exp_f32 v168, v80\n\t" \
    "v_exp_f32 v169, v81\n\t" \
    "v_exp_f32 v170, v82\n\t" \
    "v_exp_f32 v171, v83\n\t" \
    "v_mfma_f32_32x32x16_bf16 v[64:79], v[128:131], a[224:227], 0\n\t" \
    "v_cvt_pk_bf16_f32 v160, v168, v169\n\t" \
    "v_cvt_pk_bf16_f32 v161, v170, v171\n\t" \
    "v_exp_f32 v168, v84\n\t" \
    "v_exp_f32 v169, v85\n\t" \
    "v_exp_f32 v170, v86\n\t" \
    "v_exp_f32 v171, v87\n\t" \
    "v_mfma_f32_32x32x16_bf16 v[64:79], v[132:135], a[228:231], v[64:79]\n\t" \
    "v_cvt_pk_bf16_f32 v162, v168, v169\n\t" \
    "v_cvt_pk_bf16_f32 v163, v170, v171\n\t" \
    "v_exp_f32 v168, v88\n\t" \
    "v_exp_f32 v169, v89\n\t" \
    "v_exp_f32 v170, v90\n\t" \
    "v_exp_f32 v171, v91\n\t" \
    "v_mfma_f32_32x32x16_bf16 v[32:47], v[124:127], v[164:167], v[32:47]\n\t" \
    "v_cvt_pk_bf16_f32 v164, v168, v169\n\t" \
    "v_cvt_pk_bf16_f32 v165, v170, v171\n\t" \
    "v_exp_f32 v168, v92\n\t" \
    "v_exp_f32 v169, v93\n\t" \
    "v_exp_f32 v170, v94\n\t" \
    "v_exp_f32 v171, v95\n\t" \
    "v_mfma_f32_32x32x16_bf16 v[48:63], v[120:123], v[160:163], v[48:63]\n\t" \
    "v_cvt_pk_bf16_f32 v166, v168, v169\n\t" \
    "v_cvt_pk_bf16_f32 v167, v170, v171\n\t" \
    "v_exp_f32 v168, v64\n\t" \
    "v_exp_f32 v169, v65\n\t" \
    "v_exp_f32 v170, v66\n\t" \
    "v_exp_f32 v171, v67\n\t" \
    "v_mfma_f32_32x32x16_bf16 v[80:95], v[128:131], a[232:235], 0\n\t" \
    "s_min_u32 %[t0], %[wq], 49152\n\t" \
    "s_add_u32 %[t1], %[t0], %[w1s]\n\t" \
    "s_add_u32 %[t0], %[t0], 57600\n\t" \
    "s_mov_b32 m0, %[t0]\n\t" \
    "s_nop 0\n\t" \
    "buffer_load_dwordx4 %[l16], %[rs], %[t1] offen offset:0 sc1 lds\n\t" \
    "buffer_load_dwordx4 %[l16], %[rs], %[t1] offen offset:1024 sc1 lds\n\t" \
    "buffer_load_dwordx4 %[l16], %[rs], %[t1] offen offset:2048 sc1 lds\n\t" \
    "buffer_load_dwordx4 %[l16], %[rs], %[t1] offen offset:3072 sc1 lds\n\t" \
    "s_add_u32 %[wq], %[wq], 16384\n\t" \
    "v_cvt_pk_bf16_f32 v160, v168, v169\n\t" \
    "v_cvt_pk_bf16_f32 v161, v170, v171\n\t" \
    "v_exp_f32 v168, v68\n\t" \
    "v_exp_f32 v169, v69\n\t" \
    "v_exp_f32 v170, v70\n\t" \
    "v_exp_f32 v171, v71\n\t" \
    "v_mfma_f32_32x32x16_bf16 v[80:95], v[132:135], a[236:239], v[80:95]\n\t" \
    "v_cvt_pk_bf16_f32 v162, v168, v169\n\t" \
    "v_cvt_pk_bf16_f32 v163, v170, v171\n\t" \
    "v_exp_f32 v168, v72\n\t" \
    "v_exp_f32 v169, v73\n\t" \
    "v_exp_f32 v170, v74\n\t" \
    "v_exp_f32 v171, v75\n\t" \
    "v_mfma_f32_32x32x16_bf16 v[48:63], v[124:127], v[164:167], v[48:63]\n\t" \
    "v_cvt_pk_bf16_f32 v164, v168, v169\n\t" \
    "v_cvt_pk_bf16_f32 v165, v170, v171\n\t" \
    "v_exp_f32 v168, v76\n\t" \
    "v_exp_f32 v169, v77\n\t" \
    "v_exp_f32 v170, v78\n\t" \
    "v_exp_f32 v171, v79\n\t" \
    "v_mfma_f32_32x32x16_bf16 v[0:15], v[136:139], v[160:163], v[0:15]\n\t" \
    "v_cvt_pk_bf16_f32 v166, v168, v169\n\t" \
    "v_cvt_pk_bf16_f32 v167, v170, v171\n\t" \
    "v_exp_f32 v168, v80\n\t" \
    "v_exp_f32 v169, v81\n\t" \
    "v_exp_f32 v170, v82\n\t" \
    "v_exp_f32 v171, v83\n\t" \
    "v_mfma_f32_32x32x16_bf16 v[64:79], v[128:131], a[240:243], 0\n\t" \
    "v_cvt_pk_bf16_f32 v160, v168, v169\n\t" \
    "v_cvt_pk_bf16_f32 v161, v170, v171\n\t" \
    "v_exp_f32 v168, v84\n\t" \
    "v_exp_f32 v169, v85\n\t" \
    "v_exp_f32 v170, v86\n\t" \
    "v_exp_f32 v171, v87\n\t" \
    "v_mfma_f32_32x32x16_bf16 v[64:79], v[132:135], a[244:247], v[64:79]\n\t" \
    "v_cvt_pk_bf16_f32 v162, v168, v169\n\t" \
    "v_cvt_pk_bf16_f32 v163, v170, v171\n\t" \
    "v_exp_f32 v168, v88\n\t" \
    "v_exp_f32 v169, v89\n\t" \
    "v_exp_f32 v170, v90\n\t" \
    "v_exp_f32 v171, v91\n\t" \
    "v_mfma_f32_32x32x16_bf16 v[0:15], v[140:143], v[164:167], v[0:15]\n\t" \
    "v_cvt_pk_bf16_f32 v164, v168, v169\n\t" \
    "v_cvt_pk_bf16_f32 v165, v170, v171\n\t" \
    "v_exp_f32 v168, v92\n\t" \
    "v_exp_f32 v169, v93\n\t" \
    "v_exp_f32 v170, v94\n\t" \
    "v_exp_f32 v171, v95\n\t" \
    "v_mfma_f32_32x32x16_bf16 v[16:31], v[136:139], v[160:163], v[16:31]\n\t" \
    "v_cvt_pk_bf16_f32 v166, v168, v169\n\t" \
    "v_cvt_pk_bf16_f32 v167, v170, v171\n\t" \
    "v_exp_f32 v168, v64\n\t" \
    "v_exp_f32 v169, v65\n\t" \
    "v_exp_f32 v170, v66\n\t" \
    "v_exp_f32 v171, v67\n\t" \
    "v_mfma_f32_32x32x16_bf16 v[80:95], v[128:131], a[248:251], 0\n\t" \
    "v_cvt_pk_bf16_f32 v160, v168, v169\n\t" \
    "v_cvt_pk_bf16_f32 v161, v170, v171\n\t" \
    "v_exp_f32 v168, v68\n\t" \
    "v_exp_f32 v169, v69\n\t" \
    "v_exp_f32 v170, v70\n\t" \
    "v_exp_f32 v171, v71\n\t" \
    "v_mfma_f32_32x32x16_bf16 v[80:95], v[132:135], a[252:255], v[80:95]\n\t" \
    "v_cvt_pk_bf16_f32 v162, v168, v169\n\t" \
    "v_cvt_pk_bf16_f32 v163, v170, v171\n\t" \
    "v_exp_f32 v168, v72\n\t" \
    "v_exp_f32 v169, v73\n\t" \
    "v_exp_f32 v170, v74\n\t" \
    "v_exp_f32 v171, v75\n\t" \
    "v_mfma_f32_32x32x16_bf16 v[16:31], v[140:143], v[164:167], v[16:31]\n\t" \
    "s_add_u32 %[kvo], %[kvo], 4096\n\t" \
    "s_and_b32 %[kvo], %[kvo], 0xffff\n\t" \
    "s_add_u32 %[t0], %[kvs], %[kvo]\n\t" \
    "buffer_load_dwordx4 v[96:99], %[l16], %[rs], %[t0] offen offset:0 sc1\n\t" \
    "buffer_load_dwordx4 v[100:103], %[l16], %[rs], %[t0] offen offset:1024 sc1\n\t" \
    "buffer_load_dwordx4 v[104:107], %[l16], %[rs], %[t0] offen offset:2048 sc1\n\t" \
    "buffer_load_dwordx4 v[108:111], %[l16], %[rs], %[t0] offen offset:3072 sc1\n\t" \
    "s_add_u32 %[kvo], %[kvo], 4096\n\t" \
    "s_and_b32 %[kvo], %[kvo], 0xffff\n\t" \
    "s_add_u32 %[t0], %[kvs], %[kvo]\n\t" \
    "buffer_load_dwordx4 v[112:115], %[l16], %[rs], %[t0] offen offset:0 sc1\n\t" \
    "buffer_load_dwordx4 v[116:119], %[l16], %[rs], %[t0] offen offset:1024 sc1\n\t" \
    "buffer_load_dwordx4 v[120:123], %[l16], %[rs], %[t0] offen offset:2048 sc1\n\t" \
    "buffer_load_dwordx4 v[124:127], %[l16], %[rs], %[t0] offen offset:3072 sc1\n\t" \
    "v_cvt_pk_bf16_f32 v164, v168, v169\n\t" \
    "v_cvt_pk_bf16_f32 v165, v170, v171\n\t" \
    "v_exp_f32 v168, v76\n\t" \
    "v_exp_f32 v169, v77\n\t" \
    "v_exp_f32 v170, v78\n\t" \
    "v_exp_f32 v171, v79\n\t" \
    "v_mfma_f32_32x32x16_bf16 v[32:47], v[136:139], v[160:163], v[32:47]\n\t" \
    "v_cvt_pk_bf16_f32 v166, v168, v169\n\t" \
    "v_cvt_pk_bf16_f32 v167, v170, v171\n\t" \
    "s_waitcnt vmcnt(29)\n\t" \
    "v_exp_f32 v168, v80\n\t" \
    "v_exp_f32 v169, v81\n\t" \
    "v_exp_f32 v170, v82\n\t" \
    "v_exp_f32 v171, v83\n\t" \
    "v_mfma_f32_32x32x16_bf16 v[64:79], v[144:147], a[224:227], 0\n\t" \
    "v_cvt_pk_bf16_f32 v160, v168, v169\n\t" \
    "v_cvt_pk_bf16_f32 v161, v170, v171\n\t" \
    "v_exp_f32 v168, v84\n\t" \
    "v_exp_f32 v169, v85\n\t" \
    "v_exp_f32 v170, v86\n\t" \
    "v_exp_f32 v171, v87\n\t" \
    "v_mfma_f32_32x32x16_bf16 v[64:79], v[148:151], a[228:231], v[64:79]\n\t" \
    "v_cvt_pk_bf16_f32 v162, v168, v169\n\t" \
    "v_cvt_pk_bf16_f32 v163, v170, v171\n\t" \
    "v_exp_f32 v168, v88\n\t" \
    "v_exp_f32 v169, v89\n\t" \
    "v_exp_f32 v170, v90\n\t" \
    "v_exp_f32 v171, v91\n\t" \
    "v_mfma_f32_32x32x16_bf16 v[32:47], v[140:143], v[164:167], v[32:47]\n\t" \
    "v_cvt_pk_bf16_f32 v164, v168, v169\n\t" \
    "v_cvt_pk_bf16_f32 v165, v170, v171\n\t" \
    "v_exp_f32 v168, v92\n\t" \
    "v_exp_f32 v169, v93\n\t" \
    "v_exp_f32 v170, v94\n\t" \
    "v_exp_f32 v171, v95\n\t" \
    "v_mfma_f32_32x32x16_bf16 v[48:63], v[136:139], v[160:163], v[48:63]\n\t" \
    "v_cvt_pk_bf16_f32 v166, v168, v169\n\t" \
    "v_cvt_pk_bf16_f32 v167, v170, v171\n\t" \
    "v_exp_f32 v168, v64\n\t" \
    "v_exp_f32 v169, v65\n\t" \
    "v_exp_f32 v170, v66\n\t" \
    "v_exp_f32 v171, v67\n\t" \
    "v_mfma_f32_32x32x16_bf16 v[80:95], v[144:147], a[232:235], 0\n\t" \
    "s_min_u32 %[t0], %[wqo], 53248\n\t" \
    "s_add_u32 %[t1], %[t0], %[wos]\n\t" \
    "s_mov_b32 m0, %[t0]\n\t" \
    "s_nop 0\n\t" \
    "buffer_load_dwordx4 %[l16], %[rs], %[t1] offen offset:0 sc1 lds\n\t" \
    "buffer_load_dwordx4 %[l16], %[rs], %[t1] offen offset:1024 sc1 lds\n\t" \
    "buffer_load_dwordx4 %[l16], %[rs], %[t1] offen offset:2048 sc1 lds\n\t" \
    "buffer_load_dwordx4 %[l16], %[rs], %[t1] offen offset:3072 sc1 lds\n\t" \
    "s_add_u32 %[wqo], %[wqo], 16384\n\t" \
    "v_cvt_pk_bf16_f32 v160, v168, v169\n\t" \
    "v_cvt_pk_bf16_f32 v161, v170, v171\n\t" \
    "v_exp_f32 v168, v68\n\t" \
    "v_exp_f32 v169, v69\n\t" \
    "v_exp_f32 v170, v70\n\t" \
    "v_exp_f32 v171, v71\n\t" \
    "v_mfma_f32_32x32x16_bf16 v[80:95], v[148:151], a[236:239], v[80:95]\n\t" \
    "v_cvt_pk_bf16_f32 v162, v168, v169\n\t" \
    "v_cvt_pk_bf16_f32 v163, v170, v171\n\t" \
    "v_exp_f32 v168, v72\n\t" \
    "v_exp_f32 v169, v73\n\t" \
    "v_exp_f32 v170, v74\n\t" \
    "v_exp_f32 v171, v75\n\t" \
    "v_mfma_f32_32x32x16_bf16 v[48:63], v[140:143], v[164:167], v[48:63]\n\t" \
    "v_cvt_pk_bf16_f32 v164, v168, v169\n\t" \
    "v_cvt_pk_bf16_f32 v165, v170, v171\n\t" \
    "v_exp_f32 v168, v76\n\t" \
    "v_exp_f32 v169, v77\n\t" \
    "v_exp_f32 v170, v78\n\t" \
    "v_exp_f32 v171, v79\n\t" \
    "v_mfma_f32_32x32x16_bf16 v[0:15], v[152:155], v[160:163], v[0:15]\n\t" \
    "v_cvt_pk_bf16_f32 v166, v168, v169\n\t" \
    "v_cvt_pk_bf16_f32 v167, v170, v171\n\t" \
    "v_exp_f32 v168, v80\n\t" \
    "v_exp_f32 v169, v81\n\t" \
    "v_exp_f32 v170, v82\n\t" \
    "v_exp_f32 v171, v83\n\t" \
    "v_mfma_f32_32x32x16_bf16 v[64:79], v[144:147], a[240:243], 0\n\t" \
    "s_waitcnt vmcnt(24)\n\t" \
    "v_add_f32 v176, v176, v180\n\t" \
    "v_add_f32 v177, v177, v181\n\t" \
    "v_add_f32 v178, v178, v182\n\t" \
    "v_add_f32 v179, v179, v183\n\t" \
    "v_add_f32 v176, v176, v178\n\t" \
    "v_add_f32 v177, v177, v179\n\t" \
    "v_add_f32 v176, v176, v177\n\t" \
    "v_mov_b32 v177, v176\n\t" \
    "v_mov_b32 v187, 0\n\t" \
    "s_nop 0\n\t" \
    "v_permlane32_swap_b32 v176, v177\n\t" \
    "v_add_f32 v176, v176, v177\n\t" \
    "v_cvt_pk_bf16_f32 v184, v176, v176\n\t" \
    "v_and_b32 v177, 0xffff0000, v184\n\t" \
    "v_sub_f32 v176, v176, v177\n\t" \
    "v_cvt_pk_bf16_f32 v176, v176, 0\n\t" \
    "v_lshl_or_b32 v185, v186, 16, v176\n\t" \
    "ds_write_b128 %[ag], v[184:187]\n\t" \
    "v_cvt_pk_bf16_f32 v160, v168, v169\n\t" \
    "v_cvt_pk_bf16_f32 v161, v170, v171\n\t" \
    "v_exp_f32 v168, v84\n\t" \
    "v_exp_f32 v169, v85\n\t" \
    "v_exp_f32 v170, v86\n\t" \
    "v_exp_f32 v171, v87\n\t" \
    "v_mfma_f32_32x32x16_bf16 v[64:79], v[148:151], a[244:247], v[64:79]\n\t" \
    "v_cvt_pk_bf16_f32 v162, v168, v169\n\t" \
    "v_cvt_pk_bf16_f32 v163, v170, v171\n\t" \
    "v_exp_f32 v168, v88\n\t" \
    "v_exp_f32 v169, v89\n\t" \
    "v_exp_f32 v170, v90\n\t" \
    "v_exp_f32 v171, v91\n\t" \
    "v_mfma_f32_32x32x16_bf16 v[0:15], v[156:159], v[164:167], v[0:15]\n\t" \
    "v_cvt_pk_bf16_f32 v164, v168, v169\n\t" \
    "v_cvt_pk_bf16_f32 v165, v170, v171\n\t" \
    "v_exp_f32 v168, v92\n\t" \
    "v_exp_f32 v169, v93\n\t" \
    "v_exp_f32 v170, v94\n\t" \
    "v_exp_f32 v171, v95\n\t" \
    "v_mfma_f32_32x32x16_bf16 v[16:31], v[152:155], v[160:163], v[16:31]\n\t" \
    "v_cvt_pk_bf16_f32 v166, v168, v169\n\t" \
    "v_cvt_pk_bf16_f32 v167, v170, v171\n\t" \
    "v_exp_f32 v168, v64\n\t" \
    "v_exp_f32 v169, v65\n\t" \
    "v_exp_f32 v170, v66\n\t" \
    "v_exp_f32 v171, v67\n\t" \
    "v_mfma_f32_32x32x16_bf16 v[80:95], v[144:147], a[248:251], 0\n\t" \
    "s_add_u32 %[t0], %[w2s], 8192\n\t" \
    "buffer_load_dwordx4 a[32:35], %[w2a], %[rs], %[t0] offen offset:0 sc1\n\t" \
    "buffer_load_dwordx4 a[36:39], %[w2b], %[rs], %[t0] offen offset:0 sc1\n\t" \
    "buffer_load_dwordx4 a[40:43], %[w2a], %[rs], %[t0] offen offset:2048 sc1\n\t" \
    "buffer_load_dwordx4 a[44:47], %[w2b], %[rs], %[t0] offen offset:2048 sc1\n\t" \
    "s_add_u32 %[t0], %[w2s], 12288\n\t" \
    "buffer_load_dwordx4 a[48:51], %[w2a], %[rs], %[t0] offen offset:0 sc1\n\t" \
    "buffer_load_dwordx4 a[52:55], %[w2b], %[rs], %[t0] offen offset:0 sc1\n\t" \
    "buffer_load_dwordx4 a[56:59], %[w2a], %[rs], %[t0] offen offset:2048 sc1\n\t" \
    "buffer_load_dwordx4 a[60:63], %[w2b], %[rs], %[t0] offen offset:2048 sc1\n\t" \
    "v_cvt_pk_bf16_f32 v160, v168, v169\n\t" \
    "v_cvt_pk_bf16_f32 v161, v170, v171\n\t" \
    "v_exp_f32 v168, v68\n\t" \
    "v_exp_f32 v169, v69\n\t" \
    "v_exp_f32 v170, v70\n\t" \
    "v_exp_f32 v171, v71\n\t" \
    "v_mfma_f32_32x32x16_bf16 v[80:95], v[148:151], a[252:255], v[80:95]\n\t" \
    "v_cvt_pk_bf16_f32 v162, v168, v169\n\t" \
    "v_cvt_pk_bf16_f32 v163, v170, v171\n\t" \
    "v_exp_f32 v168, v72\n\t" \
    "v_exp_f32 v169, v73\n\t" \
    "v_exp_f32 v170, v74\n\t" \
    "v_exp_f32 v171, v75\n\t" \
    "v_mfma_f32_32x32x16_bf16 v[16:31], v[156:159], v[164:167], v[16:31]\n\t" \
    "v_cvt_pk_bf16_f32 v164, v168, v169\n\t" \
    "v_cvt_pk_bf16_f32 v165, v170, v171\n\t" \
    "v_exp_f32 v168, v76\n\t" \
    "v_exp_f32 v169, v77\n\t" \
    "v_exp_f32 v170, v78\n\t" \
    "v_exp_f32 v171, v79\n\t" \
    "v_mfma_f32_32x32x16_bf16 v[32:47], v[152:155], v[160:163], v[32:47]\n\t" \
    "v_cvt_pk_bf16_f32 v166, v168, v169\n\t" \
    "v_cvt_pk_bf16_f32 v167, v170, v171\n\t" \
    "s_waitcnt vmcnt(16)\n\t" \
    "v_exp_f32 v168, v80\n\t" \
    "v_exp_f32 v169, v81\n\t" \
    "v_exp_f32 v170, v82\n\t" \
    "v_exp_f32 v171, v83\n\t" \
    "v_mfma_f32_32x32x16_bf16 v[64:79], v[96:99], a[224:227], 0\n\t" \
    "v_cvt_pk_bf16_f32 v160, v168, v169\n\t" \
    "v_cvt_pk_bf16_f32 v161, v170, v171\n\t" \
    "v_exp_f32 v168, v84\n\t" \
    "v_exp_f32 v169, v85\n\t" \
    "v_exp_f32 v170, v86\n\t" \
    "v_exp_f32 v171, v87\n\t" \
    "v_mfma_f32_32x32x16_bf16 v[64:79], v[100:103], a[228:231], v[64:79]\n\t" \
    "v_cvt_pk_bf16_f32 v162, v168, v169\n\t" \
    "v_cvt_pk_bf16_f32 v163, v170, v171\n\t" \
    "v_exp_f32 v168, v88\n\t" \
    "v_exp_f32 v169, v89\n\t" \
    "v_exp_f32 v170, v90\n\t" \
    "v_exp_f32 v171, v91\n\t" \
    "v_mfma_f32_32x32x16_bf16 v[32:47], v[156:159], v[164:167], v[32:47]\n\t" \
    "v_cvt_pk_bf16_f32 v164, v168, v169\n\t" \
    "v_cvt_pk_bf16_f32 v165, v170, v171\n\t" \
    "v_exp_f32 v168, v92\n\t" \
    "v_exp_f32 v169, v93\n\t" \
    "v_exp_f32 v170, v94\n\t" \
    "v_exp_f32 v171, v95\n\t" \
    "v_mfma_f32_32x32x16_bf16 v[48:63], v[152:155], v[160:163], v[48:63]\n\t" \
    "v_cvt_pk_bf16_f32 v166, v168, v169\n\t" \
    "v_cvt_pk_bf16_f32 v167, v170, v171\n\t" \
    "v_exp_f32 v168, v64\n\t" \
    "v_exp_f32 v169, v65\n\t" \
    "v_exp_f32 v170, v66\n\t" \
    "v_exp_f32 v171, v67\n\t" \
    "v_mfma_f32_32x32x16_bf16 v[80:95], v[96:99], a[232:235], 0\n\t" \
    "v_cvt_pk_bf16_f32 v160, v168, v169\n\t" \
    "v_cvt_pk_bf16_f32 v161, v170, v171\n\t" \
    "v_exp_f32 v168, v68\n\t" \
    "v_exp_f32 v169, v69\n\t" \
    "v_exp_f32 v170, v70\n\t" \
    "v_exp_f32 v171, v71\n\t" \
    "v_mfma_f32_32x32x16_bf16 v[80:95], v[100:103], a[236:239], v[80:95]\n\t" \
    "v_cvt_pk_bf16_f32 v162, v168, v169\n\t" \
    "v_cvt_pk_bf16_f32 v163, v170, v171\n\t" \
    "v_exp_f32 v168, v72\n\t" \
    "v_exp_f32 v169, v73\n\t" \
    "v_exp_f32 v170, v74\n\t" \
    "v_exp_f32 v171, v75\n\t" \
    "v_mfma_f32_32x32x16_bf16 v[48:63], v[156:159], v[164:167], v[48:63]\n\t" \
    "v_cvt_pk_bf16_f32 v164, v168, v169\n\t" \
    "v_cvt_pk_bf16_f32 v165, v170, v171\n\t" \
    "v_exp_f32 v168, v76\n\t" \
    "v_exp_f32 v169, v77\n\t" \
    "v_exp_f32 v170, v78\n\t" \
    "v_exp_f32 v171, v79\n\t" \
    "v_mfma_f32_32x32x16_bf16 v[0:15], v[104:107], v[160:163], v[0:15]\n\t" \
    "v_cvt_pk_bf16_f32 v166, v168, v169\n\t" \
    "v_cvt_pk_bf16_f32 v167, v170, v171\n\t" \
    "v_exp_f32 v168, v80\n\t" \
    "v_exp_f32 v169, v81\n\t" \
    "v_exp_f32 v170, v82\n\t" \
    "v_exp_f32 v171, v83\n\t" \
    "v_mfma_f32_32x32x16_bf16 v[64:79], v[96:99], a[240:243], 0\n\t" \
    "v_cvt_pk_bf16_f32 v160, v168, v169\n\t" \
    "v_cvt_pk_bf16_f32 v161, v170, v171\n\t" \
    "v_exp_f32 v168, v84\n\t" \
    "v_exp_f32 v169, v85\n\t" \
    "v_exp_f32 v170, v86\n\t" \
    "v_exp_f32 v171, v87\n\t" \
    "v_mfma_f32_32x32x16_bf16 v[64:79], v[100:103], a[244:247], v[64:79]\n\t" \
    "v_cvt_pk_bf16_f32 v162, v168, v169\n\t" \
    "v_cvt_pk_bf16_f32 v163, v170, v171\n\t" \
    "v_exp_f32 v168, v88\n\t" \
    "v_exp_f32 v169, v89\n\t" \
    "v_exp_f32 v170, v90\n\t" \
    "v_exp_f32 v171, v91\n\t" \
    "v_mfma_f32_32x32x16_bf16 v[0:15], v[108:111], v[164:167], v[0:15]\n\t" \
    "v_cvt_pk_bf16_f32 v164, v168, v169\n\t" \
    "v_cvt_pk_bf16_f32 v165, v170, v171\n\t" \
    "v_exp_f32 v168, v92\n\t" \
    "v_exp_f32 v169, v93\n\t" \
    "v_exp_f32 v170, v94\n\t" \
    "v_exp_f32 v171, v95\n\t" \
    "v_mfma_f32_32x32x16_bf16 v[16:31], v[104:107], v[160:163], v[16:31]\n\t" \
    "v_cvt_pk_bf16_f32 v166, v168, v169\n\t" \
    "v_cvt_pk_bf16_f32 v167, v170, v171\n\t" \
    "v_exp_f32 v168, v64\n\t" \
    "v_exp_f32 v169, v65\n\t" \
    "v_exp_f32 v170, v66\n\t" \
    "v_exp_f32 v171, v67\n\t" \
    "v_mfma_f32_32x32x16_bf16 v[80:95], v[96:99], a[248:251], 0\n\t" \
    "v_cvt_pk_bf16_f32 v160, v168, v169\n\t" \
    "v_cvt_pk_bf16_f32 v161, v170, v171\n\t" \
    "v_exp_f32 v168, v68\n\t" \
    "v_exp_f32 v169, v69\n\t" \
    "v_exp_f32 v170, v70\n\t" \
    "v_exp_f32 v171, v71\n\t" \
    "v_mfma_f32_32x32x16_bf16 v[80:95], v[100:103], a[252:255], v[80:95]\n\t" \
    "v_cvt_pk_bf16_f32 v162, v168, v169\n\t" \
    "v_cvt_pk_bf16_f32 v163, v170, v171\n\t" \
    "v_exp_f32 v168, v72\n\t" \
    "v_exp_f32 v169, v73\n\t" \
    "v_exp_f32 v170, v74\n\t" \
    "v_exp_f32 v171, v75\n\t" \
    "v_mfma_f32_32x32x16_bf16 v[16:31], v[108:111], v[164:167], v[16:31]\n\t" \
    "s_add_u32 %[kvo], %[kvo], 4096\n\t" \
    "s_and_b32 %[kvo], %[kvo], 0xffff\n\t" \
    "s_add_u32 %[t0], %[kvs], %[kvo]\n\t" \
    "buffer_load_dwordx4 v[128:131], %[l16], %[rs], %[t0] offen offset:0 sc1\n\t" \
    "buffer_load_dwordx4 v[132:135], %[l16], %[rs], %[t0] offen offset:1024 sc1\n\t" \
    "buffer_load_dwordx4 v[136:139], %[l16], %[rs], %[t0] offen offset:2048 sc1\n\t" \
    "buffer_load_dwordx4 v[140:143], %[l16], %[rs], %[t0] offen offset:3072 sc1\n\t" \
    "s_add_u32 %[kvo], %[kvo], 4096\n\t" \
    "s_and_b32 %[kvo], %[kvo], 0xffff\n\t" \
    "s_add_u32 %[t0], %[kvs], %[kvo]\n\t" \
    "buffer_load_dwordx4 v[144:147], %[l16], %[rs], %[t0] offen offset:0 sc1\n\t" \
    "buffer_load_dwordx4 v[148:151], %[l16], %[rs], %[t0] offen offset:1024 sc1\n\t" \
    "buffer_load_dwordx4 v[152:155], %[l16], %[rs], %[t0] offen offset:2048 sc1\n\t" \
    "buffer_load_dwordx4 v[156:159], %[l16], %[rs], %[t0] offen offset:3072 sc1\n\t" \
    "v_cvt_pk_bf16_f32 v164, v168, v169\n\t" \
    "v_cvt_pk_bf16_f32 v165, v170, v171\n\t" \
    "v_exp_f32 v168, v76\n\t" \
    "v_exp_f32 v169, v77\n\t" \
    "v_exp_f32 v170, v78\n\t" \
    "v_exp_f32 v171, v79\n\t" \
    "v_mfma_f32_32x32x16_bf16 v[32:47], v[104:107], v[160:163], v[32:47]\n\t" \
    "v_cvt_pk_bf16_f32 v166, v168, v169\n\t" \
    "v_cvt_pk_bf16_f32 v167, v170, v171\n\t" \
    "s_waitcnt vmcnt(20)\n\t" \
    "v_exp_f32 v168, v80\n\t" \
    "v_exp_f32 v169, v81\n\t" \
    "v_exp_f32 v170, v82\n\t" \
    "v_exp_f32 v171, v83\n\t" \
    "v_mfma_f32_32x32x16_bf16 v[64:79], v[112:115], a[224:227], 0\n\t" \
    "v_cvt_pk_bf16_f32 v160, v168, v169\n\t" \
    "v_cvt_pk_bf16_f32 v161, v170, v171\n\t" \
    "v_exp_f32 v168, v84\n\t" \
    "v_exp_f32 v169, v85\n\t" \
    "v_exp_f32 v170, v86\n\t" \
    "v_exp_f32 v171, v87\n\t" \
    "v_mfma_f32_32x32x16_bf16 v[64:79], v[116:119], a[228:231], v[64:79]\n\t" \
    "v_cvt_pk_bf16_f32 v162, v168, v169\n\t" \
    "v_cvt_pk_bf16_f32 v163, v170, v171\n\t" \
    "v_exp_f32 v168, v88\n\t" \
    "v_exp_f32 v169, v89\n\t" \
    "v_exp_f32 v170, v90\n\t" \
    "v_exp_f32 v171, v91\n\t" \
    "v_mfma_f32_32x32x16_bf16 v[32:47], v[108:111], v[164:167], v[32:47]\n\t" \
    "v_cvt_pk_bf16_f32 v164, v168, v169\n\t" \
    "v_cvt_pk_bf16_f32 v165, v170, v171\n\t" \
    "v_exp_f32 v168, v92\n\t" \
    "v_exp_f32 v169, v93\n\t" \
    "v_exp_f32 v170, v94\n\t" \
    "v_exp_f32 v171, v95\n\t" \
    "v_mfma_f32_32x32x16_bf16 v[48:63], v[104:107], v[160:163], v[48:63]\n\t" \
    "v_cvt_pk_bf16_f32 v166, v168, v169\n\t" \
    "v_cvt_pk_bf16_f32 v167, v170, v171\n\t" \
    "v_exp_f32 v168, v64\n\t" \
    "v_exp_f32 v169, v65\n\t" \
    "v_exp_f32 v170, v66\n\t" \
    "v_exp_f32 v171, v67\n\t" \
    "v_mfma_f32_32x32x16_bf16 v[80:95], v[112:115], a[232:235], 0\n\t" \
    "v_cvt_pk_bf16_f32 v160, v168, v169\n\t" \
    "v_cvt_pk_bf16_f32 v161, v170, v171\n\t" \
    "v_exp_f32 v168, v68\n\t" \
    "v_exp_f32 v169, v69\n\t" \
    "v_exp_f32 v170, v70\n\t" \
    "v_exp_f32 v171, v71\n\t" \
    "v_mfma_f32_32x32x16_bf16 v[80:95], v[116:119], a[236:239], v[80:95]\n\t" \
    "v_cvt_pk_bf16_f32 v162, v168, v169\n\t" \
    "v_cvt_pk_bf16_f32 v163, v170, v171\n\t" \
    "v_exp_f32 v168, v72\n\t" \
    "v_exp_f32 v169, v73\n\t" \
    "v_exp_f32 v170, v74\n\t" \
    "v_exp_f32 v171, v75\n\t" \
    "v_mfma_f32_32x32x16_bf16 v[48:63], v[108:111], v[164:167], v[48:63]\n\t" \
    "v_cvt_pk_bf16_f32 v164, v168, v169\n\t" \
    "v_cvt_pk_bf16_f32 v165, v170, v171\n\t" \
    "v_exp_f32 v168, v76\n\t" \
    "v_exp_f32 v169, v77\n\t" \
    "v_exp_f32 v170, v78\n\t" \
    "v_exp_f32 v171, v79\n\t" \
    "v_mfma_f32_32x32x16_bf16 v[0:15], v[120:123], v[160:163], v[0:15]\n\t" \
    "v_cvt_pk_bf16_f32 v166, v168, v169\n\t" \
    "v_cvt_pk_bf16_f32 v167, v170, v171\n\t" \
    "v_exp_f32 v168, v80\n\t" \
    "v_exp_f32 v169, v81\n\t" \
    "v_exp_f32 v170, v82\n\t" \
    "v_exp_f32 v171, v83\n\t" \
    "v_mfma_f32_32x32x16_bf16 v[64:79], v[112:115], a[240:243], 0\n\t" \
    "v_cvt_pk_bf16_f32 v160, v168, v169\n\t" \
    "v_cvt_pk_bf16_f32 v161, v170, v171\n\t" \
    "v_exp_f32 v168, v84\n\t" \
    "v_exp_f32 v169, v85\n\t" \
    "v_exp_f32 v170, v86\n\t" \
    "v_exp_f32 v171, v87\n\t" \
    "v_mfma_f32_32x32x16_bf16 v[64:79], v[116:119], a[244:247], v[64:79]\n\t" \
    "v_cvt_pk_bf16_f32 v162, v168, v169\n\t" \
    "v_cvt_pk_bf16_f32 v163, v170, v171\n\t" \
    "v_exp_f32 v168, v88\n\t" \
    "v_exp_f32 v169, v89\n\t" \
    "v_exp_f32 v170, v90\n\t" \
    "v_exp_f32 v171, v91\n\t" \
    "v_mfma_f32_32x32x16_bf16 v[0:15], v[124:127], v[164:167], v[0:15]\n\t" \
    "v_cvt_pk_bf16_f32 v164, v168, v169\n\t" \
    "v_cvt_pk_bf16_f32 v165, v170, v171\n\t" \
    "v_exp_f32 v168, v92\n\t" \
    "v_exp_f32 v169, v93\n\t" \
    "v_exp_f32 v170, v94\n\t" \
    "v_exp_f32 v171, v95\n\t" \
    "v_mfma_f32_32x32x16_bf16 v[16:31], v[120:123], v[160:163], v[16:31]\n\t" \
    "v_cvt_pk_bf16_f32 v166, v168, v169\n\t" \
    "v_cvt_pk_bf16_f32 v167, v170, v171\n\t" \
    "v_exp_f32 v168, v64\n\t" \
    "v_exp_f32 v169, v65\n\t" \
    "v_exp_f32 v170, v66\n\t" \
    "v_exp_f32 v171, v67\n\t" \
    "v_mfma_f32_32x32x16_bf16 v[80:95], v[112:115], a[248:251], 0\n\t" \
    "s_add_u32 %[t0], %[w2s], 16384\n\t" \
    "buffer_load_dwordx4 a[64:67], %[w2a], %[rs], %[t0] offen offset:0 sc1\n\t" \
    "buffer_load_dwordx4 a[68:71], %[w2b], %[rs], %[t0] offen offset:0 sc1\n\t" \
    "buffer_load_dwordx4 a[72:75], %[w2a], %[rs], %[t0] offen offset:2048 sc1\n\t" \
    "buffer_load_dwordx4 a[76:79], %[w2b], %[rs], %[t0] offen offset:2048 sc1\n\t" \
    "s_add_u32 %[t0], %[w2s], 20480\n\t" \
    "buffer_load_dwordx4 a[80:83], %[w2a], %[rs], %[t0] offen offset:0 sc1\n\t" \
    "buffer_load_dwordx4 a[84:87], %[w2b], %[rs], %[t0] offen offset:0 sc1\n\t" \
    "buffer_load_dwordx4 a[88:91], %[w2a], %[rs], %[t0] offen offset:2048 sc1\n\t" \
    "buffer_load_dwordx4 a[92:95], %[w2b], %[rs], %[t0] offen offset:2048 sc1\n\t" \
    "v_cvt_pk_bf16_f32 v160, v168, v169\n\t" \
    "v_cvt_pk_bf16_f32 v161, v170, v171\n\t" \
    "v_exp_f32 v168, v68\n\t" \
    "v_exp_f32 v169, v69\n\t" \
    "v_exp_f32 v170, v70\n\t" \
    "v_exp_f32 v171, v71\n\t" \
    "v_mfma_f32_32x32x16_bf16 v[80:95], v[116:119], a[252:255], v[80:95]\n\t" \
    "v_cvt_pk_bf16_f32 v162, v168, v169\n\t" \
    "v_cvt_pk_bf16_f32 v163, v170, v171\n\t" \
    "v_exp_f32 v168, v72\n\t" \
    "v_exp_f32 v169, v73\n\t" \
    "v_exp_f32 v170, v74\n\t" \
    "v_exp_f32 v171, v75\n\t" \
    "v_mfma_f32_32x32x16_bf16 v[16:31], v[124:127], v[164:167], v[16:31]\n\t" \
    "v_cvt_pk_bf16_f32 v164, v168, v169\n\t" \
    "v_cvt_pk_bf16_f32 v165, v170, v171\n\t" \
    "v_exp_f32 v168, v76\n\t" \
    "v_exp_f32 v169, v77\n\t" \
    "v_exp_f32 v170, v78\n\t" \
    "v_exp_f32 v171, v79\n\t" \
    "v_mfma_f32_32x32x16_bf16 v[32:47], v[120:123], v[160:163], v[32:47]\n\t" \
    "v_cvt_pk_bf16_f32 v166, v168, v169\n\t" \
    "v_cvt_pk_bf16_f32 v167, v170, v171\n\t" \
    "s_waitcnt vmcnt(12)\n\t" \
    "v_exp_f32 v168, v80\n\t" \
    "v_exp_f32 v169, v81\n\t" \
    "v_exp_f32 v170, v82\n\t" \
    "v_exp_f32 v171, v83\n\t" \
    "v_mfma_f32_32x32x16_bf16 v[64:79], v[128:131], a[224:227], 0\n\t" \
    "v_cvt_pk_bf16_f32 v160, v168, v169\n\t" \
    "v_cvt_pk_bf16_f32 v161, v170, v171\n\t" \
    "v_exp_f32 v168, v84\n\t" \
    "v_exp_f32 v169, v85\n\t" \
    "v_exp_f32 v170, v86\n\t" \
    "v_exp_f32 v171, v87\n\t" \
    "v_mfma_f32_32x32x16_bf16 v[64:79], v[132:135], a[228:231], v[64:79]\n\t" \
    "v_cvt_pk_bf16_f32 v162, v168, v169\n\t" \
    "v_cvt_pk_bf16_f32 v163, v170, v171\n\t" \
    "v_exp_f32 v168, v88\n\t" \
    "v_exp_f32 v169, v89\n\t" \
    "v_exp_f32 v170, v90\n\t" \
    "v_exp_f32 v171, v91\n\t" \
    "v_mfma_f32_32x32x16_bf16 v[32:47], v[124:127], v[164:167], v[32:47]\n\t" \
    "v_cvt_pk_bf16_f32 v164, v168, v169\n\t" \
    "v_cvt_pk_bf16_f32 v165, v170, v171\n\t" \
    "v_exp_f32 v168, v92\n\t" \
    "v_exp_f32 v169, v93\n\t" \
    "v_exp_f32 v170, v94\n\t" \
    "v_exp_f32 v171, v95\n\t" \
    "v_mfma_f32_32x32x16_bf16 v[48:63], v[120:123], v[160:163], v[48:63]\n\t" \
    "v_cvt_pk_bf16_f32 v166, v168, v169\n\t" \
    "v_cvt_pk_bf16_f32 v167, v170, v171\n\t" \
    "v_exp_f32 v168, v64\n\t" \
    "v_exp_f32 v169, v65\n\t" \
    "v_exp_f32 v170, v66\n\t" \
    "v_exp_f32 v171, v67\n\t" \
    "v_mfma_f32_32x32x16_bf16 v[80:95], v[128:131], a[232:235], 0\n\t" \
    "s_min_u32 %[t0], %[wq], 49152\n\t" \
    "s_add_u32 %[t1], %[t0], %[w1s]\n\t" \
    "s_add_u32 %[t0], %[t0], 57600\n\t" \
    "s_mov_b32 m0, %[t0]\n\t" \
    "s_nop 0\n\t" \
    "buffer_load_dwordx4 %[l16], %[rs], %[t1] offen offset:0 sc1 lds\n\t" \
    "buffer_load_dwordx4 %[l16], %[rs], %[t1] offen offset:1024 sc1 lds\n\t" \
    "buffer_load_dwordx4 %[l16], %[rs], %[t1] offen offset:2048 sc1 lds\n\t" \
    "buffer_load_dwordx4 %[l16], %[rs], %[t1] offen offset:3072 sc1 lds\n\t" \
    "s_add_u32 %[wq], %[wq], 16384\n\t" \
    "v_cvt_pk_bf16_f32 v160, v168, v169\n\t" \
    "v_cvt_pk_bf16_f32 v161, v170, v171\n\t" \
    "v_exp_f32 v168, v68\n\t" \
    "v_exp_f32 v169, v69\n\t" \
    "v_exp_f32 v170, v70\n\t" \
    "v_exp_f32 v171, v71\n\t" \
    "v_mfma_f32_32x32x16_bf16 v[80:95], v[132:135], a[236:239], v[80:95]\n\t" \
    "v_cvt_pk_bf16_f32 v162, v168, v169\n\t" \
    "v_cvt_pk_bf16_f32 v163, v170, v171\n\t" \
    "v_exp_f32 v168, v72\n\t" \
    "v_exp_f32 v169, v73\n\t" \
    "v_exp_f32 v170, v74\n\t" \
    "v_exp_f32 v171, v75\n\t" \
    "v_mfma_f32_32x32x16_bf16 v[48:63], v[124:127], v[164:167], v[48:63]\n\t" \
    "v_cvt_pk_bf16_f32 v164, v168, v169\n\t" \
    "v_cvt_pk_bf16_f32 v165, v170, v171\n\t" \
    "v_exp_f32 v168, v76\n\t" \
    "v_exp_f32 v169, v77\n\t" \
    "v_exp_f32 v170, v78\n\t" \
    "v_exp_f32 v171, v79\n\t" \
    "v_mfma_f32_32x32x16_bf16 v[0:15], v[136:139], v[160:163], v[0:15]\n\t" \
    "v_cvt_pk_bf16_f32 v166, v168, v169\n\t" \
    "v_cvt_pk_bf16_f32 v167, v170, v171\n\t" \
    "v_exp_f32 v168, v80\n\t" \
    "v_exp_f32 v169, v81\n\t" \
    "v_exp_f32 v170, v82\n\t" \
    "v_exp_f32 v171, v83\n\t" \
    "v_mfma_f32_32x32x16_bf16 v[64:79], v[128:131], a[240:243], 0\n\t" \
    "v_cvt_pk_bf16_f32 v160, v168, v169\n\t" \
    "v_cvt_pk_bf16_f32 v161, v170, v171\n\t" \
    "v_exp_f32 v168, v84\n\t" \
    "v_exp_f32 v169, v85\n\t" \
    "v_exp_f32 v170, v86\n\t" \
    "v_exp_f32 v171, v87\n\t" \
    "v_mfma_f32_32x32x16_bf16 v[64:79], v[132:135], a[244:247], v[64:79]\n\t" \
    "v_cvt_pk_bf16_f32 v162, v168, v169\n\t" \
    "v_cvt_pk_bf16_f32 v163, v170, v171\n\t" \
    "v_exp_f32 v168, v88\n\t" \
    "v_exp_f32 v169, v89\n\t" \
    "v_exp_f32 v170, v90\n\t" \
    "v_exp_f32 v171, v91\n\t" \
    "v_mfma_f32_32x32x16_bf16 v[0:15], v[140:143], v[164:167], v[0:15]\n\t" \
    "v_cvt_pk_bf16_f32 v164, v168, v169\n\t" \
    "v_cvt_pk_bf16_f32 v165, v170, v171\n\t" \
    "v_exp_f32 v168, v92\n\t" \
    "v_exp_f32 v169, v93\n\t" \
    "v_exp_f32 v170, v94\n\t" \
    "v_exp_f32 v171, v95\n\t" \
    "v_mfma_f32_32x32x16_bf16 v[16:31], v[136:139], v[160:163], v[16:31]\n\t" \
    "v_cvt_pk_bf16_f32 v166, v168, v169\n\t" \
    "v_cvt_pk_bf16_f32 v167, v170, v171\n\t" \
    "v_exp_f32 v168, v64\n\t" \
    "v_exp_f32 v169, v65\n\t" \
    "v_exp_f32 v170, v66\n\t" \
    "v_exp_f32 v171, v67\n\t" \
    "v_mfma_f32_32x32x16_bf16 v[80:95], v[128:131], a[248:251], 0\n\t" \
    "v_cvt_pk_bf16_f32 v160, v168, v169\n\t" \
    "v_cvt_pk_bf16_f32 v161, v170, v171\n\t" \
    "v_exp_f32 v168, v68\n\t" \
    "v_exp_f32 v169, v69\n\t" \
    "v_exp_f32 v170, v70\n\t" \
    "v_exp_f32 v171, v71\n\t" \
    "v_mfma_f32_32x32x16_bf16 v[80:95], v[132:135], a[252:255], v[80:95]\n\t" \
    "v_cvt_pk_bf16_f32 v162, v168, v169\n\t" \
    "v_cvt_pk_bf16_f32 v163, v170, v171\n\t" \
    "v_exp_f32 v168, v72\n\t" \
    "v_exp_f32 v169, v73\n\t" \
    "v_exp_f32 v170, v74\n\t" \
    "v_exp_f32 v171, v75\n\t" \
    "v_mfma_f32_32x32x16_bf16 v[16:31], v[140:143], v[164:167], v[16:31]\n\t" \
    "s_add_u32 %[kvo], %[kvo], 4096\n\t" \
    "s_and_b32 %[kvo], %[kvo], 0xffff\n\t" \
    "s_add_u32 %[t0], %[kvs], %[kvo]\n\t" \
    "buffer_load_dwordx4 v[96:99], %[l16], %[rs], %[t0] offen offset:0 sc1\n\t" \
    "buffer_load_dwordx4 v[100:103], %[l16], %[rs], %[t0] offen offset:1024 sc1\n\t" \
    "buffer_load_dwordx4 v[104:107], %[l16], %[rs], %[t0] offen offset:2048 sc1\n\t" \
    "buffer_load_dwordx4 v[108:111], %[l16], %[rs], %[t0] offen offset:3072 sc1\n\t" \
    "s_add_u32 %[kvo], %[kvo], 4096\n\t" \
    "s_and_b32 %[kvo], %[kvo], 0xffff\n\t" \
    "s_add_u32 %[t0], %[kvs], %[kvo]\n\t" \
    "buffer_load_dwordx4 v[112:115], %[l16], %[rs], %[t0] offen offset:0 sc1\n\t" \
    "buffer_load_dwordx4 v[116:119], %[l16], %[rs], %[t0] offen offset:1024 sc1\n\t" \
    "buffer_load_dwordx4 v[120:123], %[l16], %[rs], %[t0] offen offset:2048 sc1\n\t" \
    "buffer_load_dwordx4 v[124:127], %[l16], %[rs], %[t0] offen offset:3072 sc1\n\t" \
    "v_cvt_pk_bf16_f32 v164, v168, v169\n\t" \
    "v_cvt_pk_bf16_f32 v165, v170, v171\n\t" \
    "v_exp_f32 v168, v76\n\t" \
    "v_exp_f32 v169, v77\n\t" \
    "v_exp_f32 v170, v78\n\t" \
    "v_exp_f32 v171, v79\n\t" \
    "v_mfma_f32_32x32x16_bf16 v[32:47], v[136:139], v[160:163], v[32:47]\n\t" \
    "v_cvt_pk_bf16_f32 v166, v168, v169\n\t" \
    "v_cvt_pk_bf16_f32 v167, v170, v171\n\t" \
    "s_waitcnt vmcnt(20)\n\t" \
    "v_exp_f32 v168, v80\n\t" \
    "v_exp_f32 v169, v81\n\t" \
    "v_exp_f32 v170, v82\n\t" \
    "v_exp_f32 v171, v83\n\t" \
    "v_mfma_f32_32x32x16_bf16 v[64:79], v[144:147], a[224:227], 0\n\t" \
    "v_cvt_pk_bf16_f32 v160, v168, v169\n\t" \
    "v_cvt_pk_bf16_f32 v161, v170, v171\n\t" \
    "v_exp_f32 v168, v84\n\t" \
    "v_exp_f32 v169, v85\n\t" \
    "v_exp_f32 v170, v86\n\t" \
    "v_exp_f32 v171, v87\n\t" \
    "v_mfma_f32_32x32x16_bf16 v[64:79], v[148:151], a[228:231], v[64:79]\n\t" \
    "v_cvt_pk_bf16_f32 v162, v168, v169\n\t" \
    "v_cvt_pk_bf16_f32 v163, v170, v171\n\t" \
    "v_exp_f32 v168, v88\n\t" \
    "v_exp_f32 v169, v89\n\t" \
    "v_exp_f32 v170, v90\n\t" \
    "v_exp_f32 v171, v91\n\t" \
    "v_mfma_f32_32x32x16_bf16 v[32:47], v[140:143], v[164:167], v[32:47]\n\t" \
    "v_cvt_pk_bf16_f32 v164, v168, v169\n\t" \
    "v_cvt_pk_bf16_f32 v165, v170, v171\n\t" \
    "v_exp_f32 v168, v92\n\t" \
    "v_exp_f32 v169, v93\n\t" \
    "v_exp_f32 v170, v94\n\t" \
    "v_exp_f32 v171, v95\n\t" \
    "v_mfma_f32_32x32x16_bf16 v[48:63], v[136:139], v[160:163], v[48:63]\n\t" \
    "v_cvt_pk_bf16_f32 v166, v168, v169\n\t" \
    "v_cvt_pk_bf16_f32 v167, v170, v171\n\t" \
    "v_exp_f32 v168, v64\n\t" \
    "v_exp_f32 v169, v65\n\t" \
    "v_exp_f32 v170, v66\n\t" \
    "v_exp_f32 v171, v67\n\t" \
    "v_mfma_f32_32x32x16_bf16 v[80:95], v[144:147], a[232:235], 0\n\t" \
    "s_min_u32 %[t0], %[wqo], 53248\n\t" \
    "s_add_u32 %[t1], %[t0], %[wos]\n\t" \
    "s_mov_b32 m0, %[t0]\n\t" \
    "s_nop 0\n\t" \
    "buffer_load_dwordx4 %[l16], %[rs], %[t1] offen offset:0 sc1 lds\n\t" \
    "buffer_load_dwordx4 %[l16], %[rs], %[t1] offen offset:1024 sc1 lds\n\t" \
    "buffer_load_dwordx4 %[l16], %[rs], %[t1] offen offset:2048 sc1 lds\n\t" \
    "buffer_load_dwordx4 %[l16], %[rs], %[t1] offen offset:3072 sc1 lds\n\t" \
    "s_add_u32 %[wqo], %[wqo], 16384\n\t" \
    "v_cvt_pk_bf16_f32 v160, v168, v169\n\t" \
    "v_cvt_pk_bf16_f32 v161, v170, v171\n\t" \
    "v_exp_f32 v168, v68\n\t" \
    "v_exp_f32 v169, v69\n\t" \
    "v_exp_f32 v170, v70\n\t" \
    "v_exp_f32 v171, v71\n\t" \
    "v_mfma_f32_32x32x16_bf16 v[80:95], v[148:151], a[236:239], v[80:95]\n\t" \
    "v_cvt_pk_bf16_f32 v162, v168, v169\n\t" \
    "v_cvt_pk_bf16_f32 v163, v170, v171\n\t" \
    "v_exp_f32 v168, v72\n\t" \
    "v_exp_f32 v169, v73\n\t" \
    "v_exp_f32 v170, v74\n\t" \
    "v_exp_f32 v171, v75\n\t" \
    "v_mfma_f32_32x32x16_bf16 v[48:63], v[140:143], v[164:167], v[48:63]\n\t" \
    "v_cvt_pk_bf16_f32 v164, v168, v169\n\t" \
    "v_cvt_pk_bf16_f32 v165, v170, v171\n\t" \
    "v_exp_f32 v168, v76\n\t" \
    "v_exp_f32 v169, v77\n\t" \
    "v_exp_f32 v170, v78\n\t" \
    "v_exp_f32 v171, v79\n\t" \
    "v_mfma_f32_32x32x16_bf16 v[0:15], v[152:155], v[160:163], v[0:15]\n\t" \
    "v_cvt_pk_bf16_f32 v166, v168, v169\n\t" \
    "v_cvt_pk_bf16_f32 v167, v170, v171\n\t" \
    "v_exp_f32 v168, v80\n\t" \
    "v_exp_f32 v169, v81\n\t" \
    "v_exp_f32 v170, v82\n\t" \
    "v_exp_f32 v171, v83\n\t" \
    "v_mfma_f32_32x32x16_bf16 v[64:79], v[144:147], a[240:243], 0\n\t" \
    "v_cvt_pk_bf16_f32 v160, v168, v169\n\t" \
    "v_cvt_pk_bf16_f32 v161, v170, v171\n\t" \
    "v_exp_f32 v168, v84\n\t" \
    "v_exp_f32 v169, v85\n\t" \
    "v_exp_f32 v170, v86\n\t" \
    "v_exp_f32 v171, v87\n\t" \
    "v_mfma_f32_32x32x16_bf16 v[64:79], v[148:151], a[244:247], v[64:79]\n\t" \
    "v_cvt_pk_bf16_f32 v162, v168, v169\n\t" \
    "v_cvt_pk_bf16_f32 v163, v170, v171\n\t" \
    "v_exp_f32 v168, v88\n\t" \
    "v_exp_f32 v169, v89\n\t" \
    "v_exp_f32 v170, v90\n\t" \
    "v_exp_f32 v171, v91\n\t" \
    "v_mfma_f32_32x32x16_bf16 v[0:15], v[156:159], v[164:167], v[0:15]\n\t" \
    "v_cvt_pk_bf16_f32 v164, v168, v169\n\t" \
    "v_cvt_pk_bf16_f32 v165, v170, v171\n\t" \
    "v_exp_f32 v168, v92\n\t" \
    "v_exp_f32 v169, v93\n\t" \
    "v_exp_f32 v170, v94\n\t" \
    "v_exp_f32 v171, v95\n\t" \
    "v_mfma_f32_32x32x16_bf16 v[16:31], v[152:155], v[160:163], v[16:31]\n\t" \
    "v_cvt_pk_bf16_f32 v166, v168, v169\n\t" \
    "v_cvt_pk_bf16_f32 v167, v170, v171\n\t" \
    "v_exp_f32 v168, v64\n\t" \
    "v_exp_f32 v169, v65\n\t" \
    "v_exp_f32 v170, v66\n\t" \
    "v_exp_f32 v171, v67\n\t" \
    "v_mfma_f32_32x32x16_bf16 v[80:95], v[144:147], a[248:251], 0\n\t" \
    "s_add_u32 %[t0], %[w2s], 24576\n\t" \
    "buffer_load_dwordx4 a[96:99], %[w2a], %[rs], %[t0] offen offset:0 sc1\n\t" \
    "buffer_load_dwordx4 a[100:103], %[w2b], %[rs], %[t0] offen offset:0 sc1\n\t" \
    "buffer_load_dwordx4 a[104:107], %[w2a], %[rs], %[t0] offen offset:2048 sc1\n\t" \
    "buffer_load_dwordx4 a[108:111], %[w2b], %[rs], %[t0] offen offset:2048 sc1\n\t" \
    "s_add_u32 %[t0], %[w2s], 28672\n\t" \
    "buffer_load_dwordx4 a[112:115], %[w2a], %[rs], %[t0] offen offset:0 sc1\n\t" \
    "buffer_load_dwordx4 a[116:119], %[w2b], %[rs], %[t0] offen offset:0 sc1\n\t" \
    "buffer_load_dwordx4 a[120:123], %[w2a], %[rs], %[t0] offen offset:2048 sc1\n\t" \
    "buffer_load_dwordx4 a[124:127], %[w2b], %[rs], %[t0] offen offset:2048 sc1\n\t" \
    "v_cvt_pk_bf16_f32 v160, v168, v169\n\t" \
    "v_cvt_pk_bf16_f32 v161, v170, v171\n\t" \
    "v_exp_f32 v168, v68\n\t" \
    "v_exp_f32 v169, v69\n\t" \
    "v_exp_f32 v170, v70\n\t" \
    "v_exp_f32 v171, v71\n\t" \
    "v_mfma_f32_32x32x16_bf16 v[80:95], v[148:151], a[252:255], v[80:95]\n\t" \
    "v_cvt_pk_bf16_f32 v162, v168, v169\n\t" \
    "v_cvt_pk_bf16_f32 v163, v170, v171\n\t" \
    "v_exp_f32 v168, v72\n\t" \
    "v_exp_f32 v169, v73\n\t" \
    "v_exp_f32 v170, v74\n\t" \
    "v_exp_f32 v171, v75\n\t" \
    "v_mfma_f32_32x32x16_bf16 v[16:31], v[156:159], v[164:167], v[16:31]\n\t" \
    "v_cvt_pk_bf16_f32 v164, v168, v169\n\t" \
    "v_cvt_pk_bf16_f32 v165, v170, v171\n\t" \
    "v_exp_f32 v168, v76\n\t" \
    "v_exp_f32 v169, v77\n\t" \
    "v_exp_f32 v170, v78\n\t" \
    "v_exp_f32 v171, v79\n\t" \
    "v_mfma_f32_32x32x16_bf16 v[32:47], v[152:155], v[160:163], v[32:47]\n\t" \
    "v_cvt_pk_bf16_f32 v166, v168, v169\n\t" \
    "v_cvt_pk_bf16_f32 v167, v170, v171\n\t" \
    "s_waitcnt vmcnt(16)\n\t" \
    "v_exp_f32 v168, v80\n\t" \
    "v_exp_f32 v169, v81\n\t" \
    "v_exp_f32 v170, v82\n\t" \
    "v_exp_f32 v171, v83\n\t" \
    "v_mfma_f32_32x32x16_bf16 v[64:79], v[96:99], a[224:227], 0\n\t" \
    "v_cvt_pk_bf16_f32 v160, v168, v169\n\t" \
    "v_cvt_pk_bf16_f32 v161, v170, v171\n\t" \
    "v_exp_f32 v168, v84\n\t" \
    "v_exp_f32 v169, v85\n\t" \
    "v_exp_f32 v170, v86\n\t" \
    "v_exp_f32 v171, v87\n\t" \
    "v_mfma_f32_32x32x16_bf16 v[64:79], v[100:103], a[228:231], v[64:79]\n\t" \
    "v_cvt_pk_bf16_f32 v162, v168, v169\n\t" \
    "v_cvt_pk_bf16_f32 v163, v170, v171\n\t" \
    "v_exp_f32 v168, v88\n\t" \
    "v_exp_f32 v169, v89\n\t" \
    "v_exp_f32 v170, v90\n\t" \
    "v_exp_f32 v171, v91\n\t" \
    "v_mfma_f32_32x32x16_bf16 v[32:47], v[156:159], v[164:167], v[32:47]\n\t" \
    "v_cvt_pk_bf16_f32 v164, v168, v169\n\t" \
    "v_cvt_pk_bf16_f32 v165, v170, v171\n\t" \
    "v_exp_f32 v168, v92\n\t" \
    "v_exp_f32 v169, v93\n\t" \
    "v_exp_f32 v170, v94\n\t" \
    "v_exp_f32 v171, v95\n\t" \
    "v_mfma_f32_32x32x16_bf16 v[48:63], v[152:155], v[160:163], v[48:63]\n\t" \
    "v_cvt_pk_bf16_f32 v166, v168, v169\n\t" \
    "v_cvt_pk_bf16_f32 v167, v170, v171\n\t" \
    "v_exp_f32 v168, v64\n\t" \
    "v_exp_f32 v169, v65\n\t" \
    "v_exp_f32 v170, v66\n\t" \
    "v_exp_f32 v171, v67\n\t" \
    "v_mfma_f32_32x32x16_bf16 v[80:95], v[96:99], a[232:235], 0\n\t" \
    "v_cvt_pk_bf16_f32 v160, v168, v169\n\t" \
    "v_cvt_pk_bf16_f32 v161, v170, v171\n\t" \
    "v_exp_f32 v168, v68\n\t" \
    "v_exp_f32 v169, v69\n\t" \
    "v_exp_f32 v170, v70\n\t" \
    "v_exp_f32 v171, v71\n\t" \
    "v_mfma_f32_32x32x16_bf16 v[80:95], v[100:103], a[236:239], v[80:95]\n\t" \
    "v_cvt_pk_bf16_f32 v162, v168, v169\n\t" \
    "v_cvt_pk_bf16_f32 v163, v170, v171\n\t" \
    "v_exp_f32 v168, v72\n\t" \
    "v_exp_f32 v169, v73\n\t" \
    "v_exp_f32 v170, v74\n\t" \
    "v_exp_f32 v171, v75\n\t" \
    "v_mfma_f32_32x32x16_bf16 v[48:63], v[156:159], v[164:167], v[48:63]\n\t" \
    "v_cvt_pk_bf16_f32 v164, v168, v169\n\t" \
    "v_cvt_pk_bf16_f32 v165, v170, v171\n\t" \
    "v_exp_f32 v168, v76\n\t" \
    "v_exp_f32 v169, v77\n\t" \
    "v_exp_f32 v170, v78\n\t" \
    "v_exp_f32 v171, v79\n\t" \
    "v_mfma_f32_32x32x16_bf16 v[0:15], v[104:107], v[160:163], v[0:15]\n\t" \
    "v_cvt_pk_bf16_f32 v166, v168, v169\n\t" \
    "v_cvt_pk_bf16_f32 v167, v170, v171\n\t" \
    "v_exp_f32 v168, v80\n\t" \
    "v_exp_f32 v169, v81\n\t" \
    "v_exp_f32 v170, v82\n\t" \
    "v_exp_f32 v171, v83\n\t" \
    "v_mfma_f32_32x32x16_bf16 v[64:79], v[96:99], a[240:243], 0\n\t" \
    "v_cvt_pk_bf16_f32 v160, v168, v169\n\t" \
    "v_cvt_pk_bf16_f32 v161, v170, v171\n\t" \
    "v_exp_f32 v168, v84\n\t" \
    "v_exp_f32 v169, v85\n\t" \
    "v_exp_f32 v170, v86\n\t" \
    "v_exp_f32 v171, v87\n\t" \
    "v_mfma_f32_32x32x16_bf16 v[64:79], v[100:103], a[244:247], v[64:79]\n\t" \
    "v_cvt_pk_bf16_f32 v162, v168, v169\n\t" \
    "v_cvt_pk_bf16_f32 v163, v170, v171\n\t" \
    "v_exp_f32 v168, v88\n\t" \
    "v_exp_f32 v169, v89\n\t" \
    "v_exp_f32 v170, v90\n\t" \
    "v_exp_f32 v171, v91\n\t" \
    "v_mfma_f32_32x32x16_bf16 v[0:15], v[108:111], v[164:167], v[0:15]\n\t" \
    "v_cvt_pk_bf16_f32 v164, v168, v169\n\t" \
    "v_cvt_pk_bf16_f32 v165, v170, v171\n\t" \
    "v_exp_f32 v168, v92\n\t" \
    "v_exp_f32 v169, v93\n\t" \
    "v_exp_f32 v170, v94\n\t" \
    "v_exp_f32 v171, v95\n\t" \
    "v_mfma_f32_32x32x16_bf16 v[16:31], v[104:107], v[160:163], v[16:31]\n\t" \
    "v_cvt_pk_bf16_f32 v166, v168, v169\n\t" \
    "v_cvt_pk_bf16_f32 v167, v170, v171\n\t" \
    "v_exp_f32 v168, v64\n\t" \
    "v_exp_f32 v169, v65\n\t" \
    "v_exp_f32 v170, v66\n\t" \
    "v_exp_f32 v171, v67\n\t" \
    "v_mfma_f32_32x32x16_bf16 v[80:95], v[96:99], a[248:251], 0\n\t" \
    "v_cvt_pk_bf16_f32 v160, v168, v169\n\t" \
    "v_cvt_pk_bf16_f32 v161, v170, v171\n\t" \
    "v_exp_f32 v168, v68\n\t" \
    "v_exp_f32 v169, v69\n\t" \
    "v_exp_f32 v170, v70\n\t" \
    "v_exp_f32 v171, v71\n\t" \
    "v_mfma_f32_32x32x16_bf16 v[80:95], v[100:103], a[252:255], v[80:95]\n\t" \
    "v_cvt_pk_bf16_f32 v162, v168, v169\n\t" \
    "v_cvt_pk_bf16_f32 v163, v170, v171\n\t" \
    "v_exp_f32 v168, v72\n\t" \
    "v_exp_f32 v169, v73\n\t" \
    "v_exp_f32 v170, v74\n\t" \
    "v_exp_f32 v171, v75\n\t" \
    "v_mfma_f32_32x32x16_bf16 v[16:31], v[108:111], v[164:167], v[16:31]\n\t" \
    "s_add_u32 %[kvo], %[kvo], 4096\n\t" \
    "s_and_b32 %[kvo], %[kvo], 0xffff\n\t" \
    "s_add_u32 %[t0], %[kvs], %[kvo]\n\t" \
    "buffer_load_dwordx4 v[128:131], %[l16], %[rs], %[t0] offen offset:0 sc1\n\t" \
    "buffer_load_dwordx4 v[132:135], %[l16], %[rs], %[t0] offen offset:1024 sc1\n\t" \
    "buffer_load_dwordx4 v[136:139], %[l16], %[rs], %[t0] offen offset:2048 sc1\n\t" \
    "buffer_load_dwordx4 v[140:143], %[l16], %[rs], %[t0] offen offset:3072 sc1\n\t" \
    "s_add_u32 %[kvo], %[kvo], 4096\n\t" \
    "s_and_b32 %[kvo], %[kvo], 0xffff\n\t" \
    "s_add_u32 %[t0], %[kvs], %[kvo]\n\t" \
    "buffer_load_dwordx4 v[144:147], %[l16], %[rs], %[t0] offen offset:0 sc1\n\t" \
    "buffer_load_dwordx4 v[148:151], %[l16], %[rs], %[t0] offen offset:1024 sc1\n\t" \
    "buffer_load_dwordx4 v[152:155], %[l16], %[rs], %[t0] offen offset:2048 sc1\n\t" \
    "buffer_load_dwordx4 v[156:159], %[l16], %[rs], %[t0] offen offset:3072 sc1\n\t" \
    "v_cvt_pk_bf16_f32 v164, v168, v169\n\t" \
    "v_cvt_pk_bf16_f32 v165, v170, v171\n\t" \
    "v_exp_f32 v168, v76\n\t" \
    "v_exp_f32 v169, v77\n\t" \
    "v_exp_f32 v170, v78\n\t" \
    "v_exp_f32 v171, v79\n\t" \
    "v_mfma_f32_32x32x16_bf16 v[32:47], v[104:107], v[160:163], v[32:47]\n\t" \
    "v_cvt_pk_bf16_f32 v166, v168, v169\n\t" \
    "v_cvt_pk_bf16_f32 v167, v170, v171\n\t" \
    "s_waitcnt vmcnt(20)\n\t" \
    "v_exp_f32 v168, v80\n\t" \
    "v_exp_f32 v169, v81\n\t" \
    "v_exp_f32 v170, v82\n\t" \
    "v_exp_f32 v171, v83\n\t" \
    "v_mfma_f32_32x32x16_bf16 v[64:79], v[112:115], a[224:227], 0\n\t" \
    "v_cvt_pk_bf16_f32 v160, v168, v169\n\t" \
    "v_cvt_pk_bf16_f32 v161, v170, v171\n\t" \
    "v_exp_f32 v168, v84\n\t" \
    "v_exp_f32 v169, v85\n\t" \
    "v_exp_f32 v170, v86\n\t" \
    "v_exp_f32 v171, v87\n\t" \
    "v_mfma_f32_32x32x16_bf16 v[64:79], v[116:119], a[228:231], v[64:79]\n\t" \
    "v_cvt_pk_bf16_f32 v162, v168, v169\n\t" \
    "v_cvt_pk_bf16_f32 v163, v170, v171\n\t" \
    "v_exp_f32 v168, v88\n\t" \
    "v_exp_f32 v169, v89\n\t" \
    "v_exp_f32 v170, v90\n\t" \
    "v_exp_f32 v171, v91\n\t" \
    "v_mfma_f32_32x32x16_bf16 v[32:47], v[108:111], v[164:167], v[32:47]\n\t" \
    "v_cvt_pk_bf16_f32 v164, v168, v169\n\t" \
    "v_cvt_pk_bf16_f32 v165, v170, v171\n\t" \
    "v_exp_f32 v168, v92\n\t" \
    "v_exp_f32 v169, v93\n\t" \
    "v_exp_f32 v170, v94\n\t" \
    "v_exp_f32 v171, v95\n\t" \
    "v_mfma_f32_32x32x16_bf16 v[48:63], v[104:107], v[160:163], v[48:63]\n\t" \
    "v_cvt_pk_bf16_f32 v166, v168, v169\n\t" \
    "v_cvt_pk_bf16_f32 v167, v170, v171\n\t" \
    "v_exp_f32 v168, v64\n\t" \
    "v_exp_f32 v169, v65\n\t" \
    "v_exp_f32 v170, v66\n\t" \
    "v_exp_f32 v171, v67\n\t" \
    "v_mfma_f32_32x32x16_bf16 v[80:95], v[112:115], a[232:235], 0\n\t" \
    "v_cvt_pk_bf16_f32 v160, v168, v169\n\t" \
    "v_cvt_pk_bf16_f32 v161, v170, v171\n\t" \
    "v_exp_f32 v168, v68\n\t" \
    "v_exp_f32 v169, v69\n\t" \
    "v_exp_f32 v170, v70\n\t" \
    "v_exp_f32 v171, v71\n\t" \
    "v_mfma_f32_32x32x16_bf16 v[80:95], v[116:119], a[236:239], v[80:95]\n\t" \
    "v_cvt_pk_bf16_f32 v162, v168, v169\n\t" \
    "v_cvt_pk_bf16_f32 v163, v170, v171\n\t" \
    "v_exp_f32 v168, v72\n\t" \
    "v_exp_f32 v169, v73\n\t" \
    "v_exp_f32 v170, v74\n\t" \
    "v_exp_f32 v171, v75\n\t" \
    "v_mfma_f32_32x32x16_bf16 v[48:63], v[108:111], v[164:167], v[48:63]\n\t" \
    "v_cvt_pk_bf16_f32 v164, v168, v169\n\t" \
    "v_cvt_pk_bf16_f32 v165, v170, v171\n\t" \
    "v_exp_f32 v168, v76\n\t" \
    "v_exp_f32 v169, v77\n\t" \
    "v_exp_f32 v170, v78\n\t" \
    "v_exp_f32 v171, v79\n\t" \
    "v_mfma_f32_32x32x16_bf16 v[0:15], v[120:123], v[160:163], v[0:15]\n\t" \
    "v_cvt_pk_bf16_f32 v166, v168, v169\n\t" \
    "v_cvt_pk_bf16_f32 v167, v170, v171\n\t" \
    "v_exp_f32 v168, v80\n\t" \
    "v_exp_f32 v169, v81\n\t" \
    "v_exp_f32 v170, v82\n\t" \
    "v_exp_f32 v171, v83\n\t" \
    "v_mfma_f32_32x32x16_bf16 v[64:79], v[112:115], a[240:243], 0\n\t" \
    "v_cvt_pk_bf16_f32 v160, v168, v169\n\t" \
    "v_cvt_pk_bf16_f32 v161, v170, v171\n\t" \
    "v_exp_f32 v168, v84\n\t" \
    "v_exp_f32 v169, v85\n\t" \
    "v_exp_f32 v170, v86\n\t" \
    "v_exp_f32 v171, v87\n\t" \
    "v_mfma_f32_32x32x16_bf16 v[64:79], v[116:119], a[244:247], v[64:79]\n\t" \
    "v_cvt_pk_bf16_f32 v162, v168, v169\n\t" \
    "v_cvt_pk_bf16_f32 v163, v170, v171\n\t" \
    "v_exp_f32 v168, v88\n\t" \
    "v_exp_f32 v169, v89\n\t" \
    "v_exp_f32 v170, v90\n\t" \
    "v_exp_f32 v171, v91\n\t" \
    "v_mfma_f32_32x32x16_bf16 v[0:15], v[124:127], v[164:167], v[0:15]\n\t" \
    "v_cvt_pk_bf16_f32 v164, v168, v169\n\t" \
    "v_cvt_pk_bf16_f32 v165, v170, v171\n\t" \
    "v_exp_f32 v168, v92\n\t" \
    "v_exp_f32 v169, v93\n\t" \
    "v_exp_f32 v170, v94\n\t" \
    "v_exp_f32 v171, v95\n\t" \
    "v_mfma_f32_32x32x16_bf16 v[16:31], v[120:123], v[160:163], v[16:31]\n\t" \
    "v_cvt_pk_bf16_f32 v166, v168, v169\n\t" \
    "v_cvt_pk_bf16_f32 v167, v170, v171\n\t" \
    "v_exp_f32 v168, v64\n\t" \
    "v_exp_f32 v169, v65\n\t" \
    "v_exp_f32 v170, v66\n\t" \
    "v_exp_f32 v171, v67\n\t" \
    "v_mfma_f32_32x32x16_bf16 v[80:95], v[112:115], a[248:251], 0\n\t" \
    "s_add_u32 %[t0], %[w2s], 32768\n\t" \
    "buffer_load_dwordx4 a[128:131], %[w2a], %[rs], %[t0] offen offset:0 sc1\n\t" \
    "buffer_load_dwordx4 a[132:135], %[w2b], %[rs], %[t0] offen offset:0 sc1\n\t" \
    "buffer_load_dwordx4 a[136:139], %[w2a], %[rs], %[t0] offen offset:2048 sc1\n\t" \
    "buffer_load_dwordx4 a[140:143], %[w2b], %[rs], %[t0] offen offset:2048 sc1\n\t" \
    "s_add_u32 %[t0], %[w2s], 36864\n\t" \
    "buffer_load_dwordx4 a[144:147], %[w2a], %[rs], %[t0] offen offset:0 sc1\n\t" \
    "buffer_load_dwordx4 a[148:151], %[w2b], %[rs], %[t0] offen offset:0 sc1\n\t" \
    "buffer_load_dwordx4 a[152:155], %[w2a], %[rs], %[t0] offen offset:2048 sc1\n\t" \
    "buffer_load_dwordx4 a[156:159], %[w2b], %[rs], %[t0] offen offset:2048 sc1\n\t" \
    "v_cvt_pk_bf16_f32 v160, v168, v169\n\t" \
    "v_cvt_pk_bf16_f32 v161, v170, v171\n\t" \
    "v_exp_f32 v168, v68\n\t" \
    "v_exp_f32 v169, v69\n\t" \
    "v_exp_f32 v170, v70\n\t" \
    "v_exp_f32 v171, v71\n\t" \
    "v_mfma_f32_32x32x16_bf16 v[80:95], v[116:119], a[252:255], v[80:95]\n\t" \
    "v_cvt_pk_bf16_f32 v162, v168, v169\n\t" \
    "v_cvt_pk_bf16_f32 v163, v170, v171\n\t" \
    "v_exp_f32 v168, v72\n\t" \
    "v_exp_f32 v169, v73\n\t" \
    "v_exp_f32 v170, v74\n\t" \
    "v_exp_f32 v171, v75\n\t" \
    "v_mfma_f32_32x32x16_bf16 v[16:31], v[124:127], v[164:167], v[16:31]\n\t" \
    "v_cvt_pk_bf16_f32 v164, v168, v169\n\t" \
    "v_cvt_pk_bf16_f32 v165, v170, v171\n\t" \
    "v_exp_f32 v168, v76\n\t" \
    "v_exp_f32 v169, v77\n\t" \
    "v_exp_f32 v170, v78\n\t" \
    "v_exp_f32 v171, v79\n\t" \
    "v_mfma_f32_32x32x16_bf16 v[32:47], v[120:123], v[160:163], v[32:47]\n\t" \
    "v_cvt_pk_bf16_f32 v166, v168, v169\n\t" \
    "v_cvt_pk_bf16_f32 v167, v170, v171\n\t" \
    "s_waitcnt vmcnt(12)\n\t" \
    "v_exp_f32 v168, v80\n\t" \
    "v_exp_f32 v169, v81\n\t" \
    "v_exp_f32 v170, v82\n\t" \
    "v_exp_f32 v171, v83\n\t" \
    "v_mfma_f32_32x32x16_bf16 v[64:79], v[128:131], a[224:227], 0\n\t" \
    "v_cvt_pk_bf16_f32 v160, v168, v169\n\t" \
    "v_cvt_pk_bf16_f32 v161, v170, v171\n\t" \
    "v_exp_f32 v168, v84\n\t" \
    "v_exp_f32 v169, v85\n\t" \
    "v_exp_f32 v170, v86\n\t" \
    "v_exp_f32 v171, v87\n\t" \
    "v_mfma_f32_32x32x16_bf16 v[64:79], v[132:135], a[228:231], v[64:79]\n\t" \
    "v_cvt_pk_bf16_f32 v162, v168, v169\n\t" \
    "v_cvt_pk_bf16_f32 v163, v170, v171\n\t" \
    "v_exp_f32 v168, v88\n\t" \
    "v_exp_f32 v169, v89\n\t" \
    "v_exp_f32 v170, v90\n\t" \
    "v_exp_f32 v171, v91\n\t" \
    "v_mfma_f32_32x32x16_bf16 v[32:47], v[124:127], v[164:167], v[32:47]\n\t" \
    "v_cvt_pk_bf16_f32 v164, v168, v169\n\t" \
    "v_cvt_pk_bf16_f32 v165, v170, v171\n\t" \
    "v_exp_f32 v168, v92\n\t" \
    "v_exp_f32 v169, v93\n\t" \
    "v_exp_f32 v170, v94\n\t" \
    "v_exp_f32 v171, v95\n\t" \
    "v_mfma_f32_32x32x16_bf16 v[48:63], v[120:123], v[160:163], v[48:63]\n\t" \
    "v_cvt_pk_bf16_f32 v166, v168, v169\n\t" \
    "v_cvt_pk_bf16_f32 v167, v170, v171\n\t" \
    "v_exp_f32 v168, v64\n\t" \
    "v_exp_f32 v169, v65\n\t" \
    "v_exp_f32 v170, v66\n\t" \
    "v_exp_f32 v171, v67\n\t" \
    "v_mfma_f32_32x32x16_bf16 v[80:95], v[128:131], a[232:235], 0\n\t" \
    "s_min_u32 %[t0], %[wq], 49152\n\t" \
    "s_add_u32 %[t1], %[t0], %[w1s]\n\t" \
    "s_add_u32 %[t0], %[t0], 57600\n\t" \
    "s_mov_b32 m0, %[t0]\n\t" \
    "s_nop 0\n\t" \
    "buffer_load_dwordx4 %[l16], %[rs], %[t1] offen offset:0 sc1 lds\n\t" \
    "buffer_load_dwordx4 %[l16], %[rs], %[t1] offen offset:1024 sc1 lds\n\t" \
    "buffer_load_dwordx4 %[l16], %[rs], %[t1] offen offset:2048 sc1 lds\n\t" \
    "buffer_load_dwordx4 %[l16], %[rs], %[t1] offen offset:3072 sc1 lds\n\t" \
    "s_add_u32 %[wq], %[wq], 16384\n\t" \
    "v_cvt_pk_bf16_f32 v160, v168, v169\n\t" \
    "v_cvt_pk_bf16_f32 v161, v170, v171\n\t" \
    "v_exp_f32 v168, v68\n\t" \
    "v_exp_f32 v169, v69\n\t" \
    "v_exp_f32 v170, v70\n\t" \
    "v_exp_f32 v171, v71\n\t" \
    "v_mfma_f32_32x32x16_bf16 v[80:95], v[132:135], a[236:239], v[80:95]\n\t" \
    "v_cvt_pk_bf16_f32 v162, v168, v169\n\t" \
    "v_cvt_pk_bf16_f32 v163, v170, v171\n\t" \
    "v_exp_f32 v168, v72\n\t" \
    "v_exp_f32 v169, v73\n\t" \
    "v_exp_f32 v170, v74\n\t" \
    "v_exp_f32 v171, v75\n\t" \
    "v_mfma_f32_32x32x16_bf16 v[48:63], v[124:127], v[164:167], v[48:63]\n\t" \
    "v_cvt_pk_bf16_f32 v164, v168, v169\n\t" \
    "v_cvt_pk_bf16_f32 v165, v170, v171\n\t" \
    "v_exp_f32 v168, v76\n\t" \
    "v_exp_f32 v169, v77\n\t" \
    "v_exp_f32 v170, v78\n\t" \
    "v_exp_f32 v171, v79\n\t" \
    "v_mfma_f32_32x32x16_bf16 v[0:15], v[136:139], v[160:163], v[0:15]\n\t" \
    "v_cvt_pk_bf16_f32 v166, v168, v169\n\t" \
    "v_cvt_pk_bf16_f32 v167, v170, v171\n\t" \
    "v_exp_f32 v168, v80\n\t" \
    "v_exp_f32 v169, v81\n\t" \
    "v_exp_f32 v170, v82\n\t" \
    "v_exp_f32 v171, v83\n\t" \
    "v_mfma_f32_32x32x16_bf16 v[64:79], v[128:131], a[240:243], 0\n\t" \
    "v_cvt_pk_bf16_f32 v160, v168, v169\n\t" \
    "v_cvt_pk_bf16_f32 v161, v170, v171\n\t" \
    "v_exp_f32 v168, v84\n\t" \
    "v_exp_f32 v169, v85\n\t" \
    "v_exp_f32 v170, v86\n\t" \
    "v_exp_f32 v171, v87\n\t" \
    "v_mfma_f32_32x32x16_bf16 v[64:79], v[132:135], a[244:247], v[64:79]\n\t" \
    "v_cvt_pk_bf16_f32 v162, v168, v169\n\t" \
    "v_cvt_pk_bf16_f32 v163, v170, v171\n\t" \
    "v_exp_f32 v168, v88\n\t" \
    "v_exp_f32 v169, v89\n\t" \
    "v_exp_f32 v170, v90\n\t" \
    "v_exp_f32 v171, v91\n\t" \
    "v_mfma_f32_32x32x16_bf16 v[0:15], v[140:143], v[164:167], v[0:15]\n\t" \
    "v_cvt_pk_bf16_f32 v164, v168, v169\n\t" \
    "v_cvt_pk_bf16_f32 v165, v170, v171\n\t" \
    "v_exp_f32 v168, v92\n\t" \
    "v_exp_f32 v169, v93\n\t" \
    "v_exp_f32 v170, v94\n\t" \
    "v_exp_f32 v171, v95\n\t" \
    "v_mfma_f32_32x32x16_bf16 v[16:31], v[136:139], v[160:163], v[16:31]\n\t" \
    "v_cvt_pk_bf16_f32 v166, v168, v169\n\t" \
    "v_cvt_pk_bf16_f32 v167, v170, v171\n\t" \
    "v_exp_f32 v168, v64\n\t" \
    "v_exp_f32 v169, v65\n\t" \
    "v_exp_f32 v170, v66\n\t" \
    "v_exp_f32 v171, v67\n\t" \
    "v_mfma_f32_32x32x16_bf16 v[80:95], v[128:131], a[248:251], 0\n\t" \
    "v_cvt_pk_bf16_f32 v160, v168, v169\n\t" \
    "v_cvt_pk_bf16_f32 v161, v170, v171\n\t" \
    "v_exp_f32 v168, v68\n\t" \
    "v_exp_f32 v169, v69\n\t" \
    "v_exp_f32 v170, v70\n\t" \
    "v_exp_f32 v171, v71\n\t" \
    "v_mfma_f32_32x32x16_bf16 v[80:95], v[132:135], a[252:255], v[80:95]\n\t" \
    "v_cvt_pk_bf16_f32 v162, v168, v169\n\t" \
    "v_cvt_pk_bf16_f32 v163, v170, v171\n\t" \
    "v_exp_f32 v168, v72\n\t" \
    "v_exp_f32 v169, v73\n\t" \
    "v_exp_f32 v170, v74\n\t" \
    "v_exp_f32 v171, v75\n\t" \
    "v_mfma_f32_32x32x16_bf16 v[16:31], v[140:143], v[164:167], v[16:31]\n\t" \
    "s_add_u32 %[kvo], %[kvo], 4096\n\t" \
    "s_and_b32 %[kvo], %[kvo], 0xffff\n\t" \
    "s_add_u32 %[t0], %[kvs], %[kvo]\n\t" \
    "buffer_load_dwordx4 v[96:99], %[l16], %[rs], %[t0] offen offset:0 sc1\n\t" \
    "buffer_load_dwordx4 v[100:103], %[l16], %[rs], %[t0] offen offset:1024 sc1\n\t" \
    "buffer_load_dwordx4 v[104:107], %[l16], %[rs], %[t0] offen offset:2048 sc1\n\t" \
    "buffer_load_dwordx4 v[108:111], %[l16], %[rs], %[t0] offen offset:3072 sc1\n\t" \
    "s_add_u32 %[kvo], %[kvo], 4096\n\t" \
    "s_and_b32 %[kvo], %[kvo], 0xffff\n\t" \
    "s_add_u32 %[t0], %[kvs], %[kvo]\n\t" \
    "buffer_load_dwordx4 v[112:115], %[l16], %[rs], %[t0] offen offset:0 sc1\n\t" \
    "buffer_load_dwordx4 v[116:119], %[l16], %[rs], %[t0] offen offset:1024 sc1\n\t" \
    "buffer_load_dwordx4 v[120:123], %[l16], %[rs], %[t0] offen offset:2048 sc1\n\t" \
    "buffer_load_dwordx4 v[124:127], %[l16], %[rs], %[t0] offen offset:3072 sc1\n\t" \
    "v_cvt_pk_bf16_f32 v164, v168, v169\n\t" \
    "v_cvt_pk_bf16_f32 v165, v170, v171\n\t" \
    "v_exp_f32 v168, v76\n\t" \
    "v_exp_f32 v169, v77\n\t" \
    "v_exp_f32 v170, v78\n\t" \
    "v_exp_f32 v171, v79\n\t" \
    "v_mfma_f32_32x32x16_bf16 v[32:47], v[136:139], v[160:163], v[32:47]\n\t" \
    "v_cvt_pk_bf16_f32 v166, v168, v169\n\t" \
    "v_cvt_pk_bf16_f32 v167, v170, v171\n\t" \
    "s_waitcnt vmcnt(20)\n\t" \
    "v_exp_f32 v168, v80\n\t" \
    "v_exp_f32 v169, v81\n\t" \
    "v_exp_f32 v170, v82\n\t" \
    "v_exp_f32 v171, v83\n\t" \
    "v_mfma_f32_32x32x16_bf16 v[64:79], v[144:147], a[224:227], 0\n\t" \
    "v_cvt_pk_bf16_f32 v160, v168, v169\n\t" \
    "v_cvt_pk_bf16_f32 v161, v170, v171\n\t" \
    "v_exp_f32 v168, v84\n\t" \
    "v_exp_f32 v169, v85\n\t" \
    "v_exp_f32 v170, v86\n\t" \
    "v_exp_f32 v171, v87\n\t" \
    "v_mfma_f32_32x32x16_bf16 v[64:79], v[148:151], a[228:231], v[64:79]\n\t" \
    "v_cvt_pk_bf16_f32 v162, v168, v169\n\t" \
    "v_cvt_pk_bf16_f32 v163, v170, v171\n\t" \
    "v_exp_f32 v168, v88\n\t" \
    "v_exp_f32 v169, v89\n\t" \
    "v_exp_f32 v170, v90\n\t" \
    "v_exp_f32 v171, v91\n\t" \
    "v_mfma_f32_32x32x16_bf16 v[32:47], v[140:143], v[164:167], v[32:47]\n\t" \
    "v_cvt_pk_bf16_f32 v164, v168, v169\n\t" \
    "v_cvt_pk_bf16_f32 v165, v170, v171\n\t" \
    "v_exp_f32 v168, v92\n\t" \
    "v_exp_f32 v169, v93\n\t" \
    "v_exp_f32 v170, v94\n\t" \
    "v_exp_f32 v171, v95\n\t" \
    "v_mfma_f32_32x32x16_bf16 v[48:63], v[136:139], v[160:163], v[48:63]\n\t" \
    "v_cvt_pk_bf16_f32 v166, v168, v169\n\t" \
    "v_cvt_pk_bf16_f32 v167, v170, v171\n\t" \
    "v_exp_f32 v168, v64\n\t" \
    "v_exp_f32 v169, v65\n\t" \
    "v_exp_f32 v170, v66\n\t" \
    "v_exp_f32 v171, v67\n\t" \
    "v_mfma_f32_32x32x16_bf16 v[80:95], v[144:147], a[232:235], 0\n\t" \
    "s_min_u32 %[t0], %[wqo], 53248\n\t" \
    "s_add_u32 %[t1], %[t0], %[wos]\n\t" \
    "s_mov_b32 m0, %[t0]\n\t" \
    "s_nop 0\n\t" \
    "buffer_load_dwordx4 %[l16], %[rs], %[t1] offen offset:0 sc1 lds\n\t" \
    "buffer_load_dwordx4 %[l16], %[rs], %[t1] offen offset:1024 sc1 lds\n\t" \
    "buffer_load_dwordx4 %[l16], %[rs], %[t1] offen offset:2048 sc1 lds\n\t" \
    "buffer_load_dwordx4 %[l16], %[rs], %[t1] offen offset:3072 sc1 lds\n\t" \
    "s_add_u32 %[wqo], %[wqo], 16384\n\t" \
    "v_cvt_pk_bf16_f32 v160, v168, v169\n\t" \
    "v_cvt_pk_bf16_f32 v161, v170, v171\n\t" \
    "v_exp_f32 v168, v68\n\t" \
    "v_exp_f32 v169, v69\n\t" \
    "v_exp_f32 v170, v70\n\t" \
    "v_exp_f32 v171, v71\n\t" \
    "v_mfma_f32_32x32x16_bf16 v[80:95], v[148:151], a[236:239], v[80:95]\n\t" \
    "v_cvt_pk_bf16_f32 v162, v168, v169\n\t" \
    "v_cvt_pk_bf16_f32 v163, v170, v171\n\t" \
    "v_exp_f32 v168, v72\n\t" \
    "v_exp_f32 v169, v73\n\t" \
    "v_exp_f32 v170, v74\n\t" \
    "v_exp_f32 v171, v75\n\t" \
    "v_mfma_f32_32x32x16_bf16 v[48:63], v[140:143], v[164:167], v[48:63]\n\t" \
    "v_cvt_pk_bf16_f32 v164, v168, v169\n\t" \
    "v_cvt_pk_bf16_f32 v165, v170, v171\n\t" \
    "v_exp_f32 v168, v76\n\t" \
    "v_exp_f32 v169, v77\n\t" \
    "v_exp_f32 v170, v78\n\t" \
    "v_exp_f32 v171, v79\n\t" \
    "v_mfma_f32_32x32x16_bf16 v[0:15], v[152:155], v[160:163], v[0:15]\n\t" \
    "v_cvt_pk_bf16_f32 v166, v168, v169\n\t" \
    "v_cvt_pk_bf16_f32 v167, v170, v171\n\t" \
    "v_exp_f32 v168, v80\n\t" \
    "v_exp_f32 v169, v81\n\t" \
    "v_exp_f32 v170, v82\n\t" \
    "v_exp_f32 v171, v83\n\t" \
    "v_mfma_f32_32x32x16_bf16 v[64:79], v[144:147], a[240:243], 0\n\t" \
    "v_cvt_pk_bf16_f32 v160, v168, v169\n\t" \
    "v_cvt_pk_bf16_f32 v161, v170, v171\n\t" \
    "v_exp_f32 v168, v84\n\t" \
    "v_exp_f32 v169, v85\n\t" \
    "v_exp_f32 v170, v86\n\t" \
    "v_exp_f32 v171, v87\n\t" \
    "v_mfma_f32_32x32x16_bf16 v[64:79], v[148:151], a[244:247], v[64:79]\n\t" \
    "v_cvt_pk_bf16_f32 v162, v168, v169\n\t" \
    "v_cvt_pk_bf16_f32 v163, v170, v171\n\t" \
    "v_exp_f32 v168, v88\n\t" \
    "v_exp_f32 v169, v89\n\t" \
    "v_exp_f32 v170, v90\n\t" \
    "v_exp_f32 v171, v91\n\t" \
    "v_mfma_f32_32x32x16_bf16 v[0:15], v[156:159], v[164:167], v[0:15]\n\t" \
    "v_cvt_pk_bf16_f32 v164, v168, v169\n\t" \
    "v_cvt_pk_bf16_f32 v165, v170, v171\n\t" \
    "v_exp_f32 v168, v92\n\t" \
    "v_exp_f32 v169, v93\n\t" \
    "v_exp_f32 v170, v94\n\t" \
    "v_exp_f32 v171, v95\n\t" \
    "v_mfma_f32_32x32x16_bf16 v[16:31], v[152:155], v[160:163], v[16:31]\n\t" \
    "v_cvt_pk_bf16_f32 v166, v168, v169\n\t" \
    "v_cvt_pk_bf16_f32 v167, v170, v171\n\t" \
    "v_exp_f32 v168, v64\n\t" \
    "v_exp_f32 v169, v65\n\t" \
    "v_exp_f32 v170, v66\n\t" \
    "v_exp_f32 v171, v67\n\t" \
    "v_mfma_f32_32x32x16_bf16 v[80:95], v[144:147], a[248:251], 0\n\t" \
    "s_add_u32 %[t0], %[w2s], 40960\n\t" \
    "buffer_load_dwordx4 a[160:163], %[w2a], %[rs], %[t0] offen offset:0 sc1\n\t" \
    "buffer_load_dwordx4 a[164:167], %[w2b], %[rs], %[t0] offen offset:0 sc1\n\t" \
    "buffer_load_dwordx4 a[168:171], %[w2a], %[rs], %[t0] offen offset:2048 sc1\n\t" \
    "buffer_load_dwordx4 a[172:175], %[w2b], %[rs], %[t0] offen offset:2048 sc1\n\t" \
    "s_add_u32 %[t0], %[w2s], 45056\n\t" \
    "buffer_load_dwordx4 a[176:179], %[w2a], %[rs], %[t0] offen offset:0 sc1\n\t" \
    "buffer_load_dwordx4 a[180:183], %[w2b], %[rs], %[t0] offen offset:0 sc1\n\t" \
    "buffer_load_dwordx4 a[184:187], %[w2a], %[rs], %[t0] offen offset:2048 sc1\n\t" \
    "buffer_load_dwordx4 a[188:191], %[w2b], %[rs], %[t0] offen offset:2048 sc1\n\t" \
    "v_cvt_pk_bf16_f32 v160, v168, v169\n\t" \
    "v_cvt_pk_bf16_f32 v161, v170, v171\n\t" \
    "v_exp_f32 v168, v68\n\t" \
    "v_exp_f32 v169, v69\n\t" \
    "v_exp_f32 v170, v70\n\t" \
    "v_exp_f32 v171, v71\n\t" \
    "v_mfma_f32_32x32x16_bf16 v[80:95], v[148:151], a[252:255], v[80:95]\n\t" \
    "v_cvt_pk_bf16_f32 v162, v168, v169\n\t" \
    "v_cvt_pk_bf16_f32 v163, v170, v171\n\t" \
    "v_exp_f32 v168, v72\n\t" \
    "v_exp_f32 v169, v73\n\t" \
    "v_exp_f32 v170, v74\n\t" \
    "v_exp_f32 v171, v75\n\t" \
    "v_mfma_f32_32x32x16_bf16 v[16:31], v[156:159], v[164:167], v[16:31]\n\t" \
    "v_cvt_pk_bf16_f32 v164, v168, v169\n\t" \
    "v_cvt_pk_bf16_f32 v165, v170, v171\n\t" \
    "v_exp_f32 v168, v76\n\t" \
    "v_exp_f32 v169, v77\n\t" \
    "v_exp_f32 v170, v78\n\t" \
    "v_exp_f32 v171, v79\n\t" \
    "v_mfma_f32_32x32x16_bf16 v[32:47], v[152:155], v[160:163], v[32:47]\n\t" \
    "v_cvt_pk_bf16_f32 v166, v168, v169\n\t" \
    "v_cvt_pk_bf16_f32 v167, v170, v171\n\t" \
    "s_waitcnt vmcnt(16)\n\t" \
    "v_exp_f32 v168, v80\n\t" \
    "v_exp_f32 v169, v81\n\t" \
    "v_exp_f32 v170, v82\n\t" \
    "v_exp_f32 v171, v83\n\t" \
    "v_mfma_f32_32x32x16_bf16 v[64:79], v[96:99], a[224:227], 0\n\t" \
    "v_cvt_pk_bf16_f32 v160, v168, v169\n\t" \
    "v_cvt_pk_bf16_f32 v161, v170, v171\n\t" \
    "v_exp_f32 v168, v84\n\t" \
    "v_exp_f32 v169, v85\n\t" \
    "v_exp_f32 v170, v86\n\t" \
    "v_exp_f32 v171, v87\n\t" \
    "v_mfma_f32_32x32x16_bf16 v[64:79], v[100:103], a[228:231], v[64:79]\n\t" \
    "v_cvt_pk_bf16_f32 v162, v168, v169\n\t" \
    "v_cvt_pk_bf16_f32 v163, v170, v171\n\t" \
    "v_exp_f32 v168, v88\n\t" \
    "v_exp_f32 v169, v89\n\t" \
    "v_exp_f32 v170, v90\n\t" \
    "v_exp_f32 v171, v91\n\t" \
    "v_mfma_f32_32x32x16_bf16 v[32:47], v[156:159], v[164:167], v[32:47]\n\t" \
    "v_cvt_pk_bf16_f32 v164, v168, v169\n\t" \
    "v_cvt_pk_bf16_f32 v165, v170, v171\n\t" \
    "v_exp_f32 v168, v92\n\t" \
    "v_exp_f32 v169, v93\n\t" \
    "v_exp_f32 v170, v94\n\t" \
    "v_exp_f32 v171, v95\n\t" \
    "v_mfma_f32_32x32x16_bf16 v[48:63], v[152:155], v[160:163], v[48:63]\n\t" \
    "v_cvt_pk_bf16_f32 v166, v168, v169\n\t" \
    "v_cvt_pk_bf16_f32 v167, v170, v171\n\t" \
    "v_exp_f32 v168, v64\n\t" \
    "v_exp_f32 v169, v65\n\t" \
    "v_exp_f32 v170, v66\n\t" \
    "v_exp_f32 v171, v67\n\t" \
    "v_mfma_f32_32x32x16_bf16 v[80:95], v[96:99], a[232:235], 0\n\t" \
    "v_cvt_pk_bf16_f32 v160, v168, v169\n\t" \
    "v_cvt_pk_bf16_f32 v161, v170, v171\n\t" \
    "v_exp_f32 v168, v68\n\t" \
    "v_exp_f32 v169, v69\n\t" \
    "v_exp_f32 v170, v70\n\t" \
    "v_exp_f32 v171, v71\n\t" \
    "v_mfma_f32_32x32x16_bf16 v[80:95], v[100:103], a[236:239], v[80:95]\n\t" \
    "v_cvt_pk_bf16_f32 v162, v168, v169\n\t" \
    "v_cvt_pk_bf16_f32 v163, v170, v171\n\t" \
    "v_exp_f32 v168, v72\n\t" \
    "v_exp_f32 v169, v73\n\t" \
    "v_exp_f32 v170, v74\n\t" \
    "v_exp_f32 v171, v75\n\t" \
    "v_mfma_f32_32x32x16_bf16 v[48:63], v[156:159], v[164:167], v[48:63]\n\t" \
    "v_cvt_pk_bf16_f32 v164, v168, v169\n\t" \
    "v_cvt_pk_bf16_f32 v165, v170, v171\n\t" \
    "v_exp_f32 v168, v76\n\t" \
    "v_exp_f32 v169, v77\n\t" \
    "v_exp_f32 v170, v78\n\t" \
    "v_exp_f32 v171, v79\n\t" \
    "v_mfma_f32_32x32x16_bf16 v[0:15], v[104:107], v[160:163], v[0:15]\n\t" \
    "v_cvt_pk_bf16_f32 v166, v168, v169\n\t" \
    "v_cvt_pk_bf16_f32 v167, v170, v171\n\t" \
    "v_exp_f32 v168, v80\n\t" \
    "v_exp_f32 v169, v81\n\t" \
    "v_exp_f32 v170, v82\n\t" \
    "v_exp_f32 v171, v83\n\t" \
    "v_mfma_f32_32x32x16_bf16 v[64:79], v[96:99], a[240:243], 0\n\t" \
    "v_cvt_pk_bf16_f32 v160, v168, v169\n\t" \
    "v_cvt_pk_bf16_f32 v161, v170, v171\n\t" \
    "v_exp_f32 v168, v84\n\t" \
    "v_exp_f32 v169, v85\n\t" \
    "v_exp_f32 v170, v86\n\t" \
    "v_exp_f32 v171, v87\n\t" \
    "v_mfma_f32_32x32x16_bf16 v[64:79], v[100:103], a[244:247], v[64:79]\n\t" \
    "v_cvt_pk_bf16_f32 v162, v168, v169\n\t" \
    "v_cvt_pk_bf16_f32 v163, v170, v171\n\t" \
    "v_exp_f32 v168, v88\n\t" \
    "v_exp_f32 v169, v89\n\t" \
    "v_exp_f32 v170, v90\n\t" \
    "v_exp_f32 v171, v91\n\t" \
    "v_mfma_f32_32x32x16_bf16 v[0:15], v[108:111], v[164:167], v[0:15]\n\t" \
    "v_cvt_pk_bf16_f32 v164, v168, v169\n\t" \
    "v_cvt_pk_bf16_f32 v165, v170, v171\n\t" \
    "v_exp_f32 v168, v92\n\t" \
    "v_exp_f32 v169, v93\n\t" \
    "v_exp_f32 v170, v94\n\t" \
    "v_exp_f32 v171, v95\n\t" \
    "v_mfma_f32_32x32x16_bf16 v[16:31], v[104:107], v[160:163], v[16:31]\n\t" \
    "v_cvt_pk_bf16_f32 v166, v168, v169\n\t" \
    "v_cvt_pk_bf16_f32 v167, v170, v171\n\t" \
    "v_exp_f32 v168, v64\n\t" \
    "v_exp_f32 v169, v65\n\t" \
    "v_exp_f32 v170, v66\n\t" \
    "v_exp_f32 v171, v67\n\t" \
    "v_mfma_f32_32x32x16_bf16 v[80:95], v[96:99], a[248:251], 0\n\t" \
    "v_cvt_pk_bf16_f32 v160, v168, v169\n\t" \
    "v_cvt_pk_bf16_f32 v161, v170, v171\n\t" \
    "v_exp_f32 v168, v68\n\t" \
    "v_exp_f32 v169, v69\n\t" \
    "v_exp_f32 v170, v70\n\t" \
    "v_exp_f32 v171, v71\n\t" \
    "v_mfma_f32_32x32x16_bf16 v[80:95], v[100:103], a[252:255], v[80:95]\n\t" \
    "v_cvt_pk_bf16_f32 v162, v168, v169\n\t" \
    "v_cvt_pk_bf16_f32 v163, v170, v171\n\t" \
    "v_exp_f32 v168, v72\n\t" \
    "v_exp_f32 v169, v73\n\t" \
    "v_exp_f32 v170, v74\n\t" \
    "v_exp_f32 v171, v75\n\t" \
    "v_mfma_f32_32x32x16_bf16 v[16:31], v[108:111], v[164:167], v[16:31]\n\t" \
    "s_add_u32 %[kvo], %[kvo], 4096\n\t" \
    "s_and_b32 %[kvo], %[kvo], 0xffff\n\t" \
    "s_add_u32 %[t0], %[kvs], %[kvo]\n\t" \
    "buffer_load_dwordx4 v[128:131], %[l16], %[rs], %[t0] offen offset:0 sc1\n\t" \
    "buffer_load_dwordx4 v[132:135], %[l16], %[rs], %[t0] offen offset:1024 sc1\n\t" \
    "buffer_load_dwordx4 v[136:139], %[l16], %[rs], %[t0] offen offset:2048 sc1\n\t" \
    "buffer_load_dwordx4 v[140:143], %[l16], %[rs], %[t0] offen offset:3072 sc1\n\t" \
    "s_add_u32 %[kvo], %[kvo], 4096\n\t" \
    "s_and_b32 %[kvo], %[kvo], 0xffff\n\t" \
    "s_add_u32 %[t0], %[kvs], %[kvo]\n\t" \
    "buffer_load_dwordx4 v[144:147], %[l16], %[rs], %[t0] offen offset:0 sc1\n\t" \
    "buffer_load_dwordx4 v[148:151], %[l16], %[rs], %[t0] offen offset:1024 sc1\n\t" \
    "buffer_load_dwordx4 v[152:155], %[l16], %[rs], %[t0] offen offset:2048 sc1\n\t" \
    "buffer_load_dwordx4 v[156:159], %[l16], %[rs], %[t0] offen offset:3072 sc1\n\t" \
    "v_cvt_pk_bf16_f32 v164, v168, v169\n\t" \
    "v_cvt_pk_bf16_f32 v165, v170, v171\n\t" \
    "v_exp_f32 v168, v76\n\t" \
    "v_exp_f32 v169, v77\n\t" \
    "v_exp_f32 v170, v78\n\t" \
    "v_exp_f32 v171, v79\n\t" \
    "v_mfma_f32_32x32x16_bf16 v[32:47], v[104:107], v[160:163], v[32:47]\n\t" \
    "v_cvt_pk_bf16_f32 v166, v168, v169\n\t" \
    "v_cvt_pk_bf16_f32 v167, v170, v171\n\t" \
    "s_waitcnt vmcnt(20)\n\t" \
    "v_exp_f32 v168, v80\n\t" \
    "v_exp_f32 v169, v81\n\t" \
    "v_exp_f32 v170, v82\n\t" \
    "v_exp_f32 v171, v83\n\t" \
    "v_mfma_f32_32x32x16_bf16 v[64:79], v[112:115], a[224:227], 0\n\t" \
    "v_cvt_pk_bf16_f32 v160, v168, v169\n\t" \
    "v_cvt_pk_bf16_f32 v161, v170, v171\n\t" \
    "v_exp_f32 v168, v84\n\t" \
    "v_exp_f32 v169, v85\n\t" \
    "v_exp_f32 v170, v86\n\t" \
    "v_exp_f32 v171, v87\n\t" \
    "v_mfma_f32_32x32x16_bf16 v[64:79], v[116:119], a[228:231], v[64:79]\n\t" \
    "v_cvt_pk_bf16_f32 v162, v168, v169\n\t" \
    "v_cvt_pk_bf16_f32 v163, v170, v171\n\t" \
    "v_exp_f32 v168, v88\n\t" \
    "v_exp_f32 v169, v89\n\t" \
    "v_exp_f32 v170, v90\n\t" \
    "v_exp_f32 v171, v91\n\t" \
    "v_mfma_f32_32x32x16_bf16 v[32:47], v[108:111], v[164:167], v[32:47]\n\t" \
    "v_cvt_pk_bf16_f32 v164, v168, v169\n\t" \
    "v_cvt_pk_bf16_f32 v165, v170, v171\n\t" \
    "v_exp_f32 v168, v92\n\t" \
    "v_exp_f32 v169, v93\n\t" \
    "v_exp_f32 v170, v94\n\t" \
    "v_exp_f32 v171, v95\n\t" \
    "v_mfma_f32_32x32x16_bf16 v[48:63], v[104:107], v[160:163], v[48:63]\n\t" \
    "v_cvt_pk_bf16_f32 v166, v168, v169\n\t" \
    "v_cvt_pk_bf16_f32 v167, v170, v171\n\t" \
    "v_exp_f32 v168, v64\n\t" \
    "v_exp_f32 v169, v65\n\t" \
    "v_exp_f32 v170, v66\n\t" \
    "v_exp_f32 v171, v67\n\t" \
    "v_mfma_f32_32x32x16_bf16 v[80:95], v[112:115], a[232:235], 0\n\t" \
    "v_cvt_pk_bf16_f32 v160, v168, v169\n\t" \
    "v_cvt_pk_bf16_f32 v161, v170, v171\n\t" \
    "v_exp_f32 v168, v68\n\t" \
    "v_exp_f32 v169, v69\n\t" \
    "v_exp_f32 v170, v70\n\t" \
    "v_exp_f32 v171, v71\n\t" \
    "v_mfma_f32_32x32x16_bf16 v[80:95], v[116:119], a[236:239], v[80:95]\n\t" \
    "v_cvt_pk_bf16_f32 v162, v168, v169\n\t" \
    "v_cvt_pk_bf16_f32 v163, v170, v171\n\t" \
    "v_exp_f32 v168, v72\n\t" \
    "v_exp_f32 v169, v73\n\t" \
    "v_exp_f32 v170, v74\n\t" \
    "v_exp_f32 v171, v75\n\t" \
    "v_mfma_f32_32x32x16_bf16 v[48:63], v[108:111], v[164:167], v[48:63]\n\t" \
    "v_cvt_pk_bf16_f32 v164, v168, v169\n\t" \
    "v_cvt_pk_bf16_f32 v165, v170, v171\n\t" \
    "v_exp_f32 v168, v76\n\t" \
    "v_exp_f32 v169, v77\n\t" \
    "v_exp_f32 v170, v78\n\t" \
    "v_exp_f32 v171, v79\n\t" \
    "v_mfma_f32_32x32x16_bf16 v[0:15], v[120:123], v[160:163], v[0:15]\n\t" \
    "v_cvt_pk_bf16_f32 v166, v168, v169\n\t" \
    "v_cvt_pk_bf16_f32 v167, v170, v171\n\t" \
    "v_exp_f32 v168, v80\n\t" \
    "v_exp_f32 v169, v81\n\t" \
    "v_exp_f32 v170, v82\n\t" \
    "v_exp_f32 v171, v83\n\t" \
    "v_mfma_f32_32x32x16_bf16 v[64:79], v[112:115], a[240:243], 0\n\t" \
    "v_cvt_pk_bf16_f32 v160, v168, v169\n\t" \
    "v_cvt_pk_bf16_f32 v161, v170, v171\n\t" \
    "v_exp_f32 v168, v84\n\t" \
    "v_exp_f32 v169, v85\n\t" \
    "v_exp_f32 v170, v86\n\t" \
    "v_exp_f32 v171, v87\n\t" \
    "v_mfma_f32_32x32x16_bf16 v[64:79], v[116:119], a[244:247], v[64:79]\n\t" \
    "v_cvt_pk_bf16_f32 v162, v168, v169\n\t" \
    "v_cvt_pk_bf16_f32 v163, v170, v171\n\t" \
    "v_exp_f32 v168, v88\n\t" \
    "v_exp_f32 v169, v89\n\t" \
    "v_exp_f32 v170, v90\n\t" \
    "v_exp_f32 v171, v91\n\t" \
    "v_mfma_f32_32x32x16_bf16 v[0:15], v[124:127], v[164:167], v[0:15]\n\t" \
    "v_cvt_pk_bf16_f32 v164, v168, v169\n\t" \
    "v_cvt_pk_bf16_f32 v165, v170, v171\n\t" \
    "v_exp_f32 v168, v92\n\t" \
    "v_exp_f32 v169, v93\n\t" \
    "v_exp_f32 v170, v94\n\t" \
    "v_exp_f32 v171, v95\n\t" \
    "v_mfma_f32_32x32x16_bf16 v[16:31], v[120:123], v[160:163], v[16:31]\n\t" \
    "v_cvt_pk_bf16_f32 v166, v168, v169\n\t" \
    "v_cvt_pk_bf16_f32 v167, v170, v171\n\t" \
    "v_exp_f32 v168, v64\n\t" \
    "v_exp_f32 v169, v65\n\t" \
    "v_exp_f32 v170, v66\n\t" \
    "v_exp_f32 v171, v67\n\t" \
    "v_mfma_f32_32x32x16_bf16 v[80:95], v[112:115], a[248:251], 0\n\t" \
    "s_add_u32 %[t0], %[w2s], 49152\n\t" \
    "buffer_load_dwordx4 a[192:195], %[w2a], %[rs], %[t0] offen offset:0 sc1\n\t" \
    "buffer_load_dwordx4 a[196:199], %[w2b], %[rs], %[t0] offen offset:0 sc1\n\t" \
    "buffer_load_dwordx4 a[200:203], %[w2a], %[rs], %[t0] offen offset:2048 sc1\n\t" \
    "buffer_load_dwordx4 a[204:207], %[w2b], %[rs], %[t0] offen offset:2048 sc1\n\t" \
    "s_add_u32 %[t0], %[w2s], 53248\n\t" \
    "buffer_load_dwordx4 a[208:211], %[w2a], %[rs], %[t0] offen offset:0 sc1\n\t" \
    "buffer_load_dwordx4 a[212:215], %[w2b], %[rs], %[t0] offen offset:0 sc1\n\t" \
    "buffer_load_dwordx4 a[216:219], %[w2a], %[rs], %[t0] offen offset:2048 sc1\n\t" \
    "buffer_load_dwordx4 a[220:223], %[w2b], %[rs], %[t0] offen offset:2048 sc1\n\t" \
    "v_cvt_pk_bf16_f32 v160, v168, v169\n\t" \
    "v_cvt_pk_bf16_f32 v161, v170, v171\n\t" \
    "v_exp_f32 v168, v68\n\t" \
    "v_exp_f32 v169, v69\n\t" \
    "v_exp_f32 v170, v70\n\t" \
    "v_exp_f32 v171, v71\n\t" \
    "v_mfma_f32_32x32x16_bf16 v[80:95], v[116:119], a[252:255], v[80:95]\n\t" \
    "v_cvt_pk_bf16_f32 v162, v168, v169\n\t" \
    "v_cvt_pk_bf16_f32 v163, v170, v171\n\t" \
    "v_exp_f32 v168, v72\n\t" \
    "v_exp_f32 v169, v73\n\t" \
    "v_exp_f32 v170, v74\n\t" \
    "v_exp_f32 v171, v75\n\t" \
    "v_mfma_f32_32x32x16_bf16 v[16:31], v[124:127], v[164:167], v[16:31]\n\t" \
    "v_cvt_pk_bf16_f32 v164, v168, v169\n\t" \
    "v_cvt_pk_bf16_f32 v165, v170, v171\n\t" \
    "v_exp_f32 v168, v76\n\t" \
    "v_exp_f32 v169, v77\n\t" \
    "v_exp_f32 v170, v78\n\t" \
    "v_exp_f32 v171, v79\n\t" \
    "v_mfma_f32_32x32x16_bf16 v[32:47], v[120:123], v[160:163], v[32:47]\n\t" \
    "v_cvt_pk_bf16_f32 v166, v168, v169\n\t" \
    "v_cvt_pk_bf16_f32 v167, v170, v171\n\t" \
    "s_waitcnt vmcnt(12)\n\t" \
    "v_exp_f32 v168, v80\n\t" \
    "v_exp_f32 v169, v81\n\t" \
    "v_exp_f32 v170, v82\n\t" \
    "v_exp_f32 v171, v83\n\t" \
    "v_mfma_f32_32x32x16_bf16 v[64:79], v[128:131], a[224:227], 0\n\t" \
    "v_cvt_pk_bf16_f32 v160, v168, v169\n\t" \
    "v_cvt_pk_bf16_f32 v161, v170, v171\n\t" \
    "v_exp_f32 v168, v84\n\t" \
    "v_exp_f32 v169, v85\n\t" \
    "v_exp_f32 v170, v86\n\t" \
    "v_exp_f32 v171, v87\n\t" \
    "v_mfma_f32_32x32x16_bf16 v[64:79], v[132:135], a[228:231], v[64:79]\n\t" \
    "v_cvt_pk_bf16_f32 v162, v168, v169\n\t" \
    "v_cvt_pk_bf16_f32 v163, v170, v171\n\t" \
    "v_exp_f32 v168, v88\n\t" \
    "v_exp_f32 v169, v89\n\t" \
    "v_exp_f32 v170, v90\n\t" \
    "v_exp_f32 v171, v91\n\t" \
    "v_mfma_f32_32x32x16_bf16 v[32:47], v[124:127], v[164:167], v[32:47]\n\t" \
    "v_cvt_pk_bf16_f32 v164, v168, v169\n\t" \
    "v_cvt_pk_bf16_f32 v165, v170, v171\n\t" \
    "v_exp_f32 v168, v92\n\t" \
    "v_exp_f32 v169, v93\n\t" \
    "v_exp_f32 v170, v94\n\t" \
    "v_exp_f32 v171, v95\n\t" \
    "v_mfma_f32_32x32x16_bf16 v[48:63], v[120:123], v[160:163], v[48:63]\n\t" \
    "v_cvt_pk_bf16_f32 v166, v168, v169\n\t" \
    "v_cvt_pk_bf16_f32 v167, v170, v171\n\t" \
    "v_exp_f32 v168, v64\n\t" \
    "v_exp_f32 v169, v65\n\t" \
    "v_exp_f32 v170, v66\n\t" \
    "v_exp_f32 v171, v67\n\t" \
    "v_mfma_f32_32x32x16_bf16 v[80:95], v[128:131], a[232:235], 0\n\t" \
    "s_min_u32 %[t0], %[wq], 49152\n\t" \
    "s_add_u32 %[t1], %[t0], %[w1s]\n\t" \
    "s_add_u32 %[t0], %[t0], 57600\n\t" \
    "s_mov_b32 m0, %[t0]\n\t" \
    "s_nop 0\n\t" \
    "buffer_load_dwordx4 %[l16], %[rs], %[t1] offen offset:0 sc1 lds\n\t" \
    "buffer_load_dwordx4 %[l16], %[rs], %[t1] offen offset:1024 sc1 lds\n\t" \
    "buffer_load_dwordx4 %[l16], %[rs], %[t1] offen offset:2048 sc1 lds\n\t" \
    "buffer_load_dwordx4 %[l16], %[rs], %[t1] offen offset:3072 sc1 lds\n\t" \
    "s_add_u32 %[wq], %[wq], 16384\n\t" \
    "v_cvt_pk_bf16_f32 v160, v168, v169\n\t" \
    "v_cvt_pk_bf16_f32 v161, v170, v171\n\t" \
    "v_exp_f32 v168, v68\n\t" \
    "v_exp_f32 v169, v69\n\t" \
    "v_exp_f32 v170, v70\n\t" \
    "v_exp_f32 v171, v71\n\t" \
    "v_mfma_f32_32x32x16_bf16 v[80:95], v[132:135], a[236:239], v[80:95]\n\t" \
    "v_cvt_pk_bf16_f32 v162, v168, v169\n\t" \
    "v_cvt_pk_bf16_f32 v163, v170, v171\n\t" \
    "v_exp_f32 v168, v72\n\t" \
    "v_exp_f32 v169, v73\n\t" \
    "v_exp_f32 v170, v74\n\t" \
    "v_exp_f32 v171, v75\n\t" \
    "v_mfma_f32_32x32x16_bf16 v[48:63], v[124:127], v[164:167], v[48:63]\n\t" \
    "v_cvt_pk_bf16_f32 v164, v168, v169\n\t" \
    "v_cvt_pk_bf16_f32 v165, v170, v171\n\t" \
    "v_exp_f32 v168, v76\n\t" \
    "v_exp_f32 v169, v77\n\t" \
    "v_exp_f32 v170, v78\n\t" \
    "v_exp_f32 v171, v79\n\t" \
    "v_mfma_f32_32x32x16_bf16 v[0:15], v[136:139], v[160:163], v[0:15]\n\t" \
    "v_cvt_pk_bf16_f32 v166, v168, v169\n\t" \
    "v_cvt_pk_bf16_f32 v167, v170, v171\n\t" \
    "v_exp_f32 v168, v80\n\t" \
    "v_exp_f32 v169, v81\n\t" \
    "v_exp_f32 v170, v82\n\t" \
    "v_exp_f32 v171, v83\n\t" \
    "v_mfma_f32_32x32x16_bf16 v[64:79], v[128:131], a[240:243], 0\n\t" \
    "v_cvt_pk_bf16_f32 v160, v168, v169\n\t" \
    "v_cvt_pk_bf16_f32 v161, v170, v171\n\t" \
    "v_exp_f32 v168, v84\n\t" \
    "v_exp_f32 v169, v85\n\t" \
    "v_exp_f32 v170, v86\n\t" \
    "v_exp_f32 v171, v87\n\t" \
    "v_mfma_f32_32x32x16_bf16 v[64:79], v[132:135], a[244:247], v[64:79]\n\t" \
    "v_cvt_pk_bf16_f32 v162, v168, v169\n\t" \
    "v_cvt_pk_bf16_f32 v163, v170, v171\n\t" \
    "v_exp_f32 v168, v88\n\t" \
    "v_exp_f32 v169, v89\n\t" \
    "v_exp_f32 v170, v90\n\t" \
    "v_exp_f32 v171, v91\n\t" \
    "v_mfma_f32_32x32x16_bf16 v[0:15], v[140:143], v[164:167], v[0:15]\n\t" \
    "v_cvt_pk_bf16_f32 v164, v168, v169\n\t" \
    "v_cvt_pk_bf16_f32 v165, v170, v171\n\t" \
    "v_exp_f32 v168, v92\n\t" \
    "v_exp_f32 v169, v93\n\t" \
    "v_exp_f32 v170, v94\n\t" \
    "v_exp_f32 v171, v95\n\t" \
    "v_mfma_f32_32x32x16_bf16 v[16:31], v[136:139], v[160:163], v[16:31]\n\t" \
    "v_cvt_pk_bf16_f32 v166, v168, v169\n\t" \
    "v_cvt_pk_bf16_f32 v167, v170, v171\n\t" \
    "v_exp_f32 v168, v64\n\t" \
    "v_exp_f32 v169, v65\n\t" \
    "v_exp_f32 v170, v66\n\t" \
    "v_exp_f32 v171, v67\n\t" \
    "v_mfma_f32_32x32x16_bf16 v[80:95], v[128:131], a[248:251], 0\n\t" \
    "v_cvt_pk_bf16_f32 v160, v168, v169\n\t" \
    "v_cvt_pk_bf16_f32 v161, v170, v171\n\t" \
    "v_exp_f32 v168, v68\n\t" \
    "v_exp_f32 v169, v69\n\t" \
    "v_exp_f32 v170, v70\n\t" \
    "v_exp_f32 v171, v71\n\t" \
    "v_mfma_f32_32x32x16_bf16 v[80:95], v[132:135], a[252:255], v[80:95]\n\t" \
    "v_cvt_pk_bf16_f32 v162, v168, v169\n\t" \
    "v_cvt_pk_bf16_f32 v163, v170, v171\n\t" \
    "v_exp_f32 v168, v72\n\t" \
    "v_exp_f32 v169, v73\n\t" \
    "v_exp_f32 v170, v74\n\t" \
    "v_exp_f32 v171, v75\n\t" \
    "v_mfma_f32_32x32x16_bf16 v[16:31], v[140:143], v[164:167], v[16:31]\n\t" \
    "v_cvt_pk_bf16_f32 v164, v168, v169\n\t" \
    "v_cvt_pk_bf16_f32 v165, v170, v171\n\t" \
    "v_exp_f32 v168, v76\n\t" \
    "v_exp_f32 v169, v77\n\t" \
    "v_exp_f32 v170, v78\n\t" \
    "v_exp_f32 v171, v79\n\t" \
    "v_mfma_f32_32x32x16_bf16 v[32:47], v[136:139], v[160:163], v[32:47]\n\t" \
    "v_cvt_pk_bf16_f32 v166, v168, v169\n\t" \
    "v_cvt_pk_bf16_f32 v167, v170, v171\n\t" \
    "s_waitcnt vmcnt(12)\n\t" \
    "v_exp_f32 v168, v80\n\t" \
    "v_exp_f32 v169, v81\n\t" \
    "v_exp_f32 v170, v82\n\t" \
    "v_exp_f32 v171, v83\n\t" \
    "v_mfma_f32_32x32x16_bf16 v[64:79], v[144:147], a[224:227], 0\n\t" \
    "v_cvt_pk_bf16_f32 v160, v168, v169\n\t" \
    "v_cvt_pk_bf16_f32 v161, v170, v171\n\t" \
    "v_exp_f32 v168, v84\n\t" \
    "v_exp_f32 v169, v85\n\t" \
    "v_exp_f32 v170, v86\n\t" \
    "v_exp_f32 v171, v87\n\t" \
    "v_mfma_f32_32x32x16_bf16 v[64:79], v[148:151], a[228:231], v[64:79]\n\t" \
    "v_cvt_pk_bf16_f32 v162, v168, v169\n\t" \
    "v_cvt_pk_bf16_f32 v163, v170, v171\n\t" \
    "v_exp_f32 v168, v88\n\t" \
    "v_exp_f32 v169, v89\n\t" \
    "v_exp_f32 v170, v90\n\t" \
    "v_exp_f32 v171, v91\n\t" \
    "v_mfma_f32_32x32x16_bf16 v[32:47], v[140:143], v[164:167], v[32:47]\n\t" \
    "v_cvt_pk_bf16_f32 v164, v168, v169\n\t" \
    "v_cvt_pk_bf16_f32 v165, v170, v171\n\t" \
    "v_exp_f32 v168, v92\n\t" \
    "v_exp_f32 v169, v93\n\t" \
    "v_exp_f32 v170, v94\n\t" \
    "v_exp_f32 v171, v95\n\t" \
    "v_mfma_f32_32x32x16_bf16 v[48:63], v[136:139], v[160:163], v[48:63]\n\t" \
    "v_cvt_pk_bf16_f32 v166, v168, v169\n\t" \
    "v_cvt_pk_bf16_f32 v167, v170, v171\n\t" \
    "v_exp_f32 v168, v64\n\t" \
    "v_exp_f32 v169, v65\n\t" \
    "v_exp_f32 v170, v66\n\t" \
    "v_exp_f32 v171, v67\n\t" \
    "v_mfma_f32_32x32x16_bf16 v[80:95], v[144:147], a[232:235], 0\n\t" \
    "s_min_u32 %[t0], %[wqo], 53248\n\t" \
    "s_add_u32 %[t1], %[t0], %[wos]\n\t" \
    "s_mov_b32 m0, %[t0]\n\t" \
    "s_nop 0\n\t" \
    "buffer_load_dwordx4 %[l16], %[rs], %[t1] offen offset:0 sc1 lds\n\t" \
    "buffer_load_dwordx4 %[l16], %[rs], %[t1] offen offset:1024 sc1 lds\n\t" \
    "buffer_load_dwordx4 %[l16], %[rs], %[t1] offen offset:2048 sc1 lds\n\t" \
    "buffer_load_dwordx4 %[l16], %[rs], %[t1] offen offset:3072 sc1 lds\n\t" \
    "s_add_u32 %[wqo], %[wqo], 16384\n\t" \
    "v_cvt_pk_bf16_f32 v160, v168, v169\n\t" \
    "v_cvt_pk_bf16_f32 v161, v170, v171\n\t" \
    "v_exp_f32 v168, v68\n\t" \
    "v_exp_f32 v169, v69\n\t" \
    "v_exp_f32 v170, v70\n\t" \
    "v_exp_f32 v171, v71\n\t" \
    "v_mfma_f32_32x32x16_bf16 v[80:95], v[148:151], a[236:239], v[80:95]\n\t" \
    "v_cvt_pk_bf16_f32 v162, v168, v169\n\t" \
    "v_cvt_pk_bf16_f32 v163, v170, v171\n\t" \
    "v_exp_f32 v168, v72\n\t" \
    "v_exp_f32 v169, v73\n\t" \
    "v_exp_f32 v170, v74\n\t" \
    "v_exp_f32 v171, v75\n\t" \
    "v_mfma_f32_32x32x16_bf16 v[48:63], v[140:143], v[164:167], v[48:63]\n\t" \
    "v_cvt_pk_bf16_f32 v164, v168, v169\n\t" \
    "v_cvt_pk_bf16_f32 v165, v170, v171\n\t" \
    "v_exp_f32 v168, v76\n\t" \
    "v_exp_f32 v169, v77\n\t" \
    "v_exp_f32 v170, v78\n\t" \
    "v_exp_f32 v171, v79\n\t" \
    "v_mfma_f32_32x32x16_bf16 v[0:15], v[152:155], v[160:163], v[0:15]\n\t" \
    "v_cvt_pk_bf16_f32 v166, v168, v169\n\t" \
    "v_cvt_pk_bf16_f32 v167, v170, v171\n\t" \
    "v_exp_f32 v168, v80\n\t" \
    "v_exp_f32 v169, v81\n\t" \
    "v_exp_f32 v170, v82\n\t" \
    "v_exp_f32 v171, v83\n\t" \
    "v_mfma_f32_32x32x16_bf16 v[64:79], v[144:147], a[240:243], 0\n\t" \
    "v_cvt_pk_bf16_f32 v160, v168, v169\n\t" \
    "v_cvt_pk_bf16_f32 v161, v170, v171\n\t" \
    "v_exp_f32 v168, v84\n\t" \
    "v_exp_f32 v169, v85\n\t" \
    "v_exp_f32 v170, v86\n\t" \
    "v_exp_f32 v171, v87\n\t" \
    "v_mfma_f32_32x32x16_bf16 v[64:79], v[148:151], a[244:247], v[64:79]\n\t" \
    "v_cvt_pk_bf16_f32 v162, v168, v169\n\t" \
    "v_cvt_pk_bf16_f32 v163, v170, v171\n\t" \
    "v_exp_f32 v168, v88\n\t" \
    "v_exp_f32 v169, v89\n\t" \
    "v_exp_f32 v170, v90\n\t" \
    "v_exp_f32 v171, v91\n\t" \
    "v_mfma_f32_32x32x16_bf16 v[0:15], v[156:159], v[164:167], v[0:15]\n\t" \
    "v_cvt_pk_bf16_f32 v164, v168, v169\n\t" \
    "v_cvt_pk_bf16_f32 v165, v170, v171\n\t" \
    "v_exp_f32 v168, v92\n\t" \
    "v_exp_f32 v169, v93\n\t" \
    "v_exp_f32 v170, v94\n\t" \
    "v_exp_f32 v171, v95\n\t" \
    "v_mfma_f32_32x32x16_bf16 v[16:31], v[152:155], v[160:163], v[16:31]\n\t" \
    "v_cvt_pk_bf16_f32 v166, v168, v169\n\t" \
    "v_cvt_pk_bf16_f32 v167, v170, v171\n\t" \
    ".Lattn_tail%=:\n\t" \
    "v_exp_f32 v168, v64\n\t" \
    "v_exp_f32 v169, v65\n\t" \
    "v_exp_f32 v170, v66\n\t" \
    "v_exp_f32 v171, v67\n\t" \
    "v_mfma_f32_32x32x16_bf16 v[80:95], v[144:147], a[248:251], 0\n\t" \
    "v_cvt_pk_bf16_f32 v160, v168, v169\n\t" \
    "v_cvt_pk_bf16_f32 v161, v170, v171\n\t" \
    "ds_write_b128 %[sw], v[0:3] offset:0\n\t" \
    "v_exp_f32 v168, v68\n\t" \
    "v_exp_f32 v169, v69\n\t" \
    "v_exp_f32 v170, v70\n\t" \
    "v_exp_f32 v171, v71\n\t" \
    "v_mfma_f32_32x32x16_bf16 v[80:95], v[148:151], a[252:255], v[80:95]\n\t" \
    "v_cvt_pk_bf16_f32 v162, v168, v169\n\t" \
    "v_cvt_pk_bf16_f32 v163, v170, v171\n\t" \
    "ds_write_b128 %[sw], v[4:7] offset:1024\n\t" \
    "v_exp_f32 v168, v72\n\t" \
    "v_exp_f32 v169, v73\n\t" \
    "v_exp_f32 v170, v74\n\t" \
    "v_exp_f32 v171, v75\n\t" \
    "v_mfma_f32_32x32x16_bf16 v[16:31], v[156:159], v[164:167], v[16:31]\n\t" \
    "v_cvt_pk_bf16_f32 v164, v168, v169\n\t" \
    "v_cvt_pk_bf16_f32 v165, v170, v171\n\t" \
    "ds_write_b128 %[sw], v[8:11] offset:2048\n\t" \
    "v_exp_f32 v168, v76\n\t" \
    "v_exp_f32 v169, v77\n\t" \
    "v_exp_f32 v170, v78\n\t" \
    "v_exp_f32 v171, v79\n\t" \
    "v_mfma_f32_32x32x16_bf16 v[32:47], v[152:155], v[160:163], v[32:47]\n\t" \
    "v_cvt_pk_bf16_f32 v166, v168, v169\n\t" \
    "v_cvt_pk_bf16_f32 v167, v170, v171\n\t" \
    "ds_write_b128 %[sw], v[12:15] offset:3072\n\t" \
    "v_exp_f32 v168, v80\n\t" \
    "v_exp_f32 v169, v81\n\t" \
    "v_exp_f32 v170, v82\n\t" \
    "v_exp_f32 v171, v83\n\t" \
    "s_nop 0\n\t" \
    "v_cvt_pk_bf16_f32 v160, v168, v169\n\t" \
    "v_cvt_pk_bf16_f32 v161, v170, v171\n\t" \
    "ds_write_b128 %[sw], v[16:19] offset:4096\n\t" \
    "v_exp_f32 v168, v84\n\t" \
    "v_exp_f32 v169, v85\n\t" \
    "v_exp_f32 v170, v86\n\t" \
    "v_exp_f32 v171, v87\n\t" \
    "s_nop 0\n\t" \
    "v_cvt_pk_bf16_f32 v162, v168, v169\n\t" \
    "v_cvt_pk_bf16_f32 v163, v170, v171\n\t" \
    "ds_write_b128 %[sw], v[20:23] offset:5120\n\t" \
    "v_exp_f32 v168, v88\n\t" \
    "v_exp_f32 v169, v89\n\t" \
    "v_exp_f32 v170, v90\n\t" \
    "v_exp_f32 v171, v91\n\t" \
    "v_mfma_f32_32x32x16_bf16 v[32:47], v[156:159], v[164:167], v[32:47]\n\t" \
    "v_cvt_pk_bf16_f32 v164, v168, v169\n\t" \
    "v_cvt_pk_bf16_f32 v165, v170, v171\n\t" \
    "ds_write_b128 %[sw], v[24:27] offset:6144\n\t" \
    "v_exp_f32 v168, v92\n\t" \
    "v_exp_f32 v169, v93\n\t" \
    "v_exp_f32 v170, v94\n\t" \
    "v_exp_f32 v171, v95\n\t" \
    "v_mfma_f32_32x32x16_bf16 v[48:63], v[152:155], v[160:163], v[48:63]\n\t" \
    "v_cvt_pk_bf16_f32 v166, v168, v169\n\t" \
    "v_cvt_pk_bf16_f32 v167, v170, v171\n\t" \
    "ds_write_b128 %[sw], v[28:31] offset:7168\n\t" \
    "s_nop 0\n\t" \
    "v_mfma_f32_32x32x16_bf16 v[48:63], v[156:159], v[164:167], v[48:63]\n\t" \
    "ds_write_b128 %[sw], v[32:35] offset:8192\n\t" \
    "ds_write_b128 %[sw], v[36:39] offset:9216\n\t" \
    "ds_write_b128 %[sw], v[40:43] offset:10240\n\t" \
    "ds_write_b128 %[sw], v[44:47] offset:11264\n\t" \
    "s_cmp_eq_u32 %[layer], 0\n\t" \
    "s_cbranch_scc1 .Lattn_end%=\n\t" \
    "s_waitcnt vmcnt(0)\n\t" \
    ".Lattn_end%=:\n\t" \
    "s_nop 5\n\t"
#define ATTN_CLOBBER "v64", "v65", "v66", "v67", "v68", "v69", "v70", "v71", "v72", "v73", "v74", "v75", "v76", "v77", "v78", "v79", "v80", "v81", "v82", "v83", "v84", "v85", "v86", "v87", "v88", "v89", "v90", "v91", "v92", "v93", "v94", "v95", "v112", "v113", "v114", "v115", "v116", "v117", "v118", "v119", "v120", "v121", "v122", "v123", "v124", "v125", "v126", "v127", "v128", "v129", "v130", "v131", "v132", "v133", "v134", "v135", "v136", "v137", "v138", "v139", "v140", "v141", "v142", "v143", "v144", "v145", "v146", "v147", "v148", "v149", "v150", "v151", "v152", "v153", "v154", "v155", "v156", "v157", "v158", "v159", "v160", "v161", "v162", "v163", "v164", "v165", "v166", "v167", "v168", "v169", "v170", "v171", "v172", "v173", "v174", "v175", "v176", "v177", "v178", "v179", "v180", "v181", "v182", "v183", "v184", "v185", "v186", "v187"

__global__ __launch_bounds__(256, 1) void mega(const u16* __restrict__ x, const u16* __restrict__ wqkv, const u16* __restrict__ wo, const u16* __restrict__ w1,
                                               const u16* __restrict__ b1, const u16* __restrict__ w2, u16* __restrict__ out, char* __restrict__ ws) {
    int lane = threadIdx.x & 63, w = __builtin_amdgcn_readfirstlane(threadIdx.x >> 6);
    const int b = blockIdx.x;
    int g = (b & 7) * 2 + (b >> 7), j = (b >> 3) & 15;
    int r, hh, m, n, t, u, gq, jq; unsigned trl, l16;
    int layer = 0;
#define QROLE() asm volatile("s_and_b32 %0, %2, 8\n\ts_lshl_b32 %1, %2, 4\n\ts_or_b32 %1, %1, %3\n\ts_bfe_u32 %1, %1, 0x30002\n\ts_or_b32 %0, %0, %1\n\t" \
                             "s_and_b32 %1, %2, 6\n\ts_lshl_b32 %1, %1, 1\n\ts_and_b32 vcc_lo, %3, 3\n\ts_or_b32 %1, %1, vcc_lo" : "=&s"(gq), "=&s"(jq) : "s"(g), "s"(j) : "scc", "vcc")
#define FRESH() do { asm volatile("" : "+s"(g), "+s"(j), "+s"(w), "+s"(layer)); asm volatile("" : "+v"(lane)); r = lane & 31; hh = lane >> 5; \
        asm volatile("s_lshr_b32 %0, %4, 3\n\ts_and_b32 %1, %4, 7\n\ts_lshr_b32 %2, %5, 1\n\ts_and_b32 %3, %5, 1" : "=&s"(m), "=&s"(n), "=&s"(t), "=&s"(u) : "s"(j), "s"(w) : "scc"); \
        QROLE(); \
        trl = ((lane & 15) >> 2) * 64 + ((lane >> 4) & 1) * 32 + (lane & 3) * 8; l16 = lane * 16; } while (0)
    const rsrc_t rsW = __builtin_amdgcn_make_buffer_rsrc((void*)ws, 0, WS_BYTES, 0x00020000);
    u32x2 tag;
    int left; asm volatile("s_mov_b32 %0, 400000" : "=s"(left));
    f32x16 resid;
    u32x16 kv0;

    unsigned h_vf, h_sf, h_vp, h_sp;
    auto hand_prep = [&](int S) {
        const unsigned row = sma<5 * 65536>(layer, WS_FLAGS + S * 65536);
        h_vf = lane < 16 ? lane * 8 : WS_BYTES; h_vp = (lane & 15) * 256; h_sp = sma<4096>(S == 4 ? gq : S == 0 ? jq : g, row);
        h_sf = S == 0 ? sma<4096>(jq, sma<256>(gq, row)) : S == 1 ? sma<4096>(gq, sma<256>(jq, row)) : sma<4096>(g, sma<256>(j, row));
        if (S == 0 && layer == 0 && lane >= 16) {
            if (lane < 32) h_vf = WS_FLAGS + 12 * 65536 + (j * 16 + g) * 256 + (lane & 15) * 8 - h_sf;
            h_vp += WS_FLAGS + 12 * 65536 + (lane & 16 ? j : j ^ 8) * 4096 - h_sp;
        }
        pin_v(h_vf); pin_v(h_vp); pin_s(h_sf); pin_s(h_sp);
    };
#define HAND(N, PRE) do { if (N == 0) __builtin_amdgcn_s_waitcnt(0x0070); window<N, PRE>(tag, h_vf, h_sf, h_vp, h_sp, rsW, w, left); } while (0)
    auto stage = [&](unsigned lds, unsigned src, int g0, int g1, int w0, int nw) {
        const unsigned sb = sma<T_STRIDE>(src == T_Q ? jq : j, WS_WT + src);
        for (int q = g0 + w - w0; q < g1; q += nw) dma4(lds + q * 4096, l16, rsW, sb + q * 4096);
    };
    unsigned px_s0, px_s1, px_s2, px_v1, px_v2;
    auto px_prep = [&](unsigned base) {
        const unsigned tb = sma<XT>(t, sma<2 * XT>(m, sma<4 * XT>(g, base))), fb = sma<7168>(n >> 1, tb), f0 = sma<3072>(n & 1, fb);
        px_s0 = f0 + u * 2048; px_s1 = u ? fb + 6144 : f0 + 1024; px_s2 = tb + 28672 + (2 * n + u) * 256;
        px_v1 = (u == 0 || hh == (n & 1)) ? l16 : WS_BYTES; px_v2 = hh ? WS_BYTES : r * 8;
        pin_v(px_v1); pin_v(px_v2); asm volatile("" : "+s"(px_s0), "+s"(px_s1), "+s"(px_s2));
    };
    auto publish_p = [&](const u32x4 p0, u32x4 p1) {
        stg(p0, rsW, l16, px_s0);
        if (u) {
            auto a = __builtin_amdgcn_permlane32_swap(p1[0], p1[0], false, false), c = __builtin_amdgcn_permlane32_swap(p1[1], p1[1], false, false);
            stg((u32x4){a[0], c[0], a[1], c[1]}, rsW, px_v1, px_s1);
            p1[2] = 0u; p1[3] = 0u;
        } else stg(p1, rsW, px_v1, px_s1);
        float s1 = 0.f, s2 = 0.f, s3 = 0.f, s4 = 0.f;
        sfor<4>([&](auto i) { s1 = dot2(p0[i.value], 0x3F803F80u, s1); s2 = dot2(p0[i.value], p0[i.value], s2); s3 = dot2(p1[i.value], 0x3F803F80u, s3); s4 = dot2(p1[i.value], p1[i.value], s4); });
        s1 = half_sum(s1 + s3); s2 = half_sum(s2 + s4);
        __builtin_amdgcn_raw_buffer_store_b64((u32x2){f2u(s1), f2u(s2)}, rsW, px_v2, px_s2, 16);
    };
    auto publish_x = [&]() { publish_p(pack8(resid, 0, 1.f), pack8(resid, 1, 1.f)); };
    auto red_put = [&](unsigned sw, int d, const f32x16& v) {
        sfor<4>([&](auto k) { lds_st16(sw + d * 4096 + k.value * 1024, (f32x4){v[4 * k.value], v[4 * k.value + 1], v[4 * k.value + 2], v[4 * k.value + 3]}); }); };
    auto red_put1 = [&](unsigned sw, int d, const f32x16& v, int k) { lds_st16(sw + d * 4096 + k * 1024, (f32x4){v[4 * k], v[4 * k + 1], v[4 * k + 2], v[4 * k + 3]}); };
    auto red_get = [&](f32x16& o, bool asc) {
        unsigned a[3]; f32x4 v[3][4];
        sfor<3>([&](auto d) { const int o2 = w ^ (d.value + 1); a[d.value] = LDS_SCR48 + o2 * 12288 + (asc ? w - (w > o2) : d.value) * 4096 + l16; pin_v(a[d.value]); });
        BAR();
        sfor<4>([&](auto k) { sfor<3>([&](auto d) { v[d.value][k.value] = lds_ld16(a[d.value] + k.value * 1024); }); });
        SB();
        sfor<4>([&](auto k) { const f32x4 s = v[0][k.value] + v[1][k.value];
            const f32x4 q = (f32x4){o[4 * k.value], o[4 * k.value + 1], o[4 * k.value + 2], o[4 * k.value + 3]} + v[2][k.value] + s;
            sfor<4>([&](auto e) { o[4 * k.value + e.value] = q[e.value]; }); });
    };
    auto make_waug = [&](float cs, unsigned bias16) {
        float ch; const unsigned chb = hi_lo(cs, ch);
        return (u32x4){chb | (chb << 16), (cvtpk(cs - ch, 0.f) & 0xffffu) | (bias16 << 16), bias16, 0u};
    };

    unsigned fk[2], r0[2], rr[2], ta[2], td[2]; u32x4 ov;
    auto share = [&]() {
        sfor<2>([&](auto i) { unsigned fq, fm; fk[i.value] = (i.value && g < 12) ? g + 16 : g;
            asm("s_mul_i32 %0, %3, 37\n\ts_lshr_b32 %0, %0, 8\n\ts_mul_i32 %1, %0, -7\n\ts_add_i32 %1, %1, %3\n\ts_lshl_b32 %2, %1, 3\n\ts_lshr_b64 vcc, %4, %2\n\ts_and_b32 %2, vcc_lo, 0xff"
                : "=&s"(fq), "=&s"(fm), "=&s"(r0[i.value]) : "s"(fk[i.value]), "s"(0x30584838201000ull) : "scc", "vcc");
            r0[i.value] = sma<112>(fq, r0[i.value]); rr[i.value] = (lane >> 2) + ((fm == 6 && lane >= 32) ? 48 : 0);
            ta[i.value] = LDS_WO + w * 8192 + i.value * 1024 + trl + (fm == 6 ? 512 : 256) * hh; td[i.value] = fm == 6 ? 256 : 512; });
    };
    auto late_load = [&](int k) {
        const unsigned c16 = (lane & 3) * 16, v896 = __umul24(lane >> 2, 896) + c16, stg0 = LDS_WO + w * 8192;
        if (k < 2) { const unsigned i2 = 14 * g + 7 * m + min(2 * w + k, 6);
            dma_nt(stg0 + k * 1024, __umul24(rr[k], 3584) + c16, __builtin_amdgcn_make_buffer_rsrc((void*)w1, 0, 448 * 3584, 0x00020000), sma<3584>(r0[k], sma<224>(j, w * 64)));
            dma_nt(stg0 + (2 + k) * 1024, v896, __builtin_amdgcn_make_buffer_rsrc((void*)w2, 0, 1792 * 896, 0x00020000), sma<16 * 896>(i2 >> 1, sma<112>(n, (i2 & 1) * 64)));
        } else { const int po = min(2 * g + m + 32 * u, 55), pq = po >= 28;
            ald_x4(ov, v896, __builtin_amdgcn_make_buffer_rsrc((void*)wo, 0, 448 * 896, 0x00020000), sma<16 * 896>(po - 28 * pq, sma<112>(n, pq * 64))); }
    };
    u32x4 dq[4]; float dc;
    auto late_read = [&]() {
        dq[0] = trfrag(ta[0], ta[0] + td[0]); dq[1] = trfrag(ta[1], ta[1] + td[1]);
        sfor<2>([&](auto i) { const unsigned a = LDS_WO + w * 8192 + (2 + i.value) * 1024 + trl + 256 * hh; dq[2 + i.value] = trfrag(a, a + 512);
            if (((7 * m + min(2 * w + i.value, 6)) & 1) && r >= 24) dq[2 + i.value] = (u32x4){0u, 0u, 0u, 0u}; });
        if (2 * g + m + 32 * u >= 28 && (lane & 3) == 3) ov = (u32x4){0u, 0u, 0u, 0u};
        float c1 = 0.f, c2 = 0.f;
        sfor<4>([&](auto e) { c1 = dot2(dq[0][e.value], 0x3F803F80u, c1); c2 = dot2(dq[1][e.value], 0x3F803F80u, c2); });
        if (g < 12) c1 += c2;
        dc = half_sum(c1);
        sfor<4>([&](auto i) { pin_v(dq[i.value]); }); pin_v(ov); pin_v(dc);
    };
    auto late_store = [&](int k) {
        const unsigned wt = sma<T_STRIDE>(j, WS_WT), wn = sma<T_STRIDE>(n, WS_WT);
        if (k == 0) { const unsigned fs = w < 3 ? 1024 : 512, v1 = w < 3 ? l16 : r < 16 ? (hh * 16 + r) * 16 : WS_BYTES;
            stg(dq[0], rsW, v1, wt + T_1 + w * IMG + g * fs); stg(dq[1], rsW, v1, wt + T_1 + w * IMG + (g < 12 ? g + 16 : g) * fs); }
        if (k == 1) sfor<2>([&](auto i) { stg(dq[2 + i.value], rsW, l16, wn + T_2 + (14 * g + 7 * m + min(2 * w + i.value, 6)) * 1024); });
        if (k == 2) { stg(ov, rsW, t ? WS_BYTES : l16, wn + T_O + min(2 * g + m + 32 * u, 55) * 1024);
            __builtin_amdgcn_raw_buffer_store_b32(f2u(dc), rsW, hh ? WS_BYTES : r * 4, wt + T_C1 + (g * 4 + w) * 128, 16); }
    };
    {
        u32x2 cw2;
        asm volatile("global_load_dwordx2 %0, %1, %2" : "=v"(cw2) : "v"(min((unsigned)((b >> 3) * 256 + threadIdx.x) * 8u, (unsigned)CODE_BYTES)), "s"(__builtin_amdgcn_s_getpc() & ~7ull) : "memory");
        SB();
        FRESH();
        const rsrc_t rsX = __builtin_amdgcn_make_buffer_rsrc((void*)x, 0, 2048 * 896, 0x00020000);
        const rsrc_t rsQKV = __builtin_amdgcn_make_buffer_rsrc((void*)wqkv, 0, 448 * 2688, 0x00020000);
        u32x2 xv[4]; unsigned tch;
        const unsigned vx = __umul24((4 * g + 2 * m + t) * 32 + r, 896) + (56 * n + 32 * u + 4 * hh) * 2;
        sfor<4>([&](auto i) { ald_plain<16 * i.value>(xv[i.value], vx, rsX); });
        SB();
        share();
        const int wq = min(w, 2);
        sfor<2>([&](auto i) { dma_nt(LDS_WO + w * 8192 + i.value * 1024, __umul24(rr[i.value], 2688) + (lane & 3) * 16, rsQKV, sma<2688>(r0[i.value], sma<896>(wq, j * 56))); });
        ald<0>(tag, 0u, rsW, 0u);
        asm volatile("global_load_dword %0, %1, off" : "=v"(tch) : "v"((const char*)b1 + 224 * j) : "memory");
        px_prep(WS_X0);
        if (w == 0) *(LAS unsigned*)(size_t)(LDS_WO + 2 * IMG + lane * 4) = 0u;
        const unsigned wt = sma<T_STRIDE>(j, WS_WT);
        unsigned sq[2], sc = sma<384>(g, sma<128>(wq, wt + T_CQ)), vc = hh ? WS_BYTES : r * 4;
        sfor<2>([&](auto i) { sq[i.value] = sma<3072>(fk[i.value], sma<1024>(wq, wt + T_Q)); pin_s(sq[i.value]); pin_v(ta[i.value]); });
        h_vf = (w || lane >= 32) ? WS_BYTES : (lane & 16 ? (16 + j) * 16 + g : g * 16 + j) * 256 + (lane & 15) * 8; h_vp = ((lane & 16 ? 16 + jq : gq) * 16 + (lane & 15)) * 256;
        asm volatile("s_mov_b32 %0, %1" : "=s"(h_sf) : "i"(WS_FLAGS + 10 * 65536)); h_sp = h_sf;
        pin_s(sc); pin_v(vc); pin_v(h_vf); pin_v(h_vp);
        SB();
        await<0>(tag); landed(cw2); landed(tch); sfor<4>([&](auto i) { landed(xv[i.value]); });
        u32x4 f0 = trfrag(ta[0], ta[0] + td[0]), f1 = trfrag(ta[1], ta[1] + td[1]);
        publish_p((u32x4){xv[0][0], xv[0][1], xv[1][0], xv[1][1]}, (u32x4){xv[2][0], xv[2][1], xv[3][0], xv[3][1]});
        if (r >= 28) { f0 = (u32x4){0u, 0u, 0u, 0u}; f1 = f0; }
        stg(f0, rsW, l16, sq[0]); stg(f1, rsW, l16, sq[1]);
        float c1 = 0.f, c2 = 0.f;
        sfor<4>([&](auto e) { c1 = dot2(f0[e.value], 0x3F803F80u, c1); c2 = dot2(f1[e.value], 0x3F803F80u, c2); });
        if (g < 12) c1 += c2;
        __builtin_amdgcn_raw_buffer_store_b32(f2u(half_sum(c1)), rsW, vc, sc, 16);
        SB();
        tag[0] += 1; tag[1] ^= 0x5bd1e995u;
        sfor<4>([&](auto i) { resid[4 * i.value] = bflo(xv[i.value][0]); resid[4 * i.value + 1] = bfhi(xv[i.value][0]); resid[4 * i.value + 2] = bflo(xv[i.value][1]); resid[4 * i.value + 3] = bfhi(xv[i.value][1]); });
        pin_v(resid);
        bar_vm<0>();
        __builtin_amdgcn_raw_buffer_store_b64(tag, rsW, h_vf, h_sf, 16);
        SB();
        late_load(0); late_load(1); late_load(2);
        await<0>(ov);
        late_read();
        poll_only(tag, h_vp, h_sp, rsW, w, left);
        BAR();
    }

#define LB sma<L_STRIDE>(layer, WS_DATA)
        auto qkv = [&](auto first) {
            constexpr bool F = first.value;
            u32x4 xf[28]; u32x2 st[8];
            const unsigned tb = sma<XT>(w, sma<4 * XT>(gq, F ? WS_X0 : WS_DATA + L_X2)), wt = sma<T_STRIDE>(jq, WS_WT);
            auto xload = [&](auto f) { ald<(f.value & 3) * 1024>(xf[f.value], l16, rsW, tb + (f.value >> 2) * 4096); };
            auto wstage = [&](int k) { const int q = min(4 * k + w, 20); dma4(LDS_R1 + q * 4096, l16, rsW, wt + T_Q + q * 4096); };
            constexpr int X0 = F ? 4 : 12, SF = 12;
            auto clamp = [](int v, int lo, int hi) { return v < lo ? lo : v > hi ? hi : v; };
            auto issued = [clamp](int f) { return F ? clamp(X0 + 2 * (f + 1), 0, 28) + 4 * clamp(f - SF + 1, 0, 4) + 4 * ((f >= 0) + (f >= QB2) + (f >= QC1) + (f >= QC2))
                                                    : clamp(X0 + f + 1, 0, 28) + clamp(f, 0, 8); };
            auto pos = [clamp](int k) { if (F) { const int fs = k < X0 ? -1 : (k - X0) / 2; return k + 4 * ((fs >= 0) + (fs >= QB2) + (fs >= QC1) + (fs >= QC2)); }
                                        return k + clamp(k - X0 - 1, 0, 8); };
            float cq[8]; const unsigned vq = hh * 3072 + r * 4, sq = wt + T_CQ + min(w, 2) * 128;
            if constexpr (F) { wstage(0); wstage(1); }
            sfor<4>([&](auto f) { xload(f); });
            SB();
            unsigned wa = LDS_R1 + l16, wb = wa + 16 * 3072, ga = LDS_AUG + 2048 + r * 16, vs = hh * 2048 + r * 8, rb = LDS_HALF + w * 128 + hh * 16;
            constexpr unsigned LQ = WS_DATA + (F ? 0 : L_STRIDE);
            unsigned kvb = sma<4096>(gq, sma<KVQ>(w, sma<4 * KVQ>(jq, LQ + L_KV))), qo = LDS_Q + w * 2048 + l16;
            pin_v(wa); pin_v(wb); pin_v(ga); pin_v(vs); pin_v(rb); pin_s(kvb); pin_v(qo);
            SB();
            if constexpr (F) bar_vm<X0>(); else sfor<8>([&](auto f) { xload(std::integral_constant<int, 4 + f.value>{}); });
            SB();
            auto wfrag = [&](int f, int i) { return f == 28 ? lds_frag(ga + i * 512) : f < 16 ? lds_frag(wa + (3 * f + i) * 1024) : lds_frag(wb + (3 * (f - 16) + i) * 1024); };
            f32x16 aq, ak, av; u32x4 wf[3][3], xa; XAug xg; f32x4 rs4[4];
            sfor<2>([&](auto f) { sfor<3>([&](auto i) { wf[f.value][i.value] = wfrag(f.value, i.value); }); });
            await<X0 - 1>(xf[0]);
            sfor<28>([&](auto ff) {
                constexpr int f = ff.value;
                SB();
                if constexpr (f == 0) mfma0(aq, wf[0][0], xf[0]); else mfma_x(aq, wf[f % 3][0], xf[f], wf[f % 3][1], wf[f % 3][2]);
                SB();
                if constexpr (F && f == 8) bar_vm<issued(7) - (X0 + 8 + 2 * QB2)>();
                if constexpr (!F && f == 6) BAR();
                if constexpr (F && f == 19) BAR();
                if constexpr (f + 2 <= 28) sfor<3>([&](auto i) { wf[(f + 2) % 3][i.value] = wfrag(f + 2, i.value); });
                SB();
                if constexpr (f == 0) mfma0(ak, wf[0][1], xf[0]); else mfma(ak, wf[f % 3][1], xf[f]);
                SB();
                if constexpr (F && (f == 0 || f == QB2 || f == QC1 || f == QC2)) wstage(f == 0 ? 2 : f == QB2 ? 3 : f == QC1 ? 4 : 5);
                if constexpr (F && f < SF) xload(std::integral_constant<int, X0 + 2 * f>{});
                if constexpr (!F && f < 16) xload(std::integral_constant<int, X0 + f>{});
                if constexpr (F && f >= SF && f < SF + 4) sfor<2>([&](auto i) { ald<(2 * (f - SF) + i.value) * 256>(st[2 * (f - SF) + i.value], vs, rsW, tb + 28672); });
                if constexpr (F && f >= SF && f < SF + 4) sfor<2>([&](auto i) { constexpr int k = 2 * (f - SF) + i.value; def_v(cq[k]); ald<k * 384>(cq[k], vq, rsW, sq); });
                if constexpr (F && f == 18) {
                    const u32x4 gq = make_waug(half_sum(((cq[0] + cq[1]) + (cq[2] + cq[3])) + ((cq[4] + cq[5]) + (cq[6] + cq[7]))), 0u);
                    if (hh == 0 && w < 3) *(LAS u32x4*)(size_t)(LDS_AUG + 2048 + w * 512 + r * 16) = gq;
                }
                if constexpr (f >= 18 && f < 27) xaug_step<f - 18>(xg, st, xa);
                if constexpr (F && f >= 20 && f < 23) late_store(f - 20);
                if constexpr (f == 25) *(LAS float*)(size_t)(LDS_HALF + w * 128 + r * 4) = xg.rstd;
                if constexpr (f == 27) sfor<4>([&](auto i) { rs4[i.value] = lds_ld16(rb + 32 * i.value); });
                SB();
                if constexpr (f == 0) { mfma0(av, xf[0], wf[0][2]); } else mfma(av, xf[f], wf[f % 3][2]);
                SB();
                if constexpr (F && f < SF) xload(std::integral_constant<int, X0 + 2 * f + 1>{});
                if constexpr (!F && f >= 1 && f < 9) ald<(f - 1) * 256>(st[f - 1], vs, rsW, tb + 28672);
                if constexpr (f < (F ? 17 : 27)) await<(!F && f == 17 ? 7 : issued(f) - pos(f + 1) - 1)>(xf[f + 1]);
                if constexpr (F && f == 17) { await<0>(xf[18]); sfor<9>([&](auto i) { landed(xf[19 + i.value]); }); sfor<8>([&](auto i) { landed(st[i.value]); landed(cq[i.value]); }); }
                if constexpr (!F && f == 17) sfor<8>([&](auto i) { landed(st[i.value]); });
            });
            SB();
            mfma_x(ak, wf[1][1], xa, wf[1][0], wf[1][2]); mfma(av, xa, wf[1][2]); mfma(aq, wf[1][0], xa);
            const float rstd = xg.rstd;
            SETTLE3(aq, ak, av);

            u32x4 k1 = pack8(ak, 1, rstd);
            if (hh) { k1[2] = 0u; k1[3] = 0u; }
            const u32x4 k0 = pack8(ak, 0, rstd);
            stg(k0, rsW, l16, kvb); stg(k1, rsW, l16 + 1024, kvb);
            sfor<16>([&](auto i) { av[i.value] *= rs4[i.value >> 2][i.value & 3]; });
            u32x4 v0 = pack8(av, 0, 1.f), v1 = pack8(av, 1, 1.f);
            if (r >= 28) { const unsigned c = r == 28 ? 0x3F803F80u : 0u; v0 = (u32x4){c, c, c, c}; v1 = v0; }
            stg(v0, rsW, l16 + 2048, kvb); stg(v1, rsW, l16 + 3072, kvb);
            const float qs = rstd * (0.18898223650461361f * 1.4426950408889634f);
            u32x4 q0 = pack8(aq, 0, qs), q1 = pack8(aq, 1, qs);
            if (hh) { q1[2] = 0u; q1[3] = 0u; }
            *(LAS u32x4*)(size_t)qo = q0; *(LAS u32x4*)(size_t)(qo + 1024) = q1;
            sfor<4>([&](auto i) { kv0[i.value] = k0[i.value]; kv0[4 + i.value] = k1[i.value]; kv0[8 + i.value] = v0[i.value]; kv0[12 + i.value] = v1[i.value]; });
            pin16<F>(kv0);
        };
    FRESH();
    qkv(std::true_type{});
#pragma unroll 1
    for (;; ++layer) {
        FRESH();
        hand_prep(0);
        {
            f32x16 O[4]; u32x16 kvx;
            unsigned kvs = sma<KVQ>(w, sma<4 * KVQ>(jq, LB + L_KV)), kvo = gq * 4096, wq = w * 4096, wqo = w * 4096, t0, t1, it;
            const unsigned qb = LDS_Q + l16, wn = sma<T_STRIDE>(n, WS_WT);
            unsigned w2s = sma<57344>(w, wn + T_2), w1s = sma<T_STRIDE>(j, WS_WT + T_1), ww = sma<12288>(w, w1s + 13 * 4096), wos = wn + T_O;
            unsigned q0 = qb + (w == 0) * 2048, q1 = qb + (w < 2 ? 4096 : 2048), q2 = qb + (w < 3 ? 6144 : 4096), q3 = qb + w * 2048;
            unsigned w2a = l16 + u * 1024, w2b = l16 + (u ^ 1) * 1024, sw = LDS_SCR48 + w * 12288 + l16;
            unsigned vc1 = hh * 4096 + r * 4, sc1 = w1s - T_1 + T_C1 + w * 128, ag = LDS_AUG + w * 512 + r * 16; const u16* b1p = b1 + 112 * j + 32 * w + (w == 3 ? r & 15 : r);
            pin_v(vc1); pin_s(sc1); pin_v(ag); asm volatile("" : "+v"(b1p));
            pin_s(kvs); pin_s(wq); pin_s(wqo); pin_s(w2s); pin_s(w1s); pin_s(ww); pin_s(wos); pin_s(kvo); pin_v(q0); pin_v(q1); pin_v(q2); pin_v(q3);
            pin_v(l16); pin_v(w2a); pin_v(w2b); pin_v(sw);
            HAND(0, PRE_X);
            BAR();
            asm volatile(ATTN_ASM
                : "=&{v[0:15]}"(O[0]), "=&{v[16:31]}"(O[1]), "=&{v[32:47]}"(O[2]), "=&{v[48:63]}"(O[3]), "={v[96:111]}"(kvx), [kvs] "+s"(kvs), [kvo] "+s"(kvo), [wq] "+s"(wq), [wqo] "+s"(wqo), [ww] "+s"(ww), [t0] "=&s"(t0), [t1] "=&s"(t1), [it] "=&s"(it)
                : "{v[96:111]}"(kv0), [l16] "v"(l16), [w2a] "v"(w2a), [w2b] "v"(w2b), [rs] "s"(rsW), [layer] "s"(layer), [q0] "v"(q0), [q1] "v"(q1), [q2] "v"(q2), [q3] "v"(q3),
                  [sw] "v"(sw), [w1s] "s"(w1s), [wos] "s"(wos), [w2s] "s"(w2s), [oob] "s"(WS_BYTES), [vc1] "v"(vc1), [sc1] "s"(sc1), [ag] "v"(ag), [b1p] "v"(b1p)
                : "memory", "scc", "a255", ATTN_CLOBBER);
            FRESH();
            unsigned ob = sma<2048>(jq & 7, sma<OH>(jq >> 3, sma<2 * OH>(w, sma<8 * OH>(gq, LB + L_O)))); pin_s(ob);
            f32x16 o = O[3];
            red_get(o, true);
            float l = upper_half(o[12]);
            float amax = 0.f;
            sfor<16>([&](auto i) { amax = __builtin_fmaxf(amax, __builtin_fabsf(o[i.value])); });
            const bool ok = l >= 5.4e-20f && l <= 1.8e19f && amax <= 3.0e38f;
            if (!__all(ok)) {
                float mrun = -INFINITY;
                float zero = 0.f; pin_v(zero);
                sfor<16>([&](auto i) { o[i.value] = zero; });
                unsigned qr[8];
                asm volatile("v_accvgpr_read_b32 %0, a248\n\tv_accvgpr_read_b32 %1, a249\n\tv_accvgpr_read_b32 %2, a250\n\tv_accvgpr_read_b32 %3, a251\n\t"
                             "v_accvgpr_read_b32 %4, a252\n\tv_accvgpr_read_b32 %5, a253\n\tv_accvgpr_read_b32 %6, a254\n\tv_accvgpr_read_b32 %7, a255"
                             : "=v"(qr[0]), "=v"(qr[1]), "=v"(qr[2]), "=v"(qr[3]), "=v"(qr[4]), "=v"(qr[5]), "=v"(qr[6]), "=v"(qr[7]));
                const u32x4 qa = {qr[0], qr[1], qr[2], qr[3]}, qb2 = {qr[4], qr[5], qr[6], qr[7]};
#pragma unroll 1
                for (int T = 0; T < 64; ++T) {
                    const unsigned so = LB + L_KV + (jq * 4 + (T >> 4)) * KVQ + (T & 15) * 4096;
                    const u32x4 k0 = ldg(rsW, l16, so), k1 = ldg(rsW, l16 + 1024, so), v0 = ldg(rsW, l16 + 2048, so), v1 = ldg(rsW, l16 + 3072, so);
                    f32x16 s;
                    mfma0_safe(s, k0, qa); mfma_safe(s, k1, qb2);
                    float mt = s[0]; sfor<15>([&](auto i) { mt = __builtin_fmaxf(mt, s[i.value + 1]); });
                    const float mn = __builtin_fmaxf(mrun, half_max(mt)), al = __builtin_amdgcn_exp2f(mrun - mn);
                    mrun = mn;
                    sfor<16>([&](auto i) { o[i.value] *= al; s[i.value] = __builtin_amdgcn_exp2f(s[i.value] - mn); });
                    mfma_safe(o, v0, pack8(s, 0, 1.f)); mfma_safe(o, v1, pack8(s, 1, 1.f));
                }
                l = upper_half(o[12]);
            }
            const float rl = __builtin_amdgcn_rcpf(l);
            u32x4 o0 = pack8(o, 0, rl), o1 = pack8(o, 1, rl);
            if (hh) { o1[2] = 0u; o1[3] = 0u; }
            stg(o0, rsW, l16, ob); stg(o1, rsW, l16 + 1024, ob);
        }
        FRESH();
        hand_prep(1);
        {
            u32x4 of[16];
            unsigned ob = sma<OH>(w, sma<4 * OH>(m, sma<8 * OH>(g, LB + L_O))); pin_s(ob); pin_v(l16);
            HAND(0, PRE_2);
            if (w) stage(LDS_R1, T_1, 13, 21, 1, 3);
            BAR();
            auto oload = [&](auto s) { ald<(s.value & 3) * 1024>(of[s.value], l16, rsW, ob + (s.value >> 2) * 4096); };
            sfor<4>([&](auto s) { oload(s); });
            SB();
            const unsigned tA = LDS_WO + trl + 256 * hh + u * (8 * 28 * 64); unsigned tO = tA + u * IMG, tS = tA + (u ^ 1) * IMG;
            unsigned xw = LDS_SCR16 + w * 4096 + l16, xr = LDS_SCR16 + (w ^ 1) * 4096 + l16;
            pin_v(tO); pin_v(tS); pin_v(xw); pin_v(xr);
            px_prep(LB + L_X1);
            SB();
            sfor<8>([&](auto s) { oload(std::integral_constant<int, 4 + s.value>{}); });
            SB();
            auto wfrag = [&](int img, int s) { const unsigned o = (img ? tS : tO) + (28 * (s >> 1) + 16 * (s & 1)) * 64; return trfrag(o, o + 512); };
            f32x16 a0, a1; u32x4 wf[3][2];
            sfor<2>([&](auto s) { wf[s.value][0] = wfrag(0, s.value); wf[s.value][1] = wfrag(1, s.value); });
            await<11>(of[0]);
            sfor<16>([&](auto ss) {
                constexpr int s = ss.value;
                SB();
                if constexpr (s == 0) mfma0(a1, wf[0][1], of[0]); else mfma_x(a1, wf[s % 3][1], of[s], wf[s % 3][0], wf[s % 3][0]);
                SB();
                if constexpr (s + 2 < 16) { wf[(s + 2) % 3][0] = wfrag(0, s + 2); wf[(s + 2) % 3][1] = wfrag(1, s + 2); }
                SB();
                if constexpr (s == 0) { mfma0(a0, wf[0][0], of[0]); } else mfma(a0, wf[s % 3][0], of[s]);
                SB();
                if constexpr (s < 2) sfor<2>([&](auto i) { oload(std::integral_constant<int, 12 + 2 * s + i.value>{}); });
                if constexpr (s < 15) await<(s == 0 ? 14 : 16) - s - 2>(of[s + 1]);
            });
            asm volatile("s_nop 10" : "+v"(a0), "+v"(a1));
            sfor<4>([&](auto k) { lds_st16(xw + k.value * 1024, (f32x4){a1[4 * k.value], a1[4 * k.value + 1], a1[4 * k.value + 2], a1[4 * k.value + 3]}); });
            sfor<16>([&](auto i) { resid[i.value] += a0[i.value]; });
            pin_v(resid);
            BAR();
            f32x4 v[4];
            sfor<4>([&](auto k) { v[k.value] = lds_ld16(xr + k.value * 1024); });
            SB();
            sfor<4>([&](auto k) { sfor<4>([&](auto e) { resid[4 * k.value + e.value] += v[k.value][e.value]; }); });
            publish_x();
        }
        FRESH();
        hand_prep(2);
        {
            u32x4 xf[28]; u32x2 st[8];
            unsigned tb = sma<XT>(w, sma<4 * XT>(g, LB + L_X1)); pin_s(tb); pin_v(l16);
            HAND(0, PRE_IN);
            if (w) stage(LDS_R1, T_1, 21, 25, 1, 3);
            BAR();
            auto xload = [&](int f) { xf[f] = ldg(rsW, l16 + (f & 3) * 1024, tb + (f >> 2) * 4096); };
            sfor<4>([&](auto f) { xload(f.value); });
            SB();
            unsigned wA = LDS_R1 + l16, wC = wA + 2 * IMG, wH = LDS_HALF + (hh * 16 + (r & 15)) * 16, wG = LDS_AUG + r * 16;
            pin_v(wA); pin_v(wC); pin_v(wH); pin_v(wG);
            const unsigned ab = sma<7168>(j & 3, sma<AQ>(j >> 2, sma<4 * AQ>(w, sma<16 * AQ>(g, LB + L_AF))));
            SB();
            sfor<8>([&](auto f) { xload(4 + f.value); });
            SB();
            auto wfrag = [&](int img, int f) { return f == 28 ? lds_frag(wG + img * 512) : img == 3 ? lds_frag(wH + f * 512) : img == 2 ? lds_frag(wC + f * 1024) : lds_frag(wA + img * IMG + f * 1024); };
            f32x16 h0, h1, h2, h3, h4; u32x4 wf[3][3], wp[8], xa; XAug xg;
            sfor<2>([&](auto f) { sfor<3>([&](auto i) { wf[f.value][i.value] = wfrag(i.value, f.value); }); });
            sfor<28>([&](auto ff) {
                constexpr int f = ff.value;
                SB();
                if constexpr (f == 0) mfma0(h0, wf[0][0], xf[0]); else mfma_x(h0, wf[f % 3][0], xf[f], wf[f % 3][1], wf[f % 3][2]);
                SB();
                if constexpr (f + 2 <= 28) sfor<3>([&](auto i) { wf[(f + 2) % 3][i.value] = wfrag(i.value, f + 2); });
                if constexpr (f == 27) { BAR(); sfor<4>([&](auto i) { wp[i.value] = wfrag(3, i.value); }); }
                SB();
                if constexpr (f == 0) mfma0(h1, wf[0][1], xf[0]); else mfma(h1, wf[f % 3][1], xf[f]);
                SB();
                if constexpr (f < 16) xload(12 + f);
                if constexpr (f >= 18 && f < 27) xaug_step<f - 18>(xg, st, xa);
                SB();
                if constexpr (f == 0) { mfma0(h2, wf[0][2], xf[0]); } else mfma(h2, wf[f % 3][2], xf[f]);
                SB();
                if constexpr (f >= 1 && f < 9) st[f - 1] = __builtin_amdgcn_raw_buffer_load_b64(rsW, (8 * hh + f - 1) * 256 + r * 8, tb + 28672, 16);
            });
            SB();
            mfma_x(h0, wf[1][0], xa, wf[1][1], wf[1][2]); mfma(h1, wf[1][1], xa); mfma(h2, wf[1][2], xa);
            sfor<4>([&](auto i) { wp[4 + i.value] = wfrag(3, 4 + i.value); });
            SB();
            mfma0(h3, wp[0], xf[0]); mfma0(h4, wp[1], xf[1]); mfma(h3, wp[2], xf[2]); mfma(h4, wp[3], xf[3]);
            SB();
            const float c1 = xg.c1;
            u32x4 pk[4]; float mt[4], me[2], md[2];
            sfor<4>([&](auto i) { def_v(mt[i.value]); }); sfor<2>([&](auto i) { def_v(me[i.value]); def_v(md[i.value]); });
            auto mish_pk = [&](float a, float b2) {
                const f32x2 tt = (f32x2){a, b2} * c1, e = {__builtin_amdgcn_exp2f(tt[0]), __builtin_amdgcn_exp2f(tt[1])};
                const f32x2 d = __builtin_elementwise_fma(e, __builtin_elementwise_fma(e, (f32x2){-0.5f, -0.5f}, (f32x2){-1.f, -1.f}), (f32x2){-1.f, -1.f});
                const f32x2 y = __builtin_elementwise_fma(tt, (f32x2){__builtin_amdgcn_rcpf(d[0]), __builtin_amdgcn_rcpf(d[1])}, tt);
                return cvtpk(y[0], y[1]);
            };
            asm volatile("s_nop 3" : "+v"(h0));
            mt[0] = h0[0] * c1; pin_v(mt[0]);
            sfor<6>([&](auto pp) {
                constexpr int p = pp.value, f = 4 + 4 * p, cur = 4 * ((p + 1) & 1), nxt = 4 * (p & 1);
                sfor<4>([&](auto i) { constexpr int nf = f + 4 + i.value; if constexpr (nf <= 28) wp[nxt + i.value] = wfrag(3, nf); });
                SB();
                unsigned res;
                mfma4_mish<(p > 0)>(h3, h4, wp[cur], wp[cur + 1], wp[cur + 2], wp[cur + 3], xf[f], xf[f + 1], xf[f + 2], xf[f + 3], mt[(2 * p) & 3], mt[(2 * p + 1) & 3], mt[(2 * p + 2) & 3], mt[(2 * p + 3) & 3],
                                    me[0], me[1], md[0], md[1], h0[2 * p + 1], h0[2 * p + 2], c1, res);
                if constexpr (p > 0) { constexpr int pr = p - 1; if constexpr ((pr & 3) == 0) def_v(pk[pr >> 2]); pk[pr >> 2][pr & 3] = res; if constexpr ((pr & 3) == 3) stg(pk[pr >> 2], rsW, l16, ab + (pr >> 2) * 1024); }
                SB();
            });
            mfma(h3, wp[4], xa);
            sfor<3>([&](auto k) { pk[1][1 + k.value] = mish_pk(h0[10 + 2 * k.value], h0[11 + 2 * k.value]); });
            stg(pk[1], rsW, l16, ab + 1024);
            sfor<4>([&](auto s) { const f32x16& h = s.value < 2 ? h1 : h2; u32x4& q = pk[2 + (s.value & 1)];
                sfor<4>([&](auto k) { q[k.value] = mish_pk(h[8 * (s.value & 1) + 2 * k.value], h[8 * (s.value & 1) + 2 * k.value + 1]); }); stg(q, rsW, l16, ab + (2 + s.value) * 1024); });
            pin_v(h3); pin_v(h4);
            sfor<4>([&](auto k) { pk[0][k.value] = mish_pk(h3[2 * k.value] + h4[2 * k.value], h3[2 * k.value + 1] + h4[2 * k.value + 1]); });
            stg(pk[0], rsW, l16, ab + 6 * 1024);
        }
        FRESH();
        hand_prep(3);
        {
            u32x4 af[28][2];
            const unsigned ab0 = sma<8 * AQ>(m, sma<16 * AQ>(g, sma<AQ>(w, LB + L_AF))); unsigned ab = sma<4 * AQ>(t, ab0), ab2 = sma<4 * AQ>(t ^ 1, ab0);
            pin_s(ab); pin_s(ab2); pin_v(l16);
            HAND(0, PRE_IN);
            if (w && layer == 0) stage(LDS_R1, T_Q, 0, WQA, 1, 3);
            BAR();
            auto ldn = [&](auto nn) { constexpr int n = nn.value, s1 = n < F2D || (n < 56 - F2D && (n - F2D) % 2 == 1), k = n < F2D ? n : n < 56 - F2D ? (s1 ? F2D + (n - F2D) / 2 : (n - F2D) / 2) : n - 28;
                ald<(k & 3) * 1024>(af[k][s1], l16, rsW, (s1 ? ab2 : ab) + (k >> 2) * 4096); };
            auto posn = [](int s1, int k) { return s1 ? (k < F2D ? k : F2D + 2 * (k - F2D) + 1) : (k < 28 - F2D ? F2D + 2 * k : k + 28); };
            sfor<4>([&](auto n) { ldn(n); });
            SB();
            f32x16 y[4];
            px_prep(LB + L_X2);
            unsigned sw = LDS_SCR48 + w * 12288 + l16; pin_v(sw);
            SB();
            sfor<8>([&](auto n) { ldn(std::integral_constant<int, 4 + n.value>{}); });
            SB();
            sfor<56>([&](auto nn) {
                constexpr int n = nn.value, s1 = n < F2D || (n < 56 - F2D && (n - F2D) % 2 == 1), k = n < F2D ? n : n < 56 - F2D ? (s1 ? F2D + (n - F2D) / 2 : (n - F2D) / 2) : n - 28;
                static_assert(posn(s1, k) == n, "");
                await<(n + 12 < 56 ? 11 : 55 - n)>(af[k][s1]);
                SB();
                if constexpr (k == 0) mfma0_wa<4>(y[2 * s1 + 1], af[0][s1]); else mfma_wa<8 * k + 4>(y[2 * s1 + 1], af[k][s1]);
                SB();
                if constexpr (n + 12 < 56) ldn(std::integral_constant<int, n + 12>{});
                if constexpr (n > 56 - F2D) { constexpr int q = 2 * (n - (57 - F2D)); if constexpr (q == 0) asm_settle(y[2], y[3]); red_put1(sw, 2 - q / 4, q < 4 ? y[3] : y[2], q & 3); }
                SB();
                if constexpr (k == 0) mfma0_wa<0>(y[2 * s1], af[0][s1]); else mfma_wa<8 * k>(y[2 * s1], af[k][s1]);
                SB();
                if constexpr (n > 56 - F2D) { constexpr int q = 2 * (n - (57 - F2D)) + 1; red_put1(sw, 2 - q / 4, q < 4 ? y[3] : y[2], q & 3); }
            });
            SETTLE2(y[0], y[1]);
            f32x16 o = y[0];
            red_put(sw, 0, y[1]);
            red_get(o, false);
            sfor<16>([&](auto i) { resid[i.value] = __builtin_fmaf(o[i.value], 0.6931471805599453f, resid[i.value]); });
        }
        if (layer == 0) {
            publish_x();
            const int qw = min(WQA + w, 12);
            dma4(LDS_R1 + qw * 4096, l16, rsW, sma<T_STRIDE>(jq, WS_WT + T_Q) + qw * 4096);
            hand_prep(4);
            HAND(4, PRE_5);
            if (w) stage(LDS_R1, T_Q, 13, 21, 1, 3);
            BAR();
            FRESH();
            qkv(std::false_type{});
        } else {
            const u32x4 p0 = pack8(resid, 0, 1.f), p1 = pack8(resid, 1, 1.f);
            GAS char* op = (GAS char*)out + ((4 * g + 2 * m + t) * 32 + r) * 896 + 112 * n + 64 * u + 16 * hh;
            auto a = __builtin_amdgcn_permlane32_swap(p0[0], p0[2], false, false), c = __builtin_amdgcn_permlane32_swap(p0[1], p0[3], false, false);
            *(GAS u32x4*)op = (u32x4){a[0], c[0], a[1], c[1]};
            a = __builtin_amdgcn_permlane32_swap(p1[0], p1[2], false, false); c = __builtin_amdgcn_permlane32_swap(p1[1], p1[3], false, false);
            if (u == 0 || hh == 0) *(GAS u32x4*)(op + 32) = (u32x4){a[0], c[0], a[1], c[1]};
            if (b == 0 && w == 0) __builtin_amdgcn_raw_buffer_store_b64(tag, rsW, lane ? WS_BYTES : 0u, 0, 16);
            break;
        }
    }
}

extern "C" void kernel_launch(void* const* d_in, const int* in_sizes, int n_in, void* d_out, int out_size, void* d_ws, size_t ws_size, hipStream_t stream) {
    (void)hipFuncSetAttribute((const void*)mega, hipFuncAttributeMaxDynamicSharedMemorySize, LDS_BYTES);
    mega<<<256, 256, LDS_BYTES, stream>>>((const u16*)d_in[0], (const u16*)d_in[1], (const u16*)d_in[2], (const u16*)d_in[4], (const u16*)d_in[5], (const u16*)d_in[6],
                                          (u16*)d_out, (char*)d_ws);
}
```
